```python
import torch

def _softmax(x, axis=-1):
    return torch.softmax(x, dim=axis)
def _gelu(x):
    return torch.nn.functional.gelu(x, approximate="tanh")
def _silu(x):
    return torch.nn.functional.silu(x)
def _sigmoid(x):
    return torch.sigmoid(x)
def _layernorm(x, axis=-1, eps=1e-5):
    m = x.mean(dim=axis, keepdim=True)
    v = x.var(dim=axis, keepdim=True, unbiased=False)
    return (x - m) / torch.sqrt(v + eps)
def _scatter_add(tgt, idx, src, axis=0):
    return tgt.clone().index_add_(axis, idx, src)
def _segment_sum(src, seg, n):
    out = torch.zeros((n,) + src.shape[1:], dtype=src.dtype)
    return out.index_add_(0, seg, src)
def _pad(x, pw):
    flat = tuple(v for pair in reversed(pw) for v in pair)
    return torch.nn.functional.pad(x, flat)

kernel_name = 'interleave_layers__ssm_conv_s4__swiglu_ffn__prompt'

def setup_inputs():

    _w = (lambda a: (lambda t: t.to(torch.bfloat16) if t.is_floating_point() else t)(torch.as_tensor(a)))
    def _A_setup():
        B, L, D, N = 1, 2048, 512, 128
        A_re = -(lambda _x: torch.exp(torch.as_tensor(_x)))((lambda *s: torch.randn(*s, dtype=torch.bfloat16) * 1.6)(D, N))
        A_im = torch.pi * (lambda *s: torch.randn(*s, dtype=torch.bfloat16) * 0.2)(D, N)
        B_re = (lambda *s: torch.randn(*s, dtype=torch.bfloat16) * 0.2)(D, N)
        B_im = (lambda *s: torch.randn(*s, dtype=torch.bfloat16) * 0.2)(D, N)
        C_re = (lambda *s: torch.randn(*s, dtype=torch.bfloat16) * 0.2)(D, N)
        C_im = (lambda *s: torch.randn(*s, dtype=torch.bfloat16) * 0.2)(D, N)
        log_dt = (lambda *s: torch.randn(*s, dtype=torch.bfloat16) * 0.2)(D) * 0.5 - 4.0
        Dskip = (lambda *s: torch.randn(*s, dtype=torch.bfloat16) * 0.2)(D)
        return {
            "u": _w((lambda *s: torch.randn(*s, dtype=torch.bfloat16) * 0.05)(B, L, D)),
            "A_re": _w(A_re),
            "A_im": _w(A_im),
            "B_re": _w(B_re),
            "B_im": _w(B_im),
            "C_re": _w(C_re),
            "C_im": _w(C_im),
            "log_dt": _w(log_dt),
            "Dskip": _w(Dskip),
        }
    _A_in = _A_setup()

    def _B_setup():
        B, S, D, F = 1, 2048, 512, 1376
        return {
            "x": _w((lambda *s: torch.randn(*s, dtype=torch.bfloat16) * 0.05)(B, S, D)),
            "w_gate": _w((lambda *s: torch.randn(*s, dtype=torch.bfloat16) * 0.05)(D, F)),
            "w_up": _w((lambda *s: torch.randn(*s, dtype=torch.bfloat16) * 0.05)(D, F)),
            "w_down": _w((lambda *s: torch.randn(*s, dtype=torch.bfloat16) * 0.05)(F, D)),
        }
    _B_in = _B_setup()

    return {
        "A_u": _A_in["u"],
        "A_A_re": _A_in["A_re"],
        "A_A_im": _A_in["A_im"],
        "A_B_re": _A_in["B_re"],
        "A_B_im": _A_in["B_im"],
        "A_C_re": _A_in["C_re"],
        "A_C_im": _A_in["C_im"],
        "A_log_dt": _A_in["log_dt"],
        "A_Dskip": _A_in["Dskip"],
        "B_x": _B_in["x"],
        "B_w_gate": _B_in["w_gate"],
        "B_w_up": _B_in["w_up"],
        "B_w_down": _B_in["w_down"],
    }

def reference(A_u, A_A_re, A_A_im, A_B_re, A_B_im, A_C_re, A_C_im, A_log_dt, A_Dskip, B_x, B_w_gate, B_w_up, B_w_down):
    def _A_ref(u, A_re, A_im, B_re, B_im, C_re, C_im, log_dt, Dskip):
        Bsz, L, D = u.shape
        dt = torch.exp(log_dt)[:, None]
        A = A_re + 1j * A_im
        Bc = B_re + 1j * B_im
        Cc = C_re + 1j * C_im
        logdA = dt * A
        dB = dt * Bc
        l = (lambda x, dt: x.to(dt))(torch.arange(L), A_re.dtype)
        P = torch.exp(logdA[:, :, None] * l[None, None, :])
        K = torch.einsum("dn,dnl,dn->dl", Cc, P, dB).real
        L2 = 2 * L
        Kp = _pad(K, ((0, 0), (0, L)))
        up = (lambda x, *d: x.permute(*d))(u, 0, 2, 1)
        up = _pad(up, ((0, 0), (0, 0), (0, L)))
        Kf = (lambda x, axes=(-2,-1): torch.fft.rfft2(x.float(), dim=axes))(Kp[:, None, :], axes=(-2, -1))
        Uf = (lambda x, axes=(-2,-1): torch.fft.rfft2(x.float(), dim=axes))(up[:, :, None, :], axes=(-2, -1))
        Yf = Uf * Kf[None, :, :, :]
        y = (lambda x, s, axes=(-2,-1): torch.fft.irfft2(x, s=s, dim=axes).to(torch.bfloat16))(Yf, (1, L2), axes=(-2, -1))[:, :, 0, :L]
        y = (lambda x, *d: x.permute(*d))(y, 0, 2, 1) + u * Dskip[None, None, :]
        return y

    def _B_ref(x, w_gate, w_up, w_down):
        g = _silu(x @ w_gate)
        u = x @ w_up
        return (g * u) @ w_down

    x = A_u
    x = x + _A_ref(_layernorm(x, axis=-1), A_A_re, A_A_im, A_B_re, A_B_im, A_C_re, A_C_im, A_log_dt, A_Dskip)
    x = x + _B_ref(_layernorm(x, axis=-1), B_w_gate, B_w_up, B_w_down)
    x = x + _A_ref(_layernorm(x, axis=-1), A_A_re, A_A_im, A_B_re, A_B_im, A_C_re, A_C_im, A_log_dt, A_Dskip)
    x = x + _B_ref(_layernorm(x, axis=-1), B_w_gate, B_w_up, B_w_down)
    return x
```

```cpp
#include <hip/hip_runtime.h>
#include <hip/hip_bf16.h>
#define ROUND_L 0
typedef unsigned short u16;
__device__ __forceinline__ float b2f(u16 v){ return __uint_as_float(((unsigned)v)<<16); }
__device__ __forceinline__ u16 f2b(float f){ __hip_bfloat16 h = __float2bfloat16(f); return *reinterpret_cast<u16*>(&h); }
__device__ __forceinline__ float rb(float f){ return b2f(f2b(f)); }
constexpr int L=2048, D=512, N=128, F=1376;

__global__ void kgen(const u16* Are,const u16* Aim,const u16* Bre,const u16* Bim,const u16* Cre,const u16* Cim,const u16* logdt,float* K){
  int d=blockIdx.y; int l=blockIdx.x*256+threadIdx.x;
  float dt=rb(expf(b2f(logdt[d])));
  float lf = ROUND_L? rb((float)l) : (float)l;
  float acc=0;
  for(int n=0;n<N;n++){
    int i=d*N+n;
    float ar=dt*b2f(Are[i]), ai=dt*b2f(Aim[i]);
    float br=dt*b2f(Bre[i]), bi=dt*b2f(Bim[i]);
    float cr=b2f(Cre[i]), ci=b2f(Cim[i]);
    float e=expf(ar*lf); float s,c; sincosf(ai*lf,&s,&c);
    float pr=e*c, pi=e*s;
    float qr=cr*pr-ci*pi, qi=cr*pi+ci*pr;
    acc+=qr*br-qi*bi;
  }
  K[d*L+l]=acc;
}
__global__ void ln(const u16* x,u16* u){
  int t=blockIdx.x; __shared__ float red[256];
  int tid=threadIdx.x;
  float a=b2f(x[t*D+tid]), b=b2f(x[t*D+tid+256]);
  red[tid]=a+b; __syncthreads();
  for(int s=128;s>0;s>>=1){ if(tid<s) red[tid]+=red[tid+s]; __syncthreads(); }
  float mean=red[0]/D; __syncthreads();
  red[tid]=(a-mean)*(a-mean)+(b-mean)*(b-mean); __syncthreads();
  for(int s=128;s>0;s>>=1){ if(tid<s) red[tid]+=red[tid+s]; __syncthreads(); }
  float var=red[0]/D;
  float m=rb(mean), v=rb(var);
  float sd=rb(sqrtf(rb(v+1e-5f)));
  u[t*D+tid]=f2b(rb(a-m)/sd);
  u[t*D+tid+256]=f2b(rb(b-m)/sd);
}
__global__ void conv(const u16* x,const u16* u,const float* K,const u16* Dsk,u16* x1){
  int t=blockIdx.x; int d=blockIdx.y*256+threadIdx.x;
  float acc=0;
  for(int j=0;j<=t;j++) acc+=K[d*L+t-j]*b2f(u[j*D+d]);
  float y=rb(acc);
  float s=rb(b2f(u[t*D+d])*b2f(Dsk[d]));
  float y2=rb(y+s);
  x1[t*D+d]=f2b(b2f(x[t*D+d])+y2);
}
__global__ void g1(const u16* z,const u16* wg,const u16* wu,u16* h){
  int t=blockIdx.x; int f=blockIdx.y*128+threadIdx.x; if(f>=F) return;
  float g=0,u=0;
  for(int k=0;k<D;k++){ float a=b2f(z[t*D+k]); g+=a*b2f(wg[k*F+f]); u+=a*b2f(wu[k*F+f]); }
  g=rb(g); u=rb(u);
  float s=rb(g/(1.f+expf(-g)));
  h[t*F+f]=f2b(s*u);
}
__global__ void g2(const u16* h,const u16* wd,const u16* x1,u16* out){
  int t=blockIdx.x; int d=blockIdx.y*256+threadIdx.x;
  float o=0;
  for(int k=0;k<F;k++) o+=b2f(h[t*F+k])*b2f(wd[k*D+d]);
  out[t*D+d]=f2b(b2f(x1[t*D+d])+rb(o));
}
extern "C" void kernel_launch(void* const* d_in, const int* in_sizes, int n_in,
                              void* d_out, int out_size, void* d_ws, size_t ws_size,
                              hipStream_t stream) {
  const u16* x0=(const u16*)d_in[0];
  char* ws=(char*)d_ws;
  float* K=(float*)ws; ws+=D*L*4;
  u16* u=(u16*)ws; ws+=L*D*2;
  u16* x1=(u16*)ws; ws+=L*D*2;
  u16* x2=(u16*)ws; ws+=L*D*2;
  u16* h=(u16*)ws; ws+=L*F*2;
  kgen<<<dim3(L/256,D),256,0,stream>>>((u16*)d_in[1],(u16*)d_in[2],(u16*)d_in[3],(u16*)d_in[4],(u16*)d_in[5],(u16*)d_in[6],(u16*)d_in[7],K);
  const u16* x=x0;
  for(int layer=0;layer<2;layer++){
    ln<<<L,256,0,stream>>>(x,u);
    conv<<<dim3(L,2),256,0,stream>>>(x,u,K,(u16*)d_in[8],x1);
    ln<<<L,256,0,stream>>>(x1,u);
    g1<<<dim3(L,11),128,0,stream>>>(u,(u16*)d_in[10],(u16*)d_in[11],h);
    u16* o = layer==0? x2 : (u16*)d_out;
    g2<<<dim3(L,2),256,0,stream>>>(h,(u16*)d_in[12],x1,o);
    x=x2;
  }
}
```

```cpp
#include <hip/hip_runtime.h>
#include <stdint.h>

typedef unsigned short u16;
typedef unsigned int u32;
typedef u32 u32x4 __attribute__((ext_vector_type(4)));
typedef u32 u32x2 __attribute__((ext_vector_type(2)));
typedef float f32x4 __attribute__((ext_vector_type(4)));
typedef float f32x2 __attribute__((ext_vector_type(2)));
typedef short s16x4 __attribute__((ext_vector_type(4)));
typedef __bf16 bf16x8 __attribute__((ext_vector_type(8)));
typedef __bf16 bf16x2 __attribute__((ext_vector_type(2)));
#define LAS __attribute__((address_space(3)))
#define DEV __device__ __forceinline__

constexpr int L = 2048, D = 512, NS = 128, F = 1376;
constexpr int NT1 = 172;
constexpr int KS1 = 16;
constexpr int KS2 = 43;
constexpr float EPS = 1e-5f;

constexpr size_t OFF_K    = 0;
constexpr size_t OFF_XF   = OFF_K + (size_t)D * L * 2;
constexpr size_t OFF_X1   = OFF_XF + (size_t)D * L * 2;
constexpr size_t OFF_H    = OFF_X1 + (size_t)D * L * 2;
constexpr size_t OFF_WGU  = OFF_H + (size_t)F * L * 2;
constexpr size_t OFF_WD   = OFF_WGU + (size_t)NT1 * KS1 * 1024;
constexpr size_t OFF_CS   = OFF_WD + (size_t)32 * KS2 * 1024;
constexpr size_t OFF_ST   = OFF_CS + (size_t)2 * NT1 * 16 * 4;
constexpr size_t WS_END   = OFF_ST + (size_t)8 * L * 8;

DEV float lo_f(u32 v) { return __uint_as_float(v << 16); }
DEV float hi_f(u32 v) { return __uint_as_float(v & 0xffff0000u); }
DEV u32 pk_bf16(float a, float b) {
  f32x2 v = {a, b};
  return __builtin_bit_cast(u32, __builtin_convertvector(v, bf16x2));
}
DEV float rbf(float a) { return lo_f(pk_bf16(a, a)); }
DEV f32x4 mfma16(u32x4 a, u32x4 b, f32x4 c) {
  return __builtin_amdgcn_mfma_f32_16x16x32_bf16(__builtin_bit_cast(bf16x8, a), __builtin_bit_cast(bf16x8, b), c, 0, 0, 0);
}
DEV void ln_scalars(float sum, float sq, float& m, float& rs) {
  float mean = sum * (1.0f / D);
  float var = fmaxf(sq * (1.0f / D) - mean * mean, 0.0f);
  m = rbf(mean);
  float s = rbf(__builtin_sqrtf(rbf(rbf(var) + EPS)));
  rs = 1.0f / s;
}

constexpr int NB_RPK_GU = 88, NB_RPK_D = 43, NB_LN = 64, NB_KG = 512;
constexpr int NB_K0 = NB_RPK_GU + NB_RPK_D + NB_LN + NB_KG;

DEV void transpose8x8(const u32x4 (&r)[8], u32x4 (&o)[8]) {
#pragma unroll
  for (int w = 0; w < 4; ++w) {
#pragma unroll
    for (int p = 0; p < 4; ++p) {
      o[2 * w][p]     = __builtin_amdgcn_perm(r[2 * p + 1][w], r[2 * p][w], 0x05040100u);
      o[2 * w + 1][p] = __builtin_amdgcn_perm(r[2 * p + 1][w], r[2 * p][w], 0x07060302u);
    }
  }
}

DEV void cexp_l(float ar, float ai, float l, float& er, float& ei) {
  const float LOG2E = 1.4426950408889634f;
  const float I2PI_HI = 0.15915494309189535f;
  const float I2PI_LO = (float)(0.15915494309189535 - (double)0.15915494309189535f);
  float e = __builtin_amdgcn_exp2f((ar * l) * LOG2E);
  float p = ai * l;
  float rev = p * I2PI_HI;
  float rl = __builtin_fmaf(p, I2PI_HI, -rev);
  rl = __builtin_fmaf(p, I2PI_LO, rl);
  float fr = __builtin_amdgcn_fractf(rev) + rl;
  er = e * __builtin_amdgcn_cosf(fr);
  ei = e * __builtin_amdgcn_sinf(fr);
}

__global__ __launch_bounds__(256) void k0_kernel(
    const u16* __restrict__ x0, const u16* __restrict__ Are, const u16* __restrict__ Aim,
    const u16* __restrict__ Bre, const u16* __restrict__ Bim, const u16* __restrict__ Cre,
    const u16* __restrict__ Cim, const u16* __restrict__ logdt, const u16* __restrict__ wg,
    const u16* __restrict__ wu, const u16* __restrict__ wd, char* __restrict__ ws) {
  __shared__ __attribute__((aligned(16))) float sm[4 * 768 + 768];
  const int tid = threadIdx.x, w = tid >> 6, lane = tid & 63;
  int b = blockIdx.x;
  if (b < NB_RPK_GU) {
    const int mtx = b & 1, half = (b >> 1) & 1, jg = b >> 2;
    const u16* W = mtx ? wu : wg;
    const int j = jg * 8 + (lane & 7);
    const int kb = half * 32 + w * 8 + (lane >> 3);
    const bool ok = j < NT1;
    const int jc = ok ? j : NT1 - 1;
    u32x4 r[8], o[8];
#pragma unroll
    for (int i = 0; i < 8; ++i) r[i] = *(const u32x4*)(W + (size_t)(kb * 8 + i) * F + jc * 8);
    transpose8x8(r, o);
    u32x4* dst = (u32x4*)(ws + OFF_WGU) + ((size_t)jc * KS1 + (kb >> 2)) * 64 + (kb & 3) * 16 + mtx * 2;
    float cs[8];
#pragma unroll
    for (int c = 0; c < 8; ++c) {
      if (ok) dst[(c >> 1) * 4 + (c & 1)] = o[c];
      float s = 0.f;
#pragma unroll
      for (int p = 0; p < 4; ++p) s += lo_f(o[c][p]) + hi_f(o[c][p]);
      s += __shfl_xor(s, 8); s += __shfl_xor(s, 16); s += __shfl_xor(s, 32);
      cs[c] = s;
    }
    if (lane < 8) {
#pragma unroll
      for (int c = 0; c < 8; ++c) sm[w * 64 + lane * 8 + c] = cs[c];
    }
    __syncthreads();
    if (tid < 64) {
      float s = sm[tid] + sm[64 + tid] + sm[128 + tid] + sm[192 + tid];
      int jj = jg * 8 + (tid >> 3), c = tid & 7;
      if (jj < NT1) ((float*)(ws + OFF_CS))[half * (NT1 * 16) + jj * 16 + (c >> 1) * 4 + (c & 1) + mtx * 2] = s;
    }
    return;
  }
  b -= NB_RPK_GU;
  if (b < NB_RPK_D) {
    const int fb = b * 4 + w, db = lane;
    u32x4 r[8], o[8];
#pragma unroll
    for (int i = 0; i < 8; ++i) r[i] = *(const u32x4*)(wd + (size_t)(fb * 8 + i) * D + db * 8);
    transpose8x8(r, o);
    u32x4* dst = (u32x4*)(ws + OFF_WD) + ((size_t)(db >> 1) * KS2 + (fb >> 2)) * 64 + (fb & 3) * 16 + (db & 1) * 8;
#pragma unroll
    for (int c = 0; c < 8; ++c) dst[c] = o[c];
    return;
  }
  b -= NB_RPK_D;
  if (b < NB_LN) {
    const int t = (b * 4 + w) * 8 + (lane & 7), c = lane >> 3;
    u32x4 v[8];
#pragma unroll
    for (int i = 0; i < 8; ++i) v[i] = *(const u32x4*)(x0 + (size_t)t * D + (i * 8 + c) * 8);
    float s = 0.f, q = 0.f;
    u32x4* dst = (u32x4*)(ws + OFF_XF) + (size_t)(t >> 4) * 64 * 16 + (t & 15);
#pragma unroll
    for (int i = 0; i < 8; ++i) {
      dst[(i * 8 + c) * 16] = v[i];
#pragma unroll
      for (int p = 0; p < 4; ++p) {
        float a = lo_f(v[i][p]), bb = hi_f(v[i][p]);
        s += a + bb; q = __builtin_fmaf(a, a, q); q = __builtin_fmaf(bb, bb, q);
      }
    }
    s += __shfl_xor(s, 8); s += __shfl_xor(s, 16); s += __shfl_xor(s, 32);
    q += __shfl_xor(q, 8); q += __shfl_xor(q, 16); q += __shfl_xor(q, 32);
    if (lane < 8) { f32x2 o = {s, q}; ((f32x2*)(ws + OFF_ST))[t] = o; }
    return;
  }
  b -= NB_LN;
  {
    const int d = b;
    const int r = lane & 15, kq = lane >> 4;
    const int n0 = d * NS + w * 32 + kq * 8;
    u32x4 var = *(const u32x4*)(Are + n0), vai = *(const u32x4*)(Aim + n0);
    u32x4 vbr = *(const u32x4*)(Bre + n0), vbi = *(const u32x4*)(Bim + n0);
    u32x4 vcr = *(const u32x4*)(Cre + n0), vci = *(const u32x4*)(Cim + n0);
    const float dt = rbf(expf(lo_f((u32)logdt[d])));
    const float h0 = (float)((r >> 3) * 128 + (r & 7));
    const float h1 = (float)(r < 8 ? 256 + (r >> 2) * 128 + (r & 3) * 2 : 512 + ((r - 8) >> 1) * 128 + (r & 1) * 4);
    const float h2 = (float)(r <= 8 ? 1024 + r * 128 : 0);
    const float lo = (float)(8 * r);
    f32x4 acc0 = {0, 0, 0, 0}, acc1 = {0, 0, 0, 0}, acc2 = {0, 0, 0, 0};
#pragma unroll
    for (int s = 0; s < 8; ++s) {
      auto get = [&](u32x4 v) { return (s & 1) ? hi_f(v[s >> 1]) : lo_f(v[s >> 1]); };
      float ar = dt * get(var), ai = dt * get(vai), br = dt * get(vbr), bi = dt * get(vbi);
      float cr = get(vcr), ci = get(vci);
      float kr = cr * br - ci * bi, ki = cr * bi + ci * br;
      float er, ei;
      cexp_l(ar, ai, lo, er, ei);
      float lr = kr * er - ki * ei, nli = -(kr * ei + ki * er);
      cexp_l(ar, ai, h0, er, ei);
      acc0 = __builtin_amdgcn_mfma_f32_16x16x4f32(er, lr, acc0, 0, 0, 0);
      acc0 = __builtin_amdgcn_mfma_f32_16x16x4f32(ei, nli, acc0, 0, 0, 0);
      cexp_l(ar, ai, h1, er, ei);
      acc1 = __builtin_amdgcn_mfma_f32_16x16x4f32(er, lr, acc1, 0, 0, 0);
      acc1 = __builtin_amdgcn_mfma_f32_16x16x4f32(ei, nli, acc1, 0, 0, 0);
      cexp_l(ar, ai, h2, er, ei);
      acc2 = __builtin_amdgcn_mfma_f32_16x16x4f32(er, lr, acc2, 0, 0, 0);
      acc2 = __builtin_amdgcn_mfma_f32_16x16x4f32(ei, nli, acc2, 0, 0, 0);
    }
#pragma unroll
    for (int i = 0; i < 4; ++i) {
      sm[w * 768 + (kq * 4 + i) * 16 + r] = acc0[i];
      sm[w * 768 + (16 + kq * 4 + i) * 16 + r] = acc1[i];
      sm[w * 768 + (32 + kq * 4 + i) * 16 + r] = acc2[i];
    }
    __syncthreads();
#pragma unroll
    for (int i = 0; i < 3; ++i) {
      int e = tid + i * 256;
      sm[3072 + e] = (sm[e] + sm[768 + e]) + (sm[1536 + e] + sm[2304 + e]);
    }
    __syncthreads();
    float kv[8];
#pragma unroll
    for (int i = 0; i < 8; ++i) {
      int l = tid * 8 + i;
      int lp = (int)rbf((float)l);
      int row;
      if (lp < 256) row = (lp >> 7) * 8 + (lp & 7);
      else if (lp < 512) row = 16 + ((lp - 256) >> 7) * 4 + ((lp & 7) >> 1);
      else if (lp < 1024) row = 24 + ((lp - 512) >> 7) * 2 + ((lp & 7) >> 2);
      else row = 32 + ((lp - 1024) >> 7);
      kv[i] = sm[3072 + row * 16 + ((lp >> 3) & 15)];
    }
    u32x4 o = {pk_bf16(kv[0], kv[1]), pk_bf16(kv[2], kv[3]), pk_bf16(kv[4], kv[5]), pk_bf16(kv[6], kv[7])};
    *(u32x4*)(ws + OFF_K + ((size_t)d * L + tid * 8) * 2) = o;
  }
}

constexpr int CV_KCOPY = 4160;
constexpr int CV_KCH = 4 * CV_KCOPY;
constexpr int CV_OFF_U = 2 * CV_KCH;
constexpr int CV_UCH = (256 + L) * 2;
constexpr int CV_OFF_Y = CV_OFF_U + 2 * CV_UCH;
constexpr int CV_YW = L * 5;
constexpr int CV_LDS = CV_OFF_Y + 8 * CV_YW;

template <int P>
__global__ __launch_bounds__(512) void conv_kernel(const u16* __restrict__ dskip, char* __restrict__ ws) {
  extern __shared__ __attribute__((aligned(16))) char smem[];
  const int tid = threadIdx.x, w = tid >> 6, lane = tid & 63;
  const int b = blockIdx.x;
  const int db = (b & 7) * 8 + (b >> 5), pr = (b >> 3) & 3;
  const int d0 = db * 8 + pr * 2;

  const int kch = tid >> 8, kp = (tid & 255) * 8;
  const u16* Kg = (const u16*)(ws + OFF_K) + (size_t)(d0 + kch) * L;
  u32x4 kc = *(const u32x4*)(Kg + kp);
  u32x2 kprev = {0u, 0u};
  if (kp > 0) kprev = *(const u32x2*)(Kg + kp - 4);
  u32 xv[4];
  const u32* xf = (const u32*)(ws + OFF_XF);
#pragma unroll
  for (int j = 0; j < 4; ++j) {
    int t = tid + 512 * j;
    xv[j] = xf[(((size_t)(t >> 4) * 64 + db) * 16 + (t & 15)) * 4 + pr];
  }
  float ssum[4], ssq[4];
  const f32x2* st = (const f32x2*)(ws + OFF_ST);
#pragma unroll
  for (int j = 0; j < 4; ++j) {
    f32x2 a = st[tid + 512 * j];
#pragma unroll
    for (int p = 1; p < P; ++p) a += st[p * L + tid + 512 * j];
    ssum[j] = a[0]; ssq[j] = a[1];
  }
  const u32 dsk = *(const u32*)(dskip + d0);

  {
    char* kb = smem + kch * CV_KCH + (kp + 16) * 2;
    u32 e[6] = {kprev[0], kprev[1], kc[0], kc[1], kc[2], kc[3]};
    u32x4 c0 = kc;
    u32x4 c2 = {e[1], e[2], e[3], e[4]};
    u32x4 c1, c3;
#pragma unroll
    for (int i = 0; i < 4; ++i) {
      c1[i] = __builtin_amdgcn_alignbit(e[i + 2], e[i + 1], 16);
      c3[i] = __builtin_amdgcn_alignbit(e[i + 1], e[i], 16);
    }
    *(u32x4*)(kb) = c0;
    *(u32x4*)(kb + CV_KCOPY) = c1;
    *(u32x4*)(kb + 2 * CV_KCOPY) = c2;
    *(u32x4*)(kb + 3 * CV_KCOPY) = c3;
    u32x4 z = {0, 0, 0, 0};
    if (tid < 16) *(u32x4*)(smem + (tid >> 1) * CV_KCOPY + (tid & 1) * 16) = z;
    if (tid >= 64 && tid < 128) { int i = tid - 64; *(u32x4*)(smem + CV_OFF_U + (i >> 5) * CV_UCH + (i & 31) * 16) = z; }
  }
  u32 ud[4];
#pragma unroll
  for (int j = 0; j < 4; ++j) {
    float m, rs;
    ln_scalars(ssum[j], ssq[j], m, rs);
    u32 xm = pk_bf16(lo_f(xv[j]) - m, hi_f(xv[j]) - m);
    u32 u = pk_bf16(lo_f(xm) * rs, hi_f(xm) * rs);
    int t = tid + 512 * j;
    *(u16*)(smem + CV_OFF_U + (256 + t) * 2) = (u16)(u & 0xffff);
    *(u16*)(smem + CV_OFF_U + CV_UCH + (256 + t) * 2) = (u16)(u >> 16);
    ud[j] = pk_bf16(lo_f(u) * lo_f(dsk), hi_f(u) * hi_f(dsk));
  }
  __syncthreads();

  const int ch = w >> 2, wr = w & 3;
  const int c16 = lane & 15, kq = lane >> 4;
  f32x4 acc[8];
#pragma unroll
  for (int m = 0; m < 8; ++m) acc[m] = (f32x4){0, 0, 0, 0};
#pragma unroll
  for (int rr = 0; rr < 2; ++rr) {
    const int rho = 1 + 2 * (wr * 2 + rr);
    const char* tb = smem + ch * CV_KCH + (c16 >> 2) * CV_KCOPY + (16 + 16 * rho - 8 * kq + 4 * (c16 & 3) - 4) * 2;
    const char* ub = smem + CV_OFF_U + ch * CV_UCH + (256 - 16 * rho + 16 * c16 + 8 * kq) * 2;
    u32x4 T[8], U[8];
#pragma unroll
    for (int q = 0; q < 8; ++q) {
      u32x2 a = __builtin_bit_cast(u32x2, __builtin_amdgcn_ds_read_tr16_b64_v4i16((LAS s16x4*)(tb + q * 512 + 8)));
      u32x2 c = __builtin_bit_cast(u32x2, __builtin_amdgcn_ds_read_tr16_b64_v4i16((LAS s16x4*)(tb + q * 512)));
      T[q] = (u32x4){a[0], a[1], c[0], c[1]};
      U[q] = *(const u32x4*)(ub + q * 512);
    }
#pragma unroll
    for (int m = 0; m < 8; ++m)
#pragma unroll
      for (int q = 0; q <= m; ++q) acc[m] = mfma16(T[q], U[m - q], acc[m]);
  }
  {
    char* yb = smem + CV_OFF_Y + w * CV_YW + c16 * 80 + kq * 16;
#pragma unroll
    for (int m = 0; m < 8; ++m) *(f32x4*)(yb + m * 1280) = acc[m];
  }
  __syncthreads();
  u32* x1 = (u32*)(ws + OFF_X1);
#pragma unroll
  for (int j = 0; j < 4; ++j) {
    int t = tid + 512 * j;
    const char* yb = smem + CV_OFF_Y + t * 4 + (t >> 4) * 16;
    float y0 = (*(const float*)(yb) + *(const float*)(yb + CV_YW)) + (*(const float*)(yb + 2 * CV_YW) + *(const float*)(yb + 3 * CV_YW));
    float y1 = (*(const float*)(yb + 4 * CV_YW) + *(const float*)(yb + 5 * CV_YW)) + (*(const float*)(yb + 6 * CV_YW) + *(const float*)(yb + 7 * CV_YW));
    u32 y = pk_bf16(y0, y1);
    u32 y2 = pk_bf16(lo_f(y) + lo_f(ud[j]), hi_f(y) + hi_f(ud[j]));
    u32 o = pk_bf16(lo_f(xv[j]) + lo_f(y2), hi_f(xv[j]) + hi_f(y2));
    x1[(((size_t)(t >> 4) * 64 + db) * 16 + (t & 15)) * 4 + pr] = o;
  }
}

constexpr int G1_LDS = 128 * 1024 + 1024;

__global__ __launch_bounds__(256, 1) void g1_kernel(char* __restrict__ ws) {
  extern __shared__ __attribute__((aligned(16))) char smem[];
  const int tid = threadIdx.x, w = tid >> 6, lane = tid & 63;
  const int b = blockIdx.x;
  const int xcd = b & 7, idx = b >> 3;
  const int rt = (xcd & 1) * 8 + (idx & 7), cb = (xcd >> 1) * 4 + (idx >> 3);
  const int start = cb < 12 ? cb * 11 : 132 + (cb - 12) * 10;
  const int cnt = cb < 12 ? 11 : 10;
  int nt[3]; bool nok[3];
  {
    int base = (w < 3) ? w * 3 : cnt - 2;
    int lim = (w < 3) ? cnt - 2 : cnt;
#pragma unroll
    for (int i = 0; i < 3; ++i) { nok[i] = base + i < lim; nt[i] = start + (nok[i] ? base + i : base); }
  }
  const u32x4* xg = (const u32x4*)(ws + OFF_X1) + (size_t)rt * 8 * 1024;
  const u32x4* wgp = (const u32x4*)(ws + OFF_WGU);
  u32x4 xs[8][4];
  u32x4 wf[16][3];
  auto ldx = [&](int c) {
#pragma unroll
    for (int i = 0; i < 4; ++i) {
      int u = i * 256 + tid;
      int ks = c * 2 + (u >> 9), tt = (u >> 6) & 7;
      xs[c][i] = xg[(size_t)tt * 1024 + ks * 64 + lane];
    }
  };
  auto ldw = [&](int ks) {
#pragma unroll
    for (int i = 0; i < 3; ++i) wf[ks][i] = wgp[((size_t)nt[i] * KS1 + ks) * 64 + lane];
  };
  auto stx = [&](int c) {
#pragma unroll
    for (int i = 0; i < 4; ++i) *(u32x4*)(smem + c * 16384 + (i * 256 + tid) * 16) = xs[c][i];
  };
#pragma unroll
  for (int c = 0; c < 6; ++c) { ldx(c); ldw(2 * c); ldw(2 * c + 1); }
  __builtin_amdgcn_sched_barrier(0);

  f32x4 acc[3][8];
#pragma unroll
  for (int i = 0; i < 3; ++i)
#pragma unroll
    for (int t = 0; t < 8; ++t) acc[i][t] = (f32x4){0, 0, 0, 0};
  f32x4 sacc[2], qacc[2];
  sacc[0] = sacc[1] = qacc[0] = qacc[1] = (f32x4){0, 0, 0, 0};
  const u32x4 ones = {0x3f803f80u, 0x3f803f80u, 0x3f803f80u, 0x3f803f80u};

  stx(0);
  __syncthreads();
#pragma unroll
  for (int c = 0; c < 8; ++c) {
    if (c + 1 < 8) stx(c + 1);
    if (c + 6 < 8) { ldx(c + 6); ldw(2 * c + 12); ldw(2 * c + 13); }
    __builtin_amdgcn_sched_barrier(0);
#pragma unroll
    for (int kk = 0; kk < 2; ++kk) {
      const int ks = c * 2 + kk;
      const char* bb = smem + ks * 8192 + lane * 16;
#pragma unroll
      for (int t = 0; t < 8; ++t) {
        u32x4 xb = *(const u32x4*)(bb + t * 1024);
#pragma unroll
        for (int i = 0; i < 3; ++i) acc[i][t] = mfma16(wf[ks][i], xb, acc[i][t]);
      }
#pragma unroll
      for (int s = 0; s < 2; ++s) {
        u32x4 xb = *(const u32x4*)(bb + (w * 2 + s) * 1024);
        sacc[s] = mfma16(ones, xb, sacc[s]);
        qacc[s] = mfma16(xb, xb, qacc[s]);
      }
    }
    __builtin_amdgcn_sched_barrier(0);
    __syncthreads();
  }
  f32x2* sst = (f32x2*)(smem + 128 * 1024);
  const int c16 = lane & 15, kq = lane >> 4;
  if ((c16 >> 2) == kq) {
#pragma unroll
    for (int s = 0; s < 2; ++s) {
      int r = c16 & 3;
      float q = r == 0 ? qacc[s][0] : r == 1 ? qacc[s][1] : r == 2 ? qacc[s][2] : qacc[s][3];
      float m, rs;
      ln_scalars(sacc[s][0], q, m, rs);
      f32x2 o = {m, rs};
      sst[(w * 2 + s) * 16 + c16] = o;
    }
  }
  f32x4 cs[3];
  const f32x4* csg = (const f32x4*)(ws + OFF_CS);
#pragma unroll
  for (int i = 0; i < 3; ++i) cs[i] = csg[nt[i] * 4 + kq] + csg[NT1 * 4 + nt[i] * 4 + kq];
  __syncthreads();
  u32* hg = (u32*)(ws + OFF_H);
#pragma unroll
  for (int t = 0; t < 8; ++t) {
    f32x2 ms = sst[t * 16 + c16];
    const float m = ms[0], rs = ms[1];
#pragma unroll
    for (int i = 0; i < 3; ++i) {
      f32x4 v = (acc[i][t] - m * cs[i]) * rs;
      float h0 = v[0] * v[2] * __builtin_amdgcn_rcpf(1.0f + __builtin_amdgcn_exp2f(v[0] * -1.4426950408889634f));
      float h1 = v[1] * v[3] * __builtin_amdgcn_rcpf(1.0f + __builtin_amdgcn_exp2f(v[1] * -1.4426950408889634f));
      if (nok[i]) hg[(((size_t)(rt * 8 + t) * NT1 + nt[i]) * 16 + c16) * 4 + kq] = pk_bf16(h0, h1);
    }
  }
}

constexpr int G2_ROW = 272;
constexpr int G2_WT = 64 * G2_ROW;
constexpr int G2_LDS = 4 * G2_WT;

template <bool LAST>
__global__ __launch_bounds__(256, 1) void g2_kernel(char* __restrict__ ws, u16* __restrict__ out) {
  extern __shared__ __attribute__((aligned(16))) char smem[];
  const int tid = threadIdx.x, w = tid >> 6, lane = tid & 63;
  const int b = blockIdx.x;
  const int xcd = b & 7, idx = b >> 3;
  const int rt = xcd * 4 + (idx & 3), ct = idx >> 2;
  const int ks0 = w * 11;
  const u32x4* hg = (const u32x4*)(ws + OFF_H) + (size_t)rt * 4 * (NT1 * 16) + ks0 * 64 + lane;
  const u32x4* wdp = (const u32x4*)(ws + OFF_WD) + ((size_t)ct * 4 * KS2 + ks0) * 64 + lane;
  const int nks = __builtin_amdgcn_readfirstlane(w) == 3 ? 10 : 11;
  const int et = rt * 64 + (tid >> 2), edb = ct * 8 + (tid & 3) * 2;
  const u32x4* x1 = (const u32x4*)(ws + OFF_X1) + ((size_t)(et >> 4) * 64 + edb) * 16 + (et & 15);
  u32x4 res0 = x1[0], res1 = x1[16];

  u32x4 hf[11][4], wf[11][4];
  auto ld = [&](int s) {
    int sc = (s == 10 && nks == 10) ? 9 : s;
#pragma unroll
    for (int i = 0; i < 4; ++i) {
      hf[s][i] = hg[(size_t)i * (NT1 * 16) + sc * 64];
      wf[s][i] = wdp[(size_t)i * KS2 * 64 + sc * 64];
    }
  };
#pragma unroll
  for (int s = 0; s < 7; ++s) ld(s);
  __builtin_amdgcn_sched_barrier(0);
  f32x4 acc[4][4];
#pragma unroll
  for (int i = 0; i < 4; ++i)
#pragma unroll
    for (int j = 0; j < 4; ++j) acc[i][j] = (f32x4){0, 0, 0, 0};
#pragma unroll
  for (int s = 0; s < 11; ++s) {
    if (s < 10 || nks == 11) {
#pragma unroll
      for (int i = 0; i < 4; ++i)
#pragma unroll
        for (int j = 0; j < 4; ++j) acc[i][j] = mfma16(wf[s][i], hf[s][j], acc[i][j]);
    }
    if (s + 7 < 11) ld(s + 7);
    __builtin_amdgcn_sched_barrier(0);
  }
  const int c16 = lane & 15, kq = lane >> 4;
  {
    char* rb = smem + w * G2_WT + c16 * G2_ROW + kq * 16;
#pragma unroll
    for (int i = 0; i < 4; ++i)
#pragma unroll
      for (int j = 0; j < 4; ++j) *(f32x4*)(rb + j * 16 * G2_ROW + i * 64) = acc[i][j];
  }
  __syncthreads();
  {
    const char* rb = smem + (tid >> 2) * G2_ROW + (tid & 3) * 64;
    u32 o[8];
    float s = 0.f, q = 0.f;
#pragma unroll
    for (int i = 0; i < 4; ++i) {
      f32x4 v = (*(const f32x4*)(rb + i * 16) + *(const f32x4*)(rb + G2_WT + i * 16)) +
                (*(const f32x4*)(rb + 2 * G2_WT + i * 16) + *(const f32x4*)(rb + 3 * G2_WT + i * 16));
#pragma unroll
      for (int p = 0; p < 2; ++p) {
        u32 r = (i < 2) ? res0[i * 2 + p] : res1[(i - 2) * 2 + p];
        u32 y = pk_bf16(v[2 * p], v[2 * p + 1]);
        u32 x = pk_bf16(lo_f(r) + lo_f(y), hi_f(r) + hi_f(y));
        o[i * 2 + p] = x;
        if (!LAST) {
          float a = lo_f(x), c = hi_f(x);
          s += a + c; q = __builtin_fmaf(a, a, q); q = __builtin_fmaf(c, c, q);
        }
      }
    }
    u32x4 o0 = {o[0], o[1], o[2], o[3]}, o1 = {o[4], o[5], o[6], o[7]};
    if (LAST) {
      u32x4* dst = (u32x4*)(out + (size_t)et * D + edb * 8);
      dst[0] = o0; dst[1] = o1;
    } else {
      u32x4* dst = (u32x4*)(ws + OFF_XF) + ((size_t)(et >> 4) * 64 + edb) * 16 + (et & 15);
      dst[0] = o0; dst[16] = o1;
      s += __shfl_xor(s, 1); s += __shfl_xor(s, 2);
      q += __shfl_xor(q, 1); q += __shfl_xor(q, 2);
      if ((tid & 3) == 0) { f32x2 st = {s, q}; ((f32x2*)(ws + OFF_ST))[ct * L + et] = st; }
    }
  }
}

extern "C" void kernel_launch(void* const* d_in, const int* in_sizes, int n_in, void* d_out, int out_size,
                              void* d_ws, size_t ws_size, hipStream_t stream) {
  char* ws = (char*)d_ws;
  (void)hipFuncSetAttribute((const void*)conv_kernel<1>, hipFuncAttributeMaxDynamicSharedMemorySize, CV_LDS);
  (void)hipFuncSetAttribute((const void*)conv_kernel<8>, hipFuncAttributeMaxDynamicSharedMemorySize, CV_LDS);
  (void)hipFuncSetAttribute((const void*)g1_kernel, hipFuncAttributeMaxDynamicSharedMemorySize, G1_LDS);
  (void)hipFuncSetAttribute((const void*)g2_kernel<false>, hipFuncAttributeMaxDynamicSharedMemorySize, G2_LDS);
  (void)hipFuncSetAttribute((const void*)g2_kernel<true>, hipFuncAttributeMaxDynamicSharedMemorySize, G2_LDS);
  k0_kernel<<<NB_K0, 256, 0, stream>>>((const u16*)d_in[0], (const u16*)d_in[1], (const u16*)d_in[2],
                                       (const u16*)d_in[3], (const u16*)d_in[4], (const u16*)d_in[5],
                                       (const u16*)d_in[6], (const u16*)d_in[7], (const u16*)d_in[10],
                                       (const u16*)d_in[11], (const u16*)d_in[12], ws);
  conv_kernel<1><<<256, 512, CV_LDS, stream>>>((const u16*)d_in[8], ws);
  g1_kernel<<<256, 256, G1_LDS, stream>>>(ws);
  g2_kernel<false><<<256, 256, G2_LDS, stream>>>(ws, (u16*)d_out);
  conv_kernel<8><<<256, 512, CV_LDS, stream>>>((const u16*)d_in[8], ws);
  g1_kernel<<<256, 256, G1_LDS, stream>>>(ws);
  g2_kernel<true><<<256, 256, G2_LDS, stream>>>(ws, (u16*)d_out);
}
```

```cpp
#include <hip/hip_runtime.h>
#include <stdint.h>
#include <type_traits>

typedef unsigned short u16;
typedef unsigned int u32;
typedef u32 u32x4 __attribute__((ext_vector_type(4)));
typedef u32 u32x2 __attribute__((ext_vector_type(2)));
typedef float f32x4 __attribute__((ext_vector_type(4)));
typedef float f32x2 __attribute__((ext_vector_type(2)));
typedef short s16x4 __attribute__((ext_vector_type(4)));
typedef __bf16 bf16x2 __attribute__((ext_vector_type(2)));
typedef _Float16 f16x2 __attribute__((ext_vector_type(2)));
#define LAS __attribute__((address_space(3)))
#define GAS __attribute__((address_space(1)))
#define DEV __device__ __forceinline__
#define SB() __builtin_amdgcn_sched_barrier(0)
#define IC(x) decltype(x)::value
typedef __amdgpu_buffer_rsrc_t rsrc_t;

constexpr int L = 2048, D = 512, NS = 128, F = 1376;
constexpr int NT1 = 172;
constexpr int KS1 = 16;
constexpr int KS2P = 44;
constexpr float EPS = 1e-5f;
constexpr int SC1 = 16;

constexpr int ACT = D * L * 2;
constexpr int HID = F * L * 2;
constexpr int N_ALL = 7;
constexpr int OFF_CA  = 0;
constexpr int OFF_CH  = OFF_CA + N_ALL * 128 * 256;
constexpr int CTRL_BYTES = OFF_CH + 64 * 256;
constexpr int OFF_XT  = CTRL_BYTES;
constexpr int OFF_X1  = OFF_XT + 2 * ACT;
constexpr int OFF_H   = OFF_X1 + 2 * ACT;
constexpr int OFF_WGU = OFF_H + 2 * HID;
constexpr int OFF_WD  = OFF_WGU + NT1 * KS1 * 1024;
constexpr int OFF_ST0 = OFF_WD + 32 * KS2P * 1024;
constexpr int OFF_ST1 = OFF_ST0 + L * 8;
constexpr int WS_END  = OFF_ST1 + 8 * L * 8;

#define STAMP_DECL
#define STAMP()

DEV float lo_f(u32 v) { return __uint_as_float(v << 16); }
DEV float hi_f(u32 v) { return __uint_as_float(v & 0xffff0000u); }
DEV u32 pk_bf16(float a, float b) {
  f32x2 v = {a, b};
  return __builtin_bit_cast(u32, __builtin_convertvector(v, bf16x2));
}
DEV u32 pk_bf16(f32x2 v) { return __builtin_bit_cast(u32, __builtin_convertvector(v, bf16x2)); }
DEV u32 pk_f16(f32x2 v) { return __builtin_bit_cast(u32, __builtin_convertvector(v, f16x2)); }
DEV f32x2 unpk(u32 v) { return (f32x2){lo_f(v), hi_f(v)}; }
DEV float rbf(float a) { return lo_f(pk_bf16(a, a)); }
DEV void ln_scalars(float sum, float sq, float& m, float& s) {
  float mean = sum * (1.0f / D);
  float var = fmaxf(__builtin_fmaf(-mean, mean, sq * (1.0f / D)), 0.0f);
  u32 mv = pk_bf16(mean, var);
  m = lo_f(mv);
  s = rbf(__builtin_amdgcn_sqrtf(rbf(hi_f(mv) + EPS)));
}
DEV f32x2 cmul(f32x2 x, f32x2 c) {
  f32x2 t, r;
  asm("v_pk_mul_f32 %0, %1, %2 op_sel:[0,0] op_sel_hi:[1,0]" : "=v"(t) : "v"(x), "v"(c));
  asm("v_pk_fma_f32 %0, %1, %2, %3 op_sel:[1,1,0] op_sel_hi:[0,1,1] neg_lo:[0,1,0]" : "=v"(r) : "v"(x), "v"(c), "v"(t));
  return r;
}

constexpr int KCOPY = 4160, KCH = 4 * KCOPY;
constexpr int LD_U = 2 * KCH;
constexpr int UCH = (256 + L) * 2;
constexpr int LD_XT = LD_U + 2 * UCH;
constexpr int LD_Y = LD_XT + 65536;
constexpr int YW = 5120;
constexpr int LD_SS = LD_Y + 4 * YW;
constexpr int LD_MS = LD_SS + 512;
constexpr int LD_OT = LD_MS + 4096;
constexpr int LD_CS = LD_OT + 4096;
constexpr int LD_W2 = LD_CS + 768;
constexpr int LDS_BYTES = LD_W2 + 16384;
constexpr int LD_KT = LD_XT + 16384;
constexpr int RROW = 144, RW = 64 * RROW;

#define ACLOB "a0","a1","a2","a3","a4","a5","a6","a7","a8","a9","a10","a11","a12","a13","a14","a15","a16","a17","a18","a19","a20","a21","a22","a23","a24","a25","a26","a27","a28","a29","a30","a31","a32","a33","a34","a35","a36","a37","a38","a39","a40","a41","a42","a43","a44","a45","a46","a47","a48","a49","a50","a51","a52","a53","a54","a55","a56","a57","a58","a59","a60","a61","a62","a63","a64","a65","a66","a67","a68","a69","a70","a71","a72","a73","a74","a75","a76","a77","a78","a79","a80","a81","a82","a83","a84","a85","a86","a87","a88","a89","a90","a91","a92","a93","a94","a95","a96","a97","a98","a99","a100","a101","a102","a103","a104","a105","a106","a107","a108","a109","a110","a111","a112","a113","a114","a115","a116","a117","a118","a119","a120","a121","a122","a123","a124","a125","a126","a127","a128","a129","a130","a131","a132","a133","a134","a135","a136","a137","a138","a139","a140","a141","a142","a143","a144","a145","a146","a147","a148","a149","a150","a151","a152","a153","a154","a155","a156","a157","a158","a159","a160","a161","a162","a163","a164","a165","a166","a167","a168","a169","a170","a171","a172","a173","a174","a175","a176","a177","a178","a179","a180","a181","a182","a183","a184","a185","a186","a187","a188","a189","a190","a191","a192","a193","a194","a195","a196","a197","a198","a199","a200","a201","a202","a203","a204","a205","a206","a207","a208","a209","a210","a211","a212","a213","a214","a215","a216","a217","a218","a219","a220","a221","a222","a223","a224","a225","a226","a227","a228","a229","a230","a231","a232","a233","a234","a235","a236","a237","a238","a239","a240","a241","a242","a243","a244","a245","a246","a247","a248","a249","a250","a251","a252","a253","a254","a255"
#define MF "v_mfma_f32_16x16x32_bf16 "
DEV void mfma_vv(f32x4& c, u32x4 a, u32x4 b) { asm(MF "%0, %1, %2, %0" : "+v"(c) : "v"(a), "v"(b)); }
DEV void mfma_v0(f32x4& c, u32x4 a, u32x4 b) { asm(MF "%0, %1, %2, 0" : "=&v"(c) : "v"(a), "v"(b)); }
template <bool FIRST> DEV void mfma_v(f32x4& c, u32x4 a, u32x4 b) { if constexpr (FIRST) mfma_v0(c, a, b); else mfma_vv(c, a, b); }
template <int R, bool FIRST> DEV void mfma_aA(f32x4& c, u32x4 b) {
  if constexpr (FIRST) asm(MF "%0, a[%c2:%c3], %1, 0" : "=&v"(c) : "v"(b), "i"(R), "i"(R + 3));
  else asm(MF "%0, a[%c2:%c3], %1, %0" : "+v"(c) : "v"(b), "i"(R), "i"(R + 3));
}
template <int R, bool FIRST> DEV void mfma_aB(f32x4& c, u32x4 a) {
  if constexpr (FIRST) asm(MF "%0, %1, a[%c2:%c3], 0" : "=&v"(c) : "v"(a), "i"(R), "i"(R + 3));
  else asm(MF "%0, %1, a[%c2:%c3], %0" : "+v"(c) : "v"(a), "i"(R), "i"(R + 3));
}
template <bool FIRST> DEV void mfma_h(f32x4& c, u32x4 a, u32x4 b) {
  if constexpr (FIRST) asm("s_nop 1\n\tv_mfma_f32_16x16x32_f16 %0, %1, %2, 0" : "=&v"(c) : "v"(a), "v"(b));
  else asm("s_nop 1\n\tv_mfma_f32_16x16x32_f16 %0, %1, %2, %0" : "+v"(c) : "v"(a), "v"(b));
}
DEV void mfma_done4(f32x4& a, f32x4& b, f32x4& c, f32x4& d) { asm volatile("s_nop 7" : "+v"(a), "+v"(b), "+v"(c), "+v"(d)); }
DEV void tie4(f32x4& a, f32x4& b, f32x4& c, f32x4& d) { asm volatile("" : "+v"(a), "+v"(b), "+v"(c), "+v"(d)); }
DEV void bar() { asm volatile("s_waitcnt lgkmcnt(0)\n\ts_barrier" ::: "memory", ACLOB); }

DEV u32x4 ld16(rsrc_t rs, int voff, int soff) { return __builtin_amdgcn_raw_buffer_load_b128(rs, voff, soff, SC1); }
DEV u32x2 ld8(rsrc_t rs, int voff, int soff) { return __builtin_amdgcn_raw_buffer_load_b64(rs, voff, soff, SC1); }
DEV void st16(rsrc_t rs, int voff, int soff, u32x4 v) { __builtin_amdgcn_raw_buffer_store_b128(v, rs, voff, soff, SC1); }
DEV int fresh_lane() {
  int l;
  asm volatile("v_mbcnt_lo_u32_b32 %0, -1, 0\n\tv_mbcnt_hi_u32_b32 %0, -1, %0" : "=v"(l));
  return l;
}
DEV u32x4 splat4(u32 c) {
  u32x4 r;
  asm volatile("v_mov_b32 %0, %4\n\tv_mov_b32 %1, %4\n\tv_mov_b32 %2, %4\n\tv_mov_b32 %3, %4"
               : "=v"(r[0]), "=v"(r[1]), "=v"(r[2]), "=v"(r[3]) : "i"(c));
  asm volatile("s_nop 1" : "+v"(r));
  return r;
}
DEV int opq(int x) { asm volatile("" : "+s"(x)); return x; }
DEV u32x2 tr_half(const char* p) { return __builtin_bit_cast(u32x2, __builtin_amdgcn_ds_read_tr16_b64_v4i16((LAS s16x4*)p)); }
DEV u32x4 tr_frag(const char* p, int o0, int o1) {
  u32x2 a = tr_half(p + o0), c = tr_half(p + o1);
  return (u32x4){a[0], a[1], c[0], c[1]};
}
template <int N, typename FN, int I = 0>
DEV void static_for(FN&& fn) {
  if constexpr (I < N) { fn(std::integral_constant<int, I>{}); static_for<N, FN, I + 1>(static_cast<FN&&>(fn)); }
}

constexpr int SPIN_CAP = 400000;
DEV int all_off(int edge, int b) { return OFF_CA + (edge * 8 + (b & 7)) * 4096; }
DEV int grp_off(int idx) { return OFF_CH + idx * 256; }
DEV u32 poll_ld(rsrc_t rs, int soff, bool all) {
  const int voff = all ? (fresh_lane() & 15) * 256 : 0;
  return __builtin_amdgcn_raw_buffer_load_b32(rs, voff, soff, SC1);
}
DEV void poll_wait(rsrc_t rs, int soff, bool all, u32 f) {
#pragma unroll 1
  for (int spins = 0; spins < SPIN_CAP; ++spins) {
    if (__all(f >= 16u)) break;
    asm volatile("" ::: "memory");
    f = poll_ld(rs, soff, all);
  }
  asm volatile("" ::: "memory");
}
template <int YOUNGER, bool LDSW>
DEV void drain_bar() {
  if constexpr (LDSW) asm volatile("s_waitcnt vmcnt(%c0) lgkmcnt(0)\n\ts_barrier" :: "i"(YOUNGER) : "memory", ACLOB);
  else asm volatile("s_waitcnt vmcnt(%c0)\n\ts_barrier" :: "i"(YOUNGER) : "memory", ACLOB);
}
DEV void signal_all(rsrc_t rs, int edge, int b, int w) {
  if (w == 0) {
    const int lane = fresh_lane();
    if (lane < 8) __builtin_amdgcn_raw_ptr_buffer_atomic_add_i32(1, rs, lane * 4096, OFF_CA + edge * 32768 + (b & 15) * 256, SC1);
  }
}
DEV void signal_grp(rsrc_t rs, int idx, int w) {
  if (w == 0) {
    const int lane = fresh_lane();
    if (lane == 0) __builtin_amdgcn_raw_ptr_buffer_atomic_add_i32(1, rs, 0, OFF_CH + idx * 256, SC1);
  }
}

template <int P> struct PrepRegs { u32x4 pa[P], pb[P]; u32x2 xv[2]; };
template <int P>
DEV void prep_ld1(rsrc_t rs, int i, int tid, int xso, int sso, PrepRegs<P>& pr) {
  if (i < 2 * P) {
    if (i & 1) pr.pb[i >> 1] = ld16(rs, tid * 32 + 16, sso + (i >> 1) * (L * 8));
    else pr.pa[i >> 1] = ld16(rs, tid * 32, sso + (i >> 1) * (L * 8));
  } else pr.xv[i - 2 * P] = ld8(rs, tid * 8, xso + (i - 2 * P) * (L * 2));
}
template <int P>
DEV void prep_load(rsrc_t rs, int tid, int xso, int sso, PrepRegs<P>& pr) {
#pragma unroll
  for (int i = 0; i < 2 * P + 2; ++i) prep_ld1<P>(rs, i, tid, xso, sso, pr);
  SB();
}
DEV void kill4(u32x4& x) { asm volatile("" : "=v"(x)); }
DEV void kill2(u32x2& x) { asm volatile("" : "=v"(x)); }
template <int P> DEV void prep_kill(PrepRegs<P>& pr) {
#pragma unroll
  for (int p = 0; p < P; ++p) { kill4(pr.pa[p]); kill4(pr.pb[p]); }
  kill2(pr.xv[0]); kill2(pr.xv[1]);
}
template <int P>
DEV void prep_ln(char* smem, int h, int tid, u32 dsk, const PrepRegs<P>& pr, u32x2 (&ud)[2]) {
  f32x4 s0 = __builtin_bit_cast(f32x4, pr.pa[0]), s1 = __builtin_bit_cast(f32x4, pr.pb[0]);
#pragma unroll
  for (int p = 1; p < P; ++p) { s0 += __builtin_bit_cast(f32x4, pr.pa[p]); s1 += __builtin_bit_cast(f32x4, pr.pb[p]); }
  float m[4], s[4];
  ln_scalars(s0[0], s0[1], m[0], s[0]);
  ln_scalars(s0[2], s0[3], m[1], s[1]);
  ln_scalars(s1[0], s1[1], m[2], s[2]);
  ln_scalars(s1[2], s1[3], m[3], s[3]);
  f32x2 mm[2] = {{m[0], m[1]}, {m[2], m[3]}};
  f32x2 rr[2] = {{__builtin_amdgcn_rcpf(s[0]), __builtin_amdgcn_rcpf(s[1])}, {__builtin_amdgcn_rcpf(s[2]), __builtin_amdgcn_rcpf(s[3])}};
#pragma unroll
  for (int c = 0; c < 2; ++c) {
    const float dk = c ? hi_f(dsk) : lo_f(dsk);
    u32x2 u;
#pragma unroll
    for (int i = 0; i < 2; ++i) {
      u32 xm = pk_bf16(unpk(pr.xv[c][i]) - mm[i]);
      u[i] = pk_bf16(unpk(xm) * rr[i]);
      ud[c][i] = pk_bf16(unpk(u[i]) * dk);
    }
    *(u32x2*)(smem + LD_U + c * UCH + (256 + h * 1024) * 2 + tid * 8) = u;
  }
}
template <int M0, int I0, int I1> struct ConvPlan {
  static constexpr int Q0 = (M0 - I1) > 0 ? (M0 - I1) : 0, Q1 = M0 + 3 - I0;
  static constexpr int NQ = Q1 - Q0 + 1, NU = I1 - I0 + 1, NF = NQ + NU;
  static constexpr int count() { int n = 0; for (int i = I0; i <= I1; ++i) for (int m = 0; m < 4; ++m) n += (M0 + m - i >= 0); return n; }
  static constexpr int NM = count();
  static constexpr int im(int t) { int n = 0; for (int i = I0; i <= I1; ++i) for (int m = 0; m < 4; ++m) if (M0 + m - i >= 0) { if (n == t) return i * 4 + m; ++n; } return 0; }
  static constexpr int frag(int f) {
    if (f == 0) return 0;
    if (f <= 4) return -(Q1 - 4 + f + 1);
    int g = f - 5, nlow = NQ - 4;
    if (g < 2 * nlow) return (g & 1) ? -(Q1 - 4 - (g >> 1) + 1) : 1 + (g >> 1);
    return 1 + nlow + (g - 2 * nlow);
  }
};
template <int M0, int I0, int I1, bool FIRST, typename FN>
DEV void conv_mfma(char* smem, int w, int lane, f32x4 (&acc)[4], FN&& fill) {
  using P = ConvPlan<M0, I0, I1>;
  const int c16 = lane & 15, kq = lane >> 4;
  const int cch = w >> 1, wr = w & 1;
  const char* tb = smem + cch * KCH + (c16 >> 2) * KCOPY + (16 + 16 * (1 + 8 * wr) - 8 * kq + 4 * (c16 & 3) - 4) * 2;
  const char* ub = smem + LD_U + cch * UCH + (256 - 16 * (1 + 8 * wr + 6) + 16 * c16 + 8 * kq) * 2;
  u32x4 T[2][P::NQ], U[2][P::NU];
  auto ldf = [&](auto rc, auto fc) {
    constexpr int rr = IC(rc), f = P::frag(IC(fc));
    if constexpr (f >= 0) U[rr & 1][f] = *(const u32x4*)(ub + (3 - rr) * 64 + (I0 + f) * 512);
    else T[rr & 1][-f - 1 - P::Q0] = tr_frag(tb, rr * 64 + (-f - 1) * 512 + 8, rr * 64 + (-f - 1) * 512);
  };
  static_for<P::NF>([&](auto fc) { ldf(std::integral_constant<int, 0>{}, fc); });
  SB();
  static_for<4 * P::NM>([&](auto nc) {
    constexpr int n = IC(nc), rr = n / P::NM, t = n % P::NM, i = P::im(t) >> 2, m = P::im(t) & 3, q = M0 + m - i;
    mfma_v<FIRST && rr == 0 && i == I0>(acc[m], T[rr & 1][q - P::Q0], U[rr & 1][i - I0]);
    if constexpr (rr < 3 && t < P::NF) ldf(std::integral_constant<int, rr + 1>{}, std::integral_constant<int, t>{});
    SB();
    fill(nc);
    SB();
  });
}
DEV void conv_out(rsrc_t rs, char* smem, int xso, int w, int lane, f32x4 (&acc)[4],
                  const u32x2 (&xv)[2], const u32x2 (&ud)[2]) {
  const int tid = w * 64 + lane;
  char* yw = smem + LD_Y + w * YW + (lane & 15) * 80 + (lane >> 4) * 16;
  const char* yb = smem + LD_Y + tid * 16 + (tid >> 2) * 16;
  const int vo = ((tid & 1) * L + (tid >> 1) * 8) * 2;
  mfma_done4(acc[0], acc[1], acc[2], acc[3]);
#pragma unroll
  for (int m = 0; m < 4; ++m) *(f32x4*)(yw + m * 1280) = acc[m];
  bar();
  u32x2 o[2];
#pragma unroll
  for (int c = 0; c < 2; ++c) {
    f32x4 y = *(const f32x4*)(yb + (2 * c) * YW) + *(const f32x4*)(yb + (2 * c + 1) * YW);
#pragma unroll
    for (int i = 0; i < 2; ++i) {
      u32 yb16 = pk_bf16(y[2 * i], y[2 * i + 1]);
      u32 y2 = pk_bf16(unpk(yb16) + unpk(ud[c][i]));
      o[c][i] = pk_bf16(unpk(xv[c][i]) + unpk(y2));
    }
  }
  const bool odd = tid & 1;
  u32 s0 = odd ? o[0][0] : o[1][0], s1 = odd ? o[0][1] : o[1][1];
  u32 g0 = __builtin_amdgcn_update_dpp(0u, s0, 0xB1, 0xf, 0xf, true);
  u32 g1 = __builtin_amdgcn_update_dpp(0u, s1, 0xB1, 0xf, 0xf, true);
  u32x4 v = odd ? (u32x4){g0, g1, o[1][0], o[1][1]} : (u32x4){o[0][0], o[0][1], g0, g1};
  st16(rs, vo, xso, v);
  SB();
}

template <int R, int IMM>
DEV void ld_agpr(rsrc_t rs, int voff, int sbase) {
  int tmp;
  asm volatile("s_add_u32 %0, %3, %c6\n\tbuffer_load_dwordx4 a[%c4:%c5], %1, %2, %0 offen sc1"
               : "=&s"(tmp) : "v"(voff), "s"(rs), "s"(sbase), "i"(R), "i"(R + 3), "i"(IMM) : "memory", "scc");
}
template <int I> DEV void ld_w1(rsrc_t rs, int voff, int so01, int so2) {
  constexpr int ks = I / 3, i = I % 3;
  if constexpr (i < 2) ld_agpr<4 * I, (i * KS1 + ks) * 1024>(rs, voff, so01);
  else ld_agpr<4 * I, ks * 1024>(rs, voff, so2);
}
template <int I> DEV void ld_w2a(rsrc_t rs, int voff, int so) { ld_agpr<192 + 4 * I, ((I & 3) * KS2P + (I >> 2)) * 1024>(rs, voff, so); }

DEV int xs_voff(int tid) { return ((tid >> 3) * L + (tid & 7) * 8) * 2; }
DEV char* xs_lds(char* smem, int tid) {
  const int fsw = ((tid >> 4) & 1) | (((tid >> 6) & 1) << 1);
  return smem + LD_XT + (tid >> 3) * 128 + ((((tid & 7) >> 1) ^ fsw) * 32) + (tid & 1) * 16;
}
constexpr int XS_STEP = 32 * L * 2;

__global__ __launch_bounds__(256, 1) void main_kernel(
    const u16* __restrict__ x0, const u16* __restrict__ Are, const u16* __restrict__ Aim,
    const u16* __restrict__ Bre, const u16* __restrict__ Bim, const u16* __restrict__ Cre,
    const u16* __restrict__ Cim, const u16* __restrict__ logdt, const u16* __restrict__ dskip,
    const u16* __restrict__ wg, const u16* __restrict__ wu, const u16* __restrict__ wd,
    char* __restrict__ ws, u16* __restrict__ out) {
  extern __shared__ __attribute__((aligned(16))) char smem[];
  const int w_ = __builtin_amdgcn_readfirstlane(threadIdx.x >> 6);
  const int b_ = blockIdx.x;
  const rsrc_t rs = __builtin_amdgcn_make_buffer_rsrc(ws, 0, WS_END, 0x00020000);
  STAMP_DECL
  STAMP();
  asm volatile("" ::: ACLOB);
  const int rt_ = ((b_ & 7) >> 1) * 4 + ((b_ >> 3) & 3), cb_ = (b_ & 1) * 8 + (b_ >> 5);
  const int rt2_ = (b_ & 7) * 4 + ((b_ >> 3) & 3), ct_ = b_ >> 5;
  const int start_ = cb_ * 10 + (cb_ < 12 ? cb_ : 12), cnt_ = cb_ < 12 ? 11 : 10;
  const int nbase_ = (w_ < 3) ? w_ * 3 : cnt_ - 2;
  const int nt0_ = start_ + nbase_;
  const int ok2_ = __builtin_amdgcn_readfirstlane((w_ < 3) && (nbase_ + 2 < cnt_ - 2));
#define ROLES \
  const int w = opq(w_), b = opq(b_), rt = opq(rt_), rt2 = opq(rt2_), ct = opq(ct_), nt0 = opq(nt0_), ok2 = opq(ok2_); \
  const int lane = fresh_lane(), tid = w * 64 + lane, c16 = lane & 15, kq = lane >> 4, wvo = lane * 16; \
  const int x1_off = OFF_X1 + layer * ACT, h_off = OFF_H + layer * HID; \
  const int hb_e = h_off + (rt2 * 2 * NT1 * 16 + w * 11 * 64) * 16, hb_l = hb_e + 64 * NT1 * 256; \
  const int s10 = (w == 3) ? 9 * 1024 : 10 * 1024; \
  (void)b; (void)rt; (void)ct; (void)nt0; (void)ok2; (void)tid; (void)c16; (void)kq; (void)wvo; (void)x1_off; (void)hb_e; (void)hb_l; (void)s10;

  u32 dsk = 0;
  PrepRegs<8> pr;
#pragma unroll 1
  for (int layer = 0; layer < 2; ++layer) {
    u32x4 xs[8];
    if (layer == 0) {
      ROLES
      const int d0 = 2 * b;
      u32x4 xf[4], xt[2], wr[4];
      {
        const rsrc_t rx = __builtin_amdgcn_make_buffer_rsrc((void*)x0, 0, L * D * 2, 0x00020000);
        const rsrc_t rg = __builtin_amdgcn_make_buffer_rsrc((void*)wg, 0, D * F * 2, 0x00020000);
        const rsrc_t ru = __builtin_amdgcn_make_buffer_rsrc((void*)wu, 0, D * F * 2, 0x00020000);
        const int tvo = (lane >> 1) * (D * 2) + (lane & 1) * 16, tso = (rt2 * 64 * D + ct * 64 + w * 16) * 2;
        xt[0] = __builtin_amdgcn_raw_buffer_load_b128(rx, tvo, tso, 0);
        xt[1] = __builtin_amdgcn_raw_buffer_load_b128(rx, tvo, tso + 32 * D * 2, 0);
        const int gvo = kq * (F * 2) + (c16 < cnt_ ? c16 : 0) * 16, gso = ((rt * 32 + w * 8) * F + start_ * 8) * 2;
        wr[0] = __builtin_amdgcn_raw_buffer_load_b128(rg, gvo, gso, 0);
        wr[1] = __builtin_amdgcn_raw_buffer_load_b128(ru, gvo, gso, 0);
        wr[2] = __builtin_amdgcn_raw_buffer_load_b128(rg, gvo, gso + 4 * F * 2, 0);
        wr[3] = __builtin_amdgcn_raw_buffer_load_b128(ru, gvo, gso + 4 * F * 2, 0);
        const int fvo = (c16 & 7) * (D * 2) + kq * 16, fso = (b * 8 * D + w * 128) * 2;
#pragma unroll
        for (int i = 0; i < 4; ++i) xf[i] = __builtin_amdgcn_raw_buffer_load_b128(rx, fvo + i * 64, fso, 0);
      }
      const int pn = b * (2 * NS) + tid;
      const u32 p_ar = Are[pn], p_ai = Aim[pn], p_br = Bre[pn], p_bi = Bim[pn], p_cr = Cre[pn], p_ci = Cim[pn];
      const u32 p_ld = logdt[d0 + (tid >> 7)];
      const u32 p_dk = *(const u32*)(dskip + d0 + (lane >> 6));
      SB();
      STAMP();
      {
        u32x4 z = splat4(0u);
        if (tid < 16) *(u32x4*)(smem + (tid >> 1) * KCOPY + (tid & 1) * 16) = z;
        if (tid >= 64 && tid < 128) *(u32x4*)(smem + LD_U + (lane >> 5) * UCH + (lane & 31) * 16) = z;
        if (tid == 128) *(u32x4*)(smem + LD_KT + 760 * 16) = z;
      }
      int ix[12];
#pragma unroll
      for (int i = 0; i < 12; i += 2) {
        const int l0 = tid * 8 - 4 + i;
        const u32 bits = pk_bf16((float)l0, (float)(l0 + 1));
        const int i0 = l0 < 256 ? (l0 < 0 ? 676 : l0) : (int)(bits & 0xffffu) - 17024;
        const int i1 = l0 < 256 ? (l0 < 0 ? 676 : l0 + 1) : (int)(bits >> 16) - 17024;
        ix[i] = (i0 + (i0 >> 3)) * 16; ix[i + 1] = (i1 + (i1 >> 3)) * 16;
      }
      SB();
      {
        char* img = smem + LD_Y + w * YW;
        *(u32x4*)(img + wvo) = xt[0];
        *(u32x4*)(img + 1024 + wvo) = xt[1];
        const char* tp = img + (kq * 8 + (c16 >> 2)) * 32 + (c16 & 3) * 8;
        u32x4 t0 = tr_frag(tp, 0, 128), t1 = tr_frag(tp, 1024, 1024 + 128);
        const int so = OFF_XT + ((ct * 64 + w * 16) * L + rt2 * 64) * 2;
        st16(rs, c16 * (L * 2) + kq * 16, so, t0);
        st16(rs, c16 * (L * 2) + kq * 16 + 64, so, t1);
      }
      constexpr int WROW = 368;
      if (c16 < 11) {
        char* wdst = smem + LD_XT + (w * 8 + kq) * WROW + c16 * 32;
#pragma unroll
        for (int i = 0; i < 2; ++i) {
          const u32x4 g = wr[2 * i], u = wr[2 * i + 1];
          *(u32x4*)(wdst + i * 4 * WROW) = (u32x4){g[0], u[0], g[1], u[1]};
          *(u32x4*)(wdst + i * 4 * WROW + 16) = (u32x4){g[2], u[2], g[3], u[3]};
        }
      }
      {
        const u32x4 ones = splat4(0x3f803f80u);
        f32x4 sa, qa;
        static_for<4>([&](auto ic) { mfma_v<IC(ic) == 0>(sa, ones, xf[IC(ic)]); mfma_v<IC(ic) == 0>(qa, xf[IC(ic)], xf[IC(ic)]); });
        asm volatile("s_nop 7" : "+v"(sa), "+v"(qa));
        if ((c16 >> 2) == kq && c16 < 8) {
          const int r = c16 & 3;
          float q = r == 0 ? qa[0] : r == 1 ? qa[1] : r == 2 ? qa[2] : qa[3];
          *(f32x2*)(smem + LD_SS + (w * 8 + c16) * 8) = (f32x2){sa[0], q};
        }
      }
      bar();
      if (tid < 8) {
        const f32x2* sp = (const f32x2*)(smem + LD_SS) + tid;
        f32x2 v = (sp[0] + sp[8]) + (sp[16] + sp[24]);
        __builtin_amdgcn_raw_buffer_store_b64(__builtin_bit_cast(u32x2, v), rs, tid * 8, OFF_ST0 + b * 64, SC1);
      }
      const int so01 = OFF_WGU + nt0 * KS1 * 1024, so2 = so01 + (ok2 ? 2 * KS1 * 1024 : 0);
      {
        const char* wsrc = smem + LD_XT + (8 * kq + (c16 >> 2)) * WROW + nbase_ * 32 + (c16 & 3) * 8;
        u32x4 f0 = tr_frag(wsrc, 0, 4 * WROW), f1 = tr_frag(wsrc, 32, 32 + 4 * WROW), f2 = tr_frag(wsrc, 64, 64 + 4 * WROW);
        st16(rs, wvo, so01 + rt * 1024, f0);
        st16(rs, wvo, so01 + (KS1 + rt) * 1024, f1);
        if (ok2) st16(rs, wvo, so2 + rt * 1024, f2);
      }
      SB();
      STAMP();
      char* pc = smem + LD_Y + w * YW + 2048;
      float kscale;
      {
        dsk = p_dk;
        const float dt = rbf(expf(lo_f(p_ld)));
        const float ar = dt * lo_f(p_ar), ai = dt * lo_f(p_ai), br = dt * lo_f(p_br), bi = dt * lo_f(p_bi);
        const float cr = lo_f(p_cr), ci = lo_f(p_ci);
        const float I2PI_HI = 0.15915494309189535f;
        const float I2PI_LO = (float)(0.15915494309189535 - (double)0.15915494309189535f);
        const float a2 = ar * 1.4426950408889634f;
        const float rh = __uint_as_float(__float_as_uint(ai * I2PI_HI) & 0xfffff000u);
        const float rl = __builtin_fmaf(ai, I2PI_LO, __builtin_fmaf(ai, I2PI_HI, -rh));
        const float kr = cr * br - ci * bi, ki = cr * bi + ci * br;
        u32 mx = max(__float_as_uint(kr) & 0x7f800000u, __float_as_uint(ki) & 0x7f800000u);
#pragma unroll
        for (int o = 1; o < 64; o <<= 1) mx = max(mx, (u32)__shfl_xor((int)mx, o));
        mx = min(max(mx, 60u << 23), 200u << 23);
        const float up = __uint_as_float((267u << 23) - mx);
        kscale = __uint_as_float(mx - (27u << 23));
        const float ge = __builtin_amdgcn_exp2f(a2 * 128.f);
        const float gf = __builtin_fmaf(128.f, rl, __builtin_amdgcn_fractf(rh * 128.f));
        *(f32x4*)(pc + lane * 32) = (f32x4){a2, rh, kr * up, -ki * up};
        *(f32x4*)(pc + lane * 32 + 16) = (f32x4){ge * __builtin_amdgcn_cosf(gf), -ge * __builtin_amdgcn_sinf(gf), rl, 0.f};
      }
      PrepRegs<1> pe, pl;
      u32 pv;
      {
        const float hrow = (float)(c16 < 8 ? c16 : c16 < 10 ? 512 + (c16 & 1) * 4 : c16 < 13 ? (c16 - 8) * 512 : 0);
        const float lcol = (float)(8 * c16);
        f32x4 ka[4];
        const char* pp = pc + kq * 128;
        static_for<4>([&](auto sc) {
          constexpr int s = IC(sc);
          if constexpr (s == 1) { drain_bar<0, false>(); signal_all(rs, 6, b, w); }
          if constexpr (s == 3) pv = poll_ld(rs, all_off(6, b), true);
          u32x4 fa, fb[4];
#pragma unroll
          for (int j = 0; j < 4; ++j) {
            const f32x4 p = *(const f32x4*)(pp + s * 512 + j * 32), g = *(const f32x4*)(pp + s * 512 + j * 32 + 16);
            const f32x2 ar = {p[0], p[1]};
            {
              const f32x2 t = __builtin_elementwise_fma(ar, (f32x2){hrow, hrow}, (f32x2){14.f, 0.f});
              const float e = __builtin_amdgcn_exp2f(t[0]);
              const float fr = __builtin_fmaf(hrow, g[2], __builtin_amdgcn_fractf(t[1]));
              fa[j] = pk_f16((f32x2){__builtin_amdgcn_cosf(fr), __builtin_amdgcn_sinf(fr)} * e);
            }
            {
              const f32x2 t = ar * lcol;
              const float e = __builtin_amdgcn_exp2f(t[0]);
              const float fr = __builtin_fmaf(lcol, g[2], __builtin_amdgcn_fractf(t[1]));
              f32x2 v = (f32x2){__builtin_amdgcn_cosf(fr), __builtin_amdgcn_sinf(-fr)} * e;
              v = cmul(v, (f32x2){p[2], p[3]});
              fb[0][j] = pk_f16(v);
#pragma unroll
              for (int c = 1; c < 4; ++c) { v = cmul(v, (f32x2){g[0], g[1]}); fb[c][j] = pk_f16(v); }
            }
          }
#pragma unroll
          for (int c = 0; c < 4; ++c) mfma_h<s == 0>(ka[c], fa, fb[c]);
        });
        mfma_done4(ka[0], ka[1], ka[2], ka[3]);
        char* kt = smem + LD_KT + w * 4;
#pragma unroll
        for (int c = 0; c < 4; ++c) {
          const int B = 16 * c + c16;
#pragma unroll
          for (int i = 0; i < 4; ++i) {
            int idx;
            if (c < 2) idx = kq < 2 ? 8 * B + 4 * kq + i : kq == 2 ? (i < 2 ? 384 + 2 * B + i : 384 + 64 * i + B) : (i == 0 ? 640 + B : 680);
            else idx = kq < 2 ? ((i & 1) ? 680 : 128 + 4 * B + 2 * kq + (i >> 1)) : kq == 2 ? (i < 2 ? 384 + 2 * B + i : 384 + 64 * i + B) : 680;
            *(float*)(kt + (idx + (idx >> 3)) * 16) = ka[c][i] * kscale;
          }
        }
      }
      STAMP();
      poll_wait(rs, all_off(6, b), true, pv);
      prep_load<1>(rs, tid, OFF_XT + d0 * L * 2, OFF_ST0, pe);
      prep_load<1>(rs, tid, OFF_XT + d0 * L * 2 + 2048, OFF_ST0 + 8192, pl);
      bar();
      STAMP();
      {
        u32 e[2][6];
#pragma unroll
        for (int i = 0; i < 6; ++i) {
          const f32x4 a = *(const f32x4*)(smem + LD_KT + ix[2 * i]), c = *(const f32x4*)(smem + LD_KT + ix[2 * i + 1]);
          e[0][i] = pk_bf16(a[0] + a[1], c[0] + c[1]);
          e[1][i] = pk_bf16(a[2] + a[3], c[2] + c[3]);
        }
#pragma unroll
        for (int c = 0; c < 2; ++c) {
          char* kb = smem + c * KCH + tid * 16 + 32;
          u32 o[5];
#pragma unroll
          for (int i = 0; i < 5; ++i) o[i] = __builtin_amdgcn_alignbit(e[c][i + 1], e[c][i], 16);
          *(u32x4*)(kb) = (u32x4){e[c][2], e[c][3], e[c][4], e[c][5]};
          *(u32x4*)(kb + KCOPY) = (u32x4){o[1], o[2], o[3], o[4]};
          *(u32x4*)(kb + 2 * KCOPY) = (u32x4){e[c][1], e[c][2], e[c][3], e[c][4]};
          *(u32x4*)(kb + 3 * KCOPY) = (u32x4){o[0], o[1], o[2], o[3]};
        }
      }
      SB();
      u32x2 ude[2], udl[2];
      prep_ln<1>(smem, 0, tid, dsk, pe, ude);
      SB();
      static_for<12>([&](auto ic) { ld_w1<IC(ic)>(rs, wvo, so01, so2); });
      bar();
      STAMP();
      f32x4 acc[4];
      conv_mfma<0, 0, 3, true>(smem, w, lane, acc, [&](auto nc) {
        constexpr int n = IC(nc);
        if constexpr (n >= 8 && (n & 3) == 0) ld_w1<12 + (n - 8) / 4>(rs, wvo, so01, so2);
      });
      prep_ln<1>(smem, 1, tid, dsk, pl, udl);
      conv_out(rs, smem, OFF_X1 + d0 * L * 2, w, lane, acc, pe.xv, ude);
      STAMP();
      const int chunk = b + 256 * w;
      const rsrc_t rwd = __builtin_amdgcn_make_buffer_rsrc((void*)wd, 0, F * D * 2, 0x00020000);
      u32x4 w2r[4];
      conv_mfma<4, 0, 7, true>(smem, w, lane, acc, [&](auto nc) {
        constexpr int n = IC(nc);
        if constexpr (n < 42 && n % 3 == 0) ld_w1<20 + n / 3>(rs, wvo, so01, so2);
        if constexpr (n == 44) { drain_bar<14, false>(); signal_all(rs, 0, b, w); }
        if constexpr (n >= 48 && n < 90 && n % 3 == 0) ld_w1<34 + (n - 48) / 3>(rs, wvo, so01, so2);
        if constexpr (n >= 92 && n < 100 && !(n & 1))
          w2r[(n - 92) >> 1] = __builtin_amdgcn_raw_buffer_load_b128(rwd, (lane >> 3) * (D * 2) + (lane & 7) * 16,
                                                                    ((chunk >> 3) * 32 * D + (chunk & 7) * 64 + ((n - 92) >> 1) * 8 * D) * 2, 0);
      });
      STAMP();
      conv_out(rs, smem, OFF_X1 + d0 * L * 2 + 2048, w, lane, acc, pl.xv, udl);
      STAMP();
      pv = poll_ld(rs, all_off(0, b), true);
      {
        asm volatile("s_waitcnt vmcnt(2)" ::: "memory");
        const u32x4 ones = splat4(0x3f803f80u);
        f32x4 c[3];
        static_for<48>([&](auto ic) { mfma_aA<4 * IC(ic), (IC(ic) < 3)>(c[IC(ic) % 3], ones); });
        asm volatile("s_nop 7" : "+v"(c[0]), "+v"(c[1]), "+v"(c[2]));
        if (c16 == 0) {
#pragma unroll
          for (int i = 0; i < 3; ++i) *(f32x4*)(smem + LD_CS + w * 192 + i * 64 + kq * 16) = -c[i];
        }
      }
      poll_wait(rs, all_off(0, b), true, pv);
      STAMP();
      const int gx = xs_voff(tid), gs = OFF_X1 + rt * 128;
#pragma unroll
      for (int i = 0; i < 8; ++i) xs[i] = ld16(rs, gx, gs + i * XS_STEP);
      SB();
      if (chunk < KS2P * 8) {
        char* img = smem + LD_W2 + w * 4096;
#pragma unroll
        for (int i = 0; i < 4; ++i)
          *(u32x4*)(img + i * 1024 + (lane >> 3) * 128 + ((((lane & 7) >> 1) ^ (((lane >> 4) & 1) | ((i & 1) << 1))) * 32) + (lane & 1) * 16) = w2r[i];
        const int fr = ((c16 >> 3) & 1) | ((kq & 1) << 1);
#pragma unroll
        for (int i = 0; i < 4; ++i)
          st16(rs, wvo, OFF_WD + (((chunk & 7) * 4 + i) * KS2P + (chunk >> 3)) * 1024,
               tr_frag(img + (8 * kq + (c16 >> 2)) * 128 + ((i ^ fr) * 32) + (c16 & 3) * 8, 0, 512));
      }
      SB();
      char* xw = xs_lds(smem, tid);
#pragma unroll
      for (int i = 0; i < 8; ++i) { *(u32x4*)(xw + i * 4096) = xs[i]; xs[i] = ld16(rs, gx, gs + (8 + i) * XS_STEP); }
      bar();
      STAMP();
    }
    else {
      ROLES
      const int xo = b * 2 * L * 2;
      u32x2 ud[2], xv[2];
      f32x4 acc[4];
      u32 pv;
      prep_ln<8>(smem, 0, tid, dsk, pr, ud);
      xv[0] = pr.xv[0]; xv[1] = pr.xv[1];
      drain_bar<0, true>();
      signal_all(rs, 3, b, w);
      STAMP();
      conv_mfma<0, 0, 3, true>(smem, w, lane, acc, [&](auto) {});
      conv_out(rs, smem, OFF_X1 + ACT + xo, w, lane, acc, xv, ud);
      STAMP();
      conv_mfma<4, 0, 3, true>(smem, w, lane, acc, [&](auto nc) {
        constexpr int n = IC(nc);
        if constexpr (n == 2) pv = poll_ld(rs, all_off(3, b), true);
        if constexpr (n == 26) { drain_bar<0, false>(); signal_all(rs, 4, b, w); poll_wait(rs, all_off(3, b), true, pv); }
        if constexpr (n >= 27 && n < 63 && (n & 1)) prep_ld1<8>(rs, (n - 27) >> 1, tid, OFF_XT + ACT + xo + 2048, OFF_ST1 + 8192, pr);
      });
      STAMP();
      prep_ln<8>(smem, 1, tid, dsk, pr, ud);
      bar();
      STAMP();
      conv_mfma<4, 4, 7, false>(smem, w, lane, acc, [&](auto nc) { if constexpr (IC(nc) == 12) pv = poll_ld(rs, all_off(4, b), true); });
      STAMP();
      poll_wait(rs, all_off(4, b), true, pv);
      STAMP();
      const int gx = xs_voff(tid), gs = OFF_X1 + ACT + rt * 128;
#pragma unroll
      for (int i = 0; i < 8; ++i) xs[i] = ld16(rs, gx, gs + i * XS_STEP);
      SB();
      conv_out(rs, smem, OFF_X1 + ACT + xo + 2048, w, lane, acc, pr.xv, ud);
      char* xw = xs_lds(smem, tid);
#pragma unroll
      for (int i = 0; i < 8; ++i) { *(u32x4*)(xw + i * 4096) = xs[i]; xs[i] = ld16(rs, gx, gs + (8 + i) * XS_STEP); }
      bar();
      STAMP();
    }
    u32x4 W2[6][4];
    u32x4 hf[11][2];
    static_for<2>([&](auto hc) {
      constexpr int h = IC(hc);
      ROLES
      const int sw2 = OFF_WD + (ct * 4 * KS2P + w * 11) * 1024;
      const int fr = ((c16 >> 3) & 1) | ((kq & 1) << 1);
      const char* xr[4];
#pragma unroll
      for (int j = 0; j < 4; ++j) xr[j] = smem + LD_XT + (8 * kq + (c16 >> 2)) * 128 + ((((w + j) & 3) ^ fr) * 32) + (c16 & 3) * 8;
      char* xw = xs_lds(smem, tid);
      const int gx = xs_voff(tid), gs = x1_off + 2048 + rt * 128;
      f32x4 ga[3][4], sa;
      const u32x4 ones = splat4(0x3f803f80u);
      const int nxt_poll = h == 0 ? all_off(layer * 4 + 1, b) : grp_off(layer * 32 + (rt2 >> 1));
      auto kloop = [&](auto gram) {
        u32x2 xb[2][4][2];
        u32 pv;
#pragma unroll
        for (int j = 0; j < 4; ++j) { xb[0][j][0] = tr_half(xr[j]); xb[0][j][1] = tr_half(xr[j] + 512); }
        SB();
        auto fill = [&](auto gc) {
          constexpr int g = IC(gc);
          if constexpr (g >= 8 && g < 16) *(u32x4*)(xw + g * 4096) = xs[g - 8];
          if constexpr (g == 16) {
            drain_bar<0, true>();
            if constexpr (h == 1) signal_grp(rs, layer * 32 + rt, w); else signal_all(rs, layer * 4 + 1, b, w);
          }
          if constexpr (h == 0) {
            if constexpr (g == 36) bar();
            if constexpr (g == 40) pv = poll_ld(rs, nxt_poll, true);
            if constexpr (g == 48) poll_wait(rs, nxt_poll, true, pv);
            if constexpr (g >= 48 && g < 56) xs[g - 48] = ld16(rs, gx, gs + (g - 48) * XS_STEP);
          } else {
            if constexpr (g < 8) { if (layer == 0) { ld_w2a<2 * g>(rs, wvo, sw2); ld_w2a<2 * g + 1>(rs, wvo, sw2); } }
            if constexpr (g >= 17 && g < 21) { if (layer == 0) xs[g - 17] = ld16(rs, wvo, sw2 + ((g - 17) * KS2P + 4) * 1024); }
            if constexpr (g >= 34 && g < 38) { if (layer == 0) *(u32x4*)(smem + LD_W2 + w * 4096 + (g - 34) * 1024 + wvo) = xs[g - 34]; }
            if constexpr (g >= 21 && g < 45) W2[(g - 21) >> 2][(g - 21) & 3] = ld16(rs, wvo, sw2 + (((g - 21) & 3) * KS2P + ((g - 21) >> 2) + 5) * 1024);
            if constexpr (g == 46) pv = poll_ld(rs, nxt_poll, false);
            if constexpr (g == 54) poll_wait(rs, nxt_poll, false, pv);
            if constexpr (g >= 54 && g < 62) hf[(g - 54) >> 1][g & 1] = ld16(rs, wvo, hb_e + ((g - 54) >> 1) * 1024 + (g & 1) * (NT1 * 256));
          }
        };
        static_for<64>([&](auto gc) {
          constexpr int g = IC(gc), ks = g >> 2, j = g & 3;
          const u32x4 x = {xb[ks & 1][j][0][0], xb[ks & 1][j][0][1], xb[ks & 1][j][1][0], xb[ks & 1][j][1][1]};
          mfma_aA<12 * ks, ks == 0>(ga[0][j], x);
          if constexpr (ks + 1 < KS1) xb[(ks + 1) & 1][j][0] = tr_half(xr[j] + (ks + 1) * 4096);
          SB();
          mfma_aA<12 * ks + 4, ks == 0>(ga[1][j], x);
          if constexpr (ks + 1 < KS1) xb[(ks + 1) & 1][j][1] = tr_half(xr[j] + (ks + 1) * 4096 + 512);
          SB();
          if constexpr (IC(gram)) mfma_v<ks == 0>(ga[2][j], x, x); else mfma_aA<12 * ks + 8, ks == 0>(ga[2][j], x);
          SB();
          fill(gc);
          SB();
          if constexpr (j == 0) { mfma_v<ks == 0>(sa, ones, x); SB(); }
        });
      };
      if (w == 3) kloop(std::integral_constant<bool, true>{}); else kloop(std::integral_constant<bool, false>{});
      STAMP();
      float* ss = (float*)(smem + LD_SS);
      if (kq == 0) ss[w * 16 + c16] = sa[0];
      f32x4 cs[3];
#pragma unroll
      for (int i = 0; i < 3; ++i) cs[i] = *(const f32x4*)(smem + LD_CS + w * 192 + i * 64 + kq * 16);
      mfma_done4(ga[2][0], ga[2][1], ga[2][2], ga[2][3]);
      tie4(ga[0][0], ga[0][1], ga[0][2], ga[0][3]);
      tie4(ga[1][0], ga[1][1], ga[1][2], ga[1][3]);
      if (w == 3 && (c16 >> 2) == kq) {
        const int r = c16 & 3;
#pragma unroll
        for (int j = 0; j < 4; ++j) {
          float q = r == 0 ? ga[2][j][0] : r == 1 ? ga[2][j][1] : r == 2 ? ga[2][j][2] : ga[2][j][3];
          ss[64 + ((3 + j) & 3) * 16 + c16] = q;
        }
      }
      if constexpr (h == 0) {
#pragma unroll
        for (int i = 0; i < 8; ++i) { *(u32x4*)(xw + i * 4096) = xs[i]; xs[i] = ld16(rs, gx, gs + (8 + i) * XS_STEP); }
      } else {
#pragma unroll
        for (int s = 4; s < 8; ++s) { hf[s][0] = ld16(rs, wvo, hb_e + s * 1024); hf[s][1] = ld16(rs, wvo, hb_e + s * 1024 + NT1 * 256); }
      }
      SB();
      bar();
      f32x4* msw = (f32x4*)(smem + LD_MS) + w * 64;
      {
        float m, s;
        ln_scalars(ss[lane], ss[64 + lane], m, s);
        msw[lane] = (f32x4){m, __builtin_amdgcn_rcpf(s) * -1.4426950408889634f, s * s, 0.f};
      }
      u32 hd[3][4];
#pragma unroll
      for (int j = 0; j < 4; ++j) {
        const f32x4 ms = msw[((w + j) & 3) * 16 + c16];
#pragma unroll
        for (int i = 0; i < 3; ++i) {
          const f32x4 v = __builtin_elementwise_fma(cs[i], (f32x4){ms[0], ms[0], ms[0], ms[0]}, ga[i][j]);
          const f32x2 g = {v[0], v[1]}, u = {v[2], v[3]};
          const f32x2 a = g * ms[1];
          const f32x2 e = {__builtin_amdgcn_exp2f(a[0]), __builtin_amdgcn_exp2f(a[1])};
          const f32x2 d = __builtin_elementwise_fma(e, (f32x2){ms[2], ms[2]}, (f32x2){ms[2], ms[2]});
          const f32x2 r = {__builtin_amdgcn_rcpf(d[0]), __builtin_amdgcn_rcpf(d[1])};
          hd[i][j] = pk_bf16((g * u) * r);
        }
      }
      const int hb = (((w + kq) & 3) * NT1 * 16 + c16) * 16, hs = h_off + ((h * 64 + rt * 4) * NT1 + nt0) * 256;
#pragma unroll
      for (int i = 0; i < 3; ++i) {
        auto a02 = __builtin_amdgcn_permlane32_swap(hd[i][0], hd[i][2], false, false);
        auto a13 = __builtin_amdgcn_permlane32_swap(hd[i][1], hd[i][3], false, false);
        auto o01 = __builtin_amdgcn_permlane16_swap(a02[0], a13[0], false, false);
        auto o23 = __builtin_amdgcn_permlane16_swap(a02[1], a13[1], false, false);
        u32x4 v = {o01[0], o01[1], o23[0], o23[1]};
        if (i < 2 || ok2) st16(rs, hb + i * 256, hs, v);
      }
      SB();
      STAMP();
    });
    static_for<2>([&](auto hc) {
      constexpr int h = IC(hc);
      ROLES
      const int tok0 = h * 1024 + rt2 * 32;
      const int evo = ((tid >> 2) * L + (tid & 3) * 8) * 2, eso = (ct * 64 * L + tok0) * 2;
      const int hb = h ? hb_l : hb_e;
      f32x4 da[4][2];
      u32x4 res, wl[4];
      u32 pv;
      static_for<88>([&](auto nc) {
        constexpr int n = IC(nc), s = n >> 3, j = (n >> 2) & 1, i = n & 3;
        if constexpr (s < 4) mfma_aB<192 + 16 * s + 4 * i, s == 0>(da[i][j], hf[s][j]);
        else if constexpr (s == 4) mfma_vv(da[i][j], hf[s][j], wl[i]);
        else mfma_vv(da[i][j], hf[s][j], W2[s - 5][i]);
        SB();
        if constexpr (s < 3 && i == 3) hf[s + 8][j] = ld16(rs, wvo, hb + (s == 2 ? s10 : (s + 8) * 1024) + j * (NT1 * 256));
        if constexpr (n >= 16 && n < 20) wl[n - 16] = *(const u32x4*)(smem + LD_W2 + w * 4096 + (n - 16) * 1024 + wvo);
        if constexpr (n == 25) res = ld16(rs, evo, x1_off + eso);
        if constexpr (h == 0 && n == 44) { drain_bar<7, false>(); signal_grp(rs, layer * 32 + 16 + rt, w); }
        if constexpr (h == 1 && n == 60) pv = poll_ld(rs, all_off(2, b), true);
        SB();
      });
      char* rw = smem + LD_XT + w * RW + c16 * RROW + kq * 16;
      const char* rb = smem + LD_XT + (tid >> 2) * RROW + (tid & 3) * 32;
      mfma_done4(da[0][1], da[1][1], da[2][1], da[3][1]);
      tie4(da[0][0], da[1][0], da[2][0], da[3][0]);
      SB();
      STAMP();
#pragma unroll
      for (int i = 0; i < 4; ++i)
#pragma unroll
        for (int j = 0; j < 2; ++j) *(f32x4*)(rw + i * 16 * RROW + j * 64) = da[i][j];
      if constexpr (h == 1) {
        if (layer == 0) {
          poll_wait(rs, all_off(2, b), true, pv);
          prep_load<8>(rs, tid, OFF_XT + ACT + b * 2 * L * 2, OFF_ST1, pr);
        } else prep_kill(pr);
      }
      bar();
      u32x4 xo;
#pragma unroll
      for (int i = 0; i < 2; ++i) {
        f32x4 v = (*(const f32x4*)(rb + i * 16) + *(const f32x4*)(rb + RW + i * 16)) +
                  (*(const f32x4*)(rb + 2 * RW + i * 16) + *(const f32x4*)(rb + 3 * RW + i * 16));
#pragma unroll
        for (int p = 0; p < 2; ++p) xo[i * 2 + p] = pk_bf16(unpk(res[i * 2 + p]) + unpk(pk_bf16(v[2 * p], v[2 * p + 1])));
      }
      *(u32x4*)(smem + LD_OT + tid * 16) = xo;
      if (layer == 0) st16(rs, evo, OFF_XT + ACT + eso, xo);
      if constexpr (h == 0) pv = poll_ld(rs, grp_off(layer * 32 + 16 + (rt2 >> 1)), false);
      const char* ob = smem + LD_OT + (8 * kq + (c16 >> 2)) * 64 + (w & 1) * 32 + (c16 & 3) * 8;
      bar();
      if (layer == 0) {
        if (w < 2) {
          u32x4 f0 = tr_frag(ob, 0, 256), f1 = tr_frag(ob, 2048, 2048 + 256);
          const u32x4 ones = splat4(0x3f803f80u);
          f32x4 sa, qa;
          mfma_v0(sa, ones, f0); mfma_v0(qa, f0, f0); mfma_vv(sa, ones, f1); mfma_vv(qa, f1, f1);
          asm volatile("s_nop 7" : "+v"(sa), "+v"(qa));
          if ((c16 >> 2) == kq) {
            const int r = c16 & 3;
            float q = r == 0 ? qa[0] : r == 1 ? qa[1] : r == 2 ? qa[2] : qa[3];
            u32x2 v = {__float_as_uint(sa[0]), __float_as_uint(q)};
            __builtin_amdgcn_raw_buffer_store_b64(v, rs, c16 * 8, OFF_ST1 + (ct * L + tok0 + w * 16) * 8, SC1);
          }
        }
      } else {
        u32x4 f = tr_frag(ob, (w >> 1) * 2048, (w >> 1) * 2048 + 256);
        const rsrc_t ro = __builtin_amdgcn_make_buffer_rsrc(out, 0, L * D * 2, 0x00020000);
        __builtin_amdgcn_raw_buffer_store_b128(f, ro, (c16 * D + kq * 8) * 2, ((tok0 + (w & 1) * 16) * D + ct * 64 + (w >> 1) * 32) * 2, 0);
      }
      SB();
      STAMP();
      if constexpr (h == 0) {
        poll_wait(rs, grp_off(layer * 32 + 16 + (rt2 >> 1)), false, pv);
        STAMP();
#pragma unroll
        for (int s = 0; s < 8; ++s) { hf[s][0] = ld16(rs, wvo, hb_l + s * 1024); hf[s][1] = ld16(rs, wvo, hb_l + s * 1024 + NT1 * 256); }
        SB();
        if (layer == 0) { drain_bar<16, false>(); signal_all(rs, 2, b, w); }
        SB();
      }
    });
  }
}

extern "C" void kernel_launch(void* const* d_in, const int* in_sizes, int n_in, void* d_out, int out_size,
                              void* d_ws, size_t ws_size, hipStream_t stream) {
  char* ws = (char*)d_ws;
  (void)hipFuncSetAttribute((const void*)main_kernel, hipFuncAttributeMaxDynamicSharedMemorySize, LDS_BYTES);
  (void)hipMemsetAsync(ws, 0, CTRL_BYTES, stream);
  main_kernel<<<256, 256, LDS_BYTES, stream>>>(
      (const u16*)d_in[0], (const u16*)d_in[1], (const u16*)d_in[2], (const u16*)d_in[3], (const u16*)d_in[4],
      (const u16*)d_in[5], (const u16*)d_in[6], (const u16*)d_in[7], (const u16*)d_in[8], (const u16*)d_in[10],
      (const u16*)d_in[11], (const u16*)d_in[12], ws, (u16*)d_out);
}
```

```cpp
#include <hip/hip_runtime.h>
#include <stdint.h>
#include <type_traits>

typedef unsigned short u16;
typedef unsigned int u32;
typedef u32 u32x4 __attribute__((ext_vector_type(4)));
typedef u32 u32x2 __attribute__((ext_vector_type(2)));
typedef float f32x4 __attribute__((ext_vector_type(4)));
typedef float f32x2 __attribute__((ext_vector_type(2)));
typedef short s16x4 __attribute__((ext_vector_type(4)));
typedef __bf16 bf16x2 __attribute__((ext_vector_type(2)));
typedef _Float16 f16x2 __attribute__((ext_vector_type(2)));
#define LAS __attribute__((address_space(3)))
#define GAS __attribute__((address_space(1)))
#define DEV __device__ __forceinline__
#define SB() __builtin_amdgcn_sched_barrier(0)
#define IC(x) decltype(x)::value
typedef __amdgpu_buffer_rsrc_t rsrc_t;

constexpr int L = 2048, D = 512, NS = 128, F = 1376;
constexpr int NT1 = 172;
constexpr int KS1 = 16;
constexpr int KS2P = 44;
constexpr float EPS = 1e-5f;
constexpr int SC1 = 16;
constexpr int SIG_CL = 9, SIG_HE = 12, SIG_HL = 48, POLL_G = 52;

constexpr int ACT = D * L * 2;
constexpr int HID = F * L * 2;
constexpr int N_ALL = 7;
constexpr int OFF_CA  = 0;
constexpr int OFF_CH  = OFF_CA + N_ALL * 128 * 256;
constexpr int OFF_INIT = OFF_CH + 64 * 256;
constexpr int CTRL_BYTES = OFF_INIT + 8 * 256;
constexpr int OFF_XT  = CTRL_BYTES;
constexpr int OFF_X1  = OFF_XT + 2 * ACT;
constexpr int OFF_H   = OFF_X1 + 2 * ACT;
constexpr int OFF_WGU = OFF_H + 2 * HID;
constexpr int OFF_WD  = OFF_WGU + NT1 * KS1 * 1024;
constexpr int OFF_ST0 = OFF_WD + 32 * KS2P * 1024;
constexpr int OFF_ST1 = OFF_ST0 + L * 8;
constexpr int WS_END  = OFF_ST1 + 8 * L * 8;

#define STAMP_DECL
#define STAMP()
#define STAMP_OUT()
#define LDS_EXTRA 0

DEV float lo_f(u32 v) { return __uint_as_float(v << 16); }
DEV float hi_f(u32 v) { return __uint_as_float(v & 0xffff0000u); }
DEV u32 pk_bf16(float a, float b) {
  f32x2 v = {a, b};
  return __builtin_bit_cast(u32, __builtin_convertvector(v, bf16x2));
}
DEV u32 pk_bf16(f32x2 v) { return __builtin_bit_cast(u32, __builtin_convertvector(v, bf16x2)); }
DEV u32 pk_f16(f32x2 v) { return __builtin_bit_cast(u32, __builtin_convertvector(v, f16x2)); }
DEV f32x2 unpk(u32 v) { return (f32x2){lo_f(v), hi_f(v)}; }
DEV float rbf(float a) { return lo_f(pk_bf16(a, a)); }
DEV f32x2 ln_mv(f32x2 sq) {
  f32x2 t = __builtin_elementwise_fma(sq, (f32x2){1.0f / D, 1.0f / D}, (f32x2){0.f, EPS});
  t[1] = fmaxf(__builtin_fmaf(-t[0], t[0], t[1]), EPS);
  return t;
}
struct CM { f32x2 re, im; };
DEV CM mkcm(f32x2 c) { return CM{(f32x2){c[0], c[0]}, (f32x2){-c[1], c[1]}}; }
DEV f32x2 cmul_h(f32x2 x, const CM& c, u32& h) {
  const f32x2 r = __builtin_elementwise_fma(x.yx, c.im, x * c.re);
  h = pk_f16(r);
  return r;
}

constexpr int KCOPY = 4160, KCH = 4 * KCOPY;
constexpr int LD_U = 2 * KCH;
constexpr int UCH = (256 + L) * 2;
constexpr int LD_XT = LD_U + 2 * UCH;
constexpr int LD_Y = LD_XT + 65536;
constexpr int YW = 5120;
constexpr int LD_SS = LD_Y + 4 * YW;
constexpr int LD_OT = LD_Y;
constexpr int LD_CS = LD_SS + 512;
constexpr int LD_W2 = LD_CS + 768;
constexpr int LDS_BYTES = LD_W2 + 32768;
static_assert(LDS_BYTES <= 163840, "LDS");
constexpr int LD_KT = LD_XT + 16384;
constexpr int RROW = 144, RW = 64 * RROW;

#define ACLOB "a0","a1","a2","a3","a4","a5","a6","a7","a8","a9","a10","a11","a12","a13","a14","a15","a16","a17","a18","a19","a20","a21","a22","a23","a24","a25","a26","a27","a28","a29","a30","a31","a32","a33","a34","a35","a36","a37","a38","a39","a40","a41","a42","a43","a44","a45","a46","a47","a48","a49","a50","a51","a52","a53","a54","a55","a56","a57","a58","a59","a60","a61","a62","a63","a64","a65","a66","a67","a68","a69","a70","a71","a72","a73","a74","a75","a76","a77","a78","a79","a80","a81","a82","a83","a84","a85","a86","a87","a88","a89","a90","a91","a92","a93","a94","a95","a96","a97","a98","a99","a100","a101","a102","a103","a104","a105","a106","a107","a108","a109","a110","a111","a112","a113","a114","a115","a116","a117","a118","a119","a120","a121","a122","a123","a124","a125","a126","a127","a128","a129","a130","a131","a132","a133","a134","a135","a136","a137","a138","a139","a140","a141","a142","a143","a144","a145","a146","a147","a148","a149","a150","a151","a152","a153","a154","a155","a156","a157","a158","a159","a160","a161","a162","a163","a164","a165","a166","a167","a168","a169","a170","a171","a172","a173","a174","a175","a176","a177","a178","a179","a180","a181","a182","a183","a184","a185","a186","a187","a188","a189","a190","a191","a192","a193","a194","a195","a196","a197","a198","a199","a200","a201","a202","a203","a204","a205","a206","a207","a208","a209","a210","a211","a212","a213","a214","a215","a216","a217","a218","a219","a220","a221","a222","a223","a224","a225","a226","a227","a228","a229","a230","a231","a232","a233","a234","a235","a236","a237","a238","a239","a240","a241","a242","a243","a244","a245","a246","a247","a248","a249","a250","a251","a252","a253","a254","a255"
#define MF "v_mfma_f32_16x16x32_bf16 "
DEV void mfma_vv(f32x4& c, u32x4 a, u32x4 b) { asm(MF "%0, %1, %2, %0" : "+v"(c) : "v"(a), "v"(b)); }
DEV void mfma_v0(f32x4& c, u32x4 a, u32x4 b) { asm(MF "%0, %1, %2, 0" : "=&v"(c) : "v"(a), "v"(b)); }
template <bool FIRST> DEV void mfma_v(f32x4& c, u32x4 a, u32x4 b) { if constexpr (FIRST) mfma_v0(c, a, b); else mfma_vv(c, a, b); }
template <int R, bool FIRST> DEV void mfma_aA(f32x4& c, u32x4 b) {
  if constexpr (FIRST) asm(MF "%0, a[%c2:%c3], %1, 0" : "=&v"(c) : "v"(b), "i"(R), "i"(R + 3));
  else asm(MF "%0, a[%c2:%c3], %1, %0" : "+v"(c) : "v"(b), "i"(R), "i"(R + 3));
}
template <int R, bool FIRST> DEV void mfma_aB(f32x4& c, u32x4 a) {
  if constexpr (FIRST) asm(MF "%0, %1, a[%c2:%c3], 0" : "=&v"(c) : "v"(a), "i"(R), "i"(R + 3));
  else asm(MF "%0, %1, a[%c2:%c3], %0" : "+v"(c) : "v"(a), "i"(R), "i"(R + 3));
}
template <bool FIRST> DEV void mfma_h(f32x4& c, u32x4 a, u32x4 b) {
  if constexpr (FIRST) asm("s_nop 1\n\tv_mfma_f32_16x16x32_f16 %0, %1, %2, 0" : "=&v"(c) : "v"(a), "v"(b));
  else asm("s_nop 1\n\tv_mfma_f32_16x16x32_f16 %0, %1, %2, %0" : "+v"(c) : "v"(a), "v"(b));
}
DEV void mfma_done4(f32x4& a, f32x4& b, f32x4& c, f32x4& d) { asm volatile("s_nop 7" : "+v"(a), "+v"(b), "+v"(c), "+v"(d)); }
DEV void tie4(f32x4& a, f32x4& b, f32x4& c, f32x4& d) { asm volatile("" : "+v"(a), "+v"(b), "+v"(c), "+v"(d)); }
DEV void bar() { asm volatile("s_waitcnt lgkmcnt(0)\n\ts_barrier" ::: "memory", ACLOB); }

DEV u32x4 ld16(rsrc_t rs, int voff, int soff) { return __builtin_amdgcn_raw_buffer_load_b128(rs, voff, soff, SC1); }
DEV u32x2 ld8(rsrc_t rs, int voff, int soff) { return __builtin_amdgcn_raw_buffer_load_b64(rs, voff, soff, SC1); }
DEV void st16(rsrc_t rs, int voff, int soff, u32x4 v) { __builtin_amdgcn_raw_buffer_store_b128(v, rs, voff, soff, SC1); }
DEV int fresh_lane() {
  int l;
  asm volatile("v_mbcnt_lo_u32_b32 %0, -1, 0\n\tv_mbcnt_hi_u32_b32 %0, -1, %0" : "=v"(l));
  return l;
}
DEV u32x4 splat4(u32 c) {
  u32x4 r;
  asm volatile("v_mov_b32 %0, %4\n\tv_mov_b32 %1, %4\n\tv_mov_b32 %2, %4\n\tv_mov_b32 %3, %4"
               : "=v"(r[0]), "=v"(r[1]), "=v"(r[2]), "=v"(r[3]) : "i"(c));
  asm volatile("s_nop 1" : "+v"(r));
  return r;
}
DEV int opq(int x) { asm volatile("" : "+s"(x)); return x; }
DEV u32x2 tr_half(const char* p) { return __builtin_bit_cast(u32x2, __builtin_amdgcn_ds_read_tr16_b64_v4i16((LAS s16x4*)p)); }
DEV u32x4 tr_frag(const char* p, int o0, int o1) {
  u32x2 a = tr_half(p + o0), c = tr_half(p + o1);
  return (u32x4){a[0], a[1], c[0], c[1]};
}
template <int N, typename FN, int I = 0>
DEV void static_for(FN&& fn) {
  if constexpr (I < N) { fn(std::integral_constant<int, I>{}); static_for<N, FN, I + 1>(static_cast<FN&&>(fn)); }
}

constexpr int SPIN_CAP = 400000;
DEV int all_off(int edge, int b) { return OFF_CA + (edge * 8 + (b & 7)) * 4096; }
DEV int grp_off(int idx) { return OFF_CH + idx * 256; }
DEV u32 poll_ld(rsrc_t rs, int soff, bool all) {
  const int voff = all ? (fresh_lane() & 15) * 256 : 0;
  return __builtin_amdgcn_raw_buffer_load_b32(rs, voff, soff, SC1);
}
DEV void poll_wait(rsrc_t rs, int soff, bool all, u32 f) {
  if (__builtin_expect(!__all(f >= 16u), 0)) {
    const int voff = all ? (fresh_lane() & 15) * 256 : 0;
    int spins = SPIN_CAP;
    u32 t;
    asm volatile("s_nop 4\n"
                 "1:\n\t"
                 "buffer_load_dword %0, %2, %3, %4 offen sc1\n\t"
                 "s_sub_u32 %1, %1, 1\n\t"
                 "s_cbranch_scc1 2f\n\t"
                 "s_waitcnt vmcnt(0)\n\t"
                 "v_cmp_gt_u32 vcc, 16, %0\n\t"
                 "s_nop 4\n\t"
                 "s_cbranch_vccnz 1b\n"
                 "2:\n\t"
                 "s_waitcnt vmcnt(0)"
                 : "=&v"(t), "+s"(spins) : "v"(voff), "s"(rs), "s"(soff) : "memory", "vcc", "scc");
  }
  asm volatile("" ::: "memory");
}
DEV void poll_wait2(rsrc_t rs, int soff, bool all, u32 f0, u32 f1) {
  if (!__all(f0 >= 16u)) poll_wait(rs, soff, all, f1);
  asm volatile("" ::: "memory");
}
template <int YOUNGER, bool LDSW>
DEV void drain_bar() {
  if constexpr (LDSW) asm volatile("s_waitcnt vmcnt(%c0) lgkmcnt(0)\n\ts_barrier" :: "i"(YOUNGER) : "memory", ACLOB);
  else asm volatile("s_waitcnt vmcnt(%c0)\n\ts_barrier" :: "i"(YOUNGER) : "memory", ACLOB);
}
constexpr int OOB = 0x7ffffff0;
DEV void signal_all(rsrc_t rs, int edge, int b, int w) {
  if (w == 0) {
    const int lane = fresh_lane();
    __builtin_amdgcn_raw_ptr_buffer_atomic_add_i32(1, rs, lane < 8 ? lane * 4096 : OOB, OFF_CA + edge * 32768 + (b & 15) * 256, SC1);
  }
}
DEV void signal_grp(rsrc_t rs, int idx, int w) {
  if (w == 0) {
    const int lane = fresh_lane();
    __builtin_amdgcn_raw_ptr_buffer_atomic_add_i32(1, rs, lane == 0 ? 0 : OOB, OFF_CH + idx * 256, SC1);
  }
}

DEV void init_bump(rsrc_t rs) {
  const int lane = fresh_lane();
  __builtin_amdgcn_raw_ptr_buffer_atomic_add_i32(1, rs, lane < 8 ? lane * 256 : OOB, OFF_INIT, SC1);
}
DEV void init_wait(rsrc_t rs, int soff, u32 f0, u32 f) {
  if (__builtin_expect(__any(f == f0), 0)) {
    int spins = SPIN_CAP;
    u32 t;
    asm volatile("s_nop 4\n"
                 "1:\n\t"
                 "buffer_load_dword %0, off, %3, %4 sc1\n\t"
                 "s_sub_u32 %1, %1, 1\n\t"
                 "s_cbranch_scc1 2f\n\t"
                 "s_waitcnt vmcnt(0)\n\t"
                 "v_cmp_eq_u32 vcc, %2, %0\n\t"
                 "s_nop 4\n\t"
                 "s_cbranch_vccnz 1b\n"
                 "2:\n\t"
                 "s_waitcnt vmcnt(0)"
                 : "=&v"(t), "+s"(spins) : "v"(f0), "s"(rs), "s"(soff) : "memory", "vcc", "scc");
  }
  asm volatile("" ::: "memory");
}

template <int P> struct PrepRegs { u32x4 pa[P], pb[P]; u32x2 xv[2]; };
template <int P>
DEV void prep_ld1(rsrc_t rs, int i, int tid, int xso, int sso, PrepRegs<P>& pr) {
  if (i < 2 * P) {
    if (i & 1) pr.pb[i >> 1] = ld16(rs, tid * 32 + 16, sso + (i >> 1) * (L * 8));
    else pr.pa[i >> 1] = ld16(rs, tid * 32, sso + (i >> 1) * (L * 8));
  } else pr.xv[i - 2 * P] = ld8(rs, tid * 8, xso + (i - 2 * P) * (L * 2));
}
template <int P>
DEV void prep_load(rsrc_t rs, int tid, int xso, int sso, PrepRegs<P>& pr) {
#pragma unroll
  for (int i = 0; i < 2 * P + 2; ++i) prep_ld1<P>(rs, i, tid, xso, sso, pr);
  SB();
}
DEV void kill4(u32x4& x) { asm volatile("" : "=v"(x)); }
DEV void kill2(u32x2& x) { asm volatile("" : "=v"(x)); }
template <int P> DEV void prep_kill(PrepRegs<P>& pr) {
#pragma unroll
  for (int p = 0; p < P; ++p) { kill4(pr.pa[p]); kill4(pr.pb[p]); }
  kill2(pr.xv[0]); kill2(pr.xv[1]);
}
template <int P>
DEV void prep_ln(char* smem, int h, int tid, u32 dsk, const PrepRegs<P>& pr, f32x4 (&z)[2]) {
  f32x4 s0 = __builtin_bit_cast(f32x4, pr.pa[0]), s1 = __builtin_bit_cast(f32x4, pr.pb[0]);
#pragma unroll
  for (int p = 1; p < P; ++p) { s0 += __builtin_bit_cast(f32x4, pr.pa[p]); s1 += __builtin_bit_cast(f32x4, pr.pb[p]); }
  const f32x2 t0 = ln_mv(s0.xy), t1 = ln_mv(s0.zw), t2 = ln_mv(s1.xy), t3 = ln_mv(s1.zw);
  const f32x2 rr[2] = {{__builtin_amdgcn_rsqf(t0[1]), __builtin_amdgcn_rsqf(t1[1])}, {__builtin_amdgcn_rsqf(t2[1]), __builtin_amdgcn_rsqf(t3[1])}};
  const f32x2 mr[2] = {{-t0[0] * rr[0][0], -t1[0] * rr[0][1]}, {-t2[0] * rr[1][0], -t3[0] * rr[1][1]}};
#pragma unroll
  for (int c = 0; c < 2; ++c) {
    const float dk = c ? hi_f(dsk) : lo_f(dsk);
    u32x2 u;
    f32x2 zz[2];
#pragma unroll
    for (int i = 0; i < 2; ++i) {
      const f32x2 x = unpk(pr.xv[c][i]);
      const f32x2 uf = __builtin_elementwise_fma(x, rr[i], mr[i]);
      u[i] = pk_bf16(uf);
      zz[i] = __builtin_elementwise_fma(uf, (f32x2){dk, dk}, x);
    }
    z[c] = (f32x4){zz[0][0], zz[0][1], zz[1][0], zz[1][1]};
    *(u32x2*)(smem + LD_U + c * UCH + (256 + h * 1024) * 2 + tid * 8) = u;
  }
}
template <int M0, int I0, int I1> struct ConvPlan {
  static constexpr int Q0 = (M0 - I1) > 0 ? (M0 - I1) : 0, Q1 = M0 + 3 - I0;
  static constexpr int NQ = Q1 - Q0 + 1, NU = I1 - I0 + 1, NF = NQ + NU;
  static constexpr int count() { int n = 0; for (int i = I0; i <= I1; ++i) for (int m = 0; m < 4; ++m) n += (M0 + m - i >= 0); return n; }
  static constexpr int NM = count();
  static constexpr int im(int t) { int n = 0; for (int i = I0; i <= I1; ++i) for (int m = 0; m < 4; ++m) if (M0 + m - i >= 0) { if (n == t) return i * 4 + m; ++n; } return 0; }
  static constexpr int frag(int f) {
    if (f == 0) return 0;
    if (f <= 4) return -(Q1 - 4 + f + 1);
    int g = f - 5, nlow = NQ - 4;
    if (g < 2 * nlow) return (g & 1) ? -(Q1 - 4 - (g >> 1) + 1) : 1 + (g >> 1);
    return 1 + nlow + (g - 2 * nlow);
  }
};
template <int M0, int I0, int I1, bool FIRST, typename FN>
DEV void conv_mfma(char* smem, int w, int lane, f32x4 (&acc)[4], FN&& fill) {
  using P = ConvPlan<M0, I0, I1>;
  const int c16 = lane & 15, kq = lane >> 4;
  const int cch = w >> 1, wr = w & 1;
  const char* tb = smem + cch * KCH + (c16 >> 2) * KCOPY + (16 + 16 * (1 + 8 * wr) - 8 * kq + 4 * (c16 & 3) - 4) * 2;
  const char* ub = smem + LD_U + cch * UCH + (256 - 16 * (1 + 8 * wr + 6) + 16 * c16 + 8 * kq) * 2;
  u32x4 T[2][P::NQ], U[2][P::NU];
  auto ldf = [&](auto rc, auto fc) {
    constexpr int rr = IC(rc), f = P::frag(IC(fc));
    if constexpr (f >= 0) U[rr & 1][f] = *(const u32x4*)(ub + (3 - rr) * 64 + (I0 + f) * 512);
    else T[rr & 1][-f - 1 - P::Q0] = tr_frag(tb, rr * 64 + (-f - 1) * 512 + 8, rr * 64 + (-f - 1) * 512);
  };
  static_for<P::NF>([&](auto fc) { ldf(std::integral_constant<int, 0>{}, fc); });
  SB();
  static_for<4 * P::NM>([&](auto nc) {
    constexpr int n = IC(nc), rr = n / P::NM, t = n % P::NM, i = P::im(t) >> 2, m = P::im(t) & 3, q = M0 + m - i;
    mfma_v<FIRST && rr == 0 && i == I0>(acc[m], T[rr & 1][q - P::Q0], U[rr & 1][i - I0]);
    if constexpr (rr < 3 && t < P::NF) ldf(std::integral_constant<int, rr + 1>{}, std::integral_constant<int, t>{});
    SB();
    fill(nc);
    SB();
  });
}
DEV void conv_out(rsrc_t rs, char* smem, int xso, int w, int lane, f32x4 (&acc)[4], const f32x4 (&z)[2]) {
  const int tid = w * 64 + lane;
  char* yw = smem + LD_Y + w * YW + (lane & 15) * 80 + (lane >> 4) * 16;
  const char* yb = smem + LD_Y + tid * 16 + (tid >> 2) * 16;
  const int vo = ((tid & 1) * L + (tid >> 1) * 8) * 2;
  mfma_done4(acc[0], acc[1], acc[2], acc[3]);
#pragma unroll
  for (int m = 0; m < 4; ++m) *(f32x4*)(yw + m * 1280) = acc[m];
  bar();
  u32x2 o[2];
#pragma unroll
  for (int c = 0; c < 2; ++c) {
    const f32x4 y = (*(const f32x4*)(yb + (2 * c) * YW) + z[c]) + *(const f32x4*)(yb + (2 * c + 1) * YW);
    o[c] = (u32x2){pk_bf16(y[0], y[1]), pk_bf16(y[2], y[3])};
  }
  const bool odd = tid & 1;
  u32 s0 = odd ? o[0][0] : o[1][0], s1 = odd ? o[0][1] : o[1][1];
  u32 g0 = __builtin_amdgcn_update_dpp(0u, s0, 0xB1, 0xf, 0xf, true);
  u32 g1 = __builtin_amdgcn_update_dpp(0u, s1, 0xB1, 0xf, 0xf, true);
  u32x4 v = odd ? (u32x4){g0, g1, o[1][0], o[1][1]} : (u32x4){o[0][0], o[0][1], g0, g1};
  st16(rs, vo, xso, v);
  SB();
}

template <int R, int IMM>
DEV void ld_agpr(rsrc_t rs, int voff, int sbase) {
  int tmp;
  asm volatile("s_add_u32 %0, %3, %c6\n\tbuffer_load_dwordx4 a[%c4:%c5], %1, %2, %0 offen sc1"
               : "=&s"(tmp) : "v"(voff), "s"(rs), "s"(sbase), "i"(R), "i"(R + 3), "i"(IMM) : "memory", "scc");
}
template <int I> DEV void ld_w1(rsrc_t rs, int voff, int so01, int so2) {
  constexpr int ks = I / 3, i = I % 3;
  if constexpr (i < 2) ld_agpr<4 * I, (i * KS1 + ks) * 1024>(rs, voff, so01);
  else ld_agpr<4 * I, ks * 1024>(rs, voff, so2);
}
template <int R, int IMM>
DEV void ld_agpr_imm(rsrc_t rs, int voff, int soff) {
  asm volatile("buffer_load_dwordx4 a[%c3:%c4], %0, %1, %2 offen offset:%c5 sc1"
               :: "v"(voff), "s"(rs), "s"(soff), "i"(R), "i"(R + 3), "i"(IMM) : "memory");
}

constexpr int XS_STEP = 32 * L * 2;
DEV int xs_voff(int w, int lane) {
  const int fsw = ((lane >> 4) & 1) | ((w & 1) << 1);
  return (w * 8 + (lane >> 3)) * (L * 2) + ((((lane & 7) >> 1) ^ fsw) * 32) + (lane & 1) * 16;
}
template <int KS>
DEV void xs_dma(rsrc_t rs, int voff, int sbase, int ldsw) {
  int tmp;
  asm volatile("s_add_u32 m0, %4, %c5\n\ts_add_u32 %0, %3, %c6\n\tbuffer_load_dwordx4 %1, %2, %0 offen sc1 lds"
               : "=&s"(tmp) : "v"(voff), "s"(rs), "s"(sbase), "s"(ldsw), "i"(LD_XT + KS * 4096), "i"(KS * XS_STEP) : "memory", "scc");
}

__global__ __launch_bounds__(256, 1) void main_kernel(
    const u16* __restrict__ x0, const u16* __restrict__ Are, const u16* __restrict__ Aim,
    const u16* __restrict__ Bre, const u16* __restrict__ Bim, const u16* __restrict__ Cre,
    const u16* __restrict__ Cim, const u16* __restrict__ logdt, const u16* __restrict__ dskip,
    const u16* __restrict__ wg, const u16* __restrict__ wu, const u16* __restrict__ wd,
    char* __restrict__ ws, u16* __restrict__ out) {
  extern __shared__ __attribute__((aligned(16))) char smem[];
  const int w_ = __builtin_amdgcn_readfirstlane(threadIdx.x >> 6);
  const int b_ = blockIdx.x;
  const rsrc_t rs = __builtin_amdgcn_make_buffer_rsrc(ws, 0, WS_END, 0x00020000);
  STAMP_DECL
  STAMP();
  asm volatile("" ::: ACLOB);
  u32 cpf;
  {
    unsigned long long pc; int len;
    asm volatile("s_getpc_b64 %0\n.Lmk_code_begin:\n\ts_mov_b32 %1, .Lmk_code_end-.Lmk_code_begin" : "=s"(pc), "=s"(len));
    const int off = min((int)((min(threadIdx.x & 63, 4u) * 4 + (threadIdx.x >> 6)) * 32 + (blockIdx.x >> 3)) * 128, len - 4) & ~3;
    cpf = *(const GAS u32*)(pc + (unsigned long long)(u32)off);
    SB();
    asm volatile("" :: "s"(x0), "s"(Are), "s"(Aim), "s"(Bre), "s"(Bim), "s"(Cre), "s"(Cim), "s"(logdt), "s"(dskip), "s"(wg), "s"(wu), "s"(wd), "s"(ws));
  }
  const int rt_ = ((b_ & 7) >> 1) * 4 + ((b_ >> 3) & 3), cb_ = (b_ & 1) * 8 + (b_ >> 5);
  const int rt2_ = (b_ & 7) * 4 + ((b_ >> 3) & 3), ct_ = b_ >> 5;
  const int start_ = cb_ * 10 + (cb_ < 12 ? cb_ : 12), cnt_ = cb_ < 12 ? 11 : 10;
  const int nbase_ = (w_ < 3) ? w_ * 3 : cnt_ - 2;
  const int nt0_ = start_ + nbase_;
  const int ok2_ = __builtin_amdgcn_readfirstlane((w_ < 3) && (nbase_ + 2 < cnt_ - 2));
#define ROLES \
  const int w = w_, b = b_, rt = rt_, rt2 = rt2_, ct = ct_, nt0 = nt0_, ok2 = ok2_; \
  const int lane = fresh_lane(), tid = w * 64 + lane, c16 = lane & 15, kq = lane >> 4, wvo = lane * 16; \
  const int x1_off = OFF_X1 + layer * ACT, h_off = OFF_H + layer * HID; \
  const int hb_e = h_off + (rt2 * 2 * NT1 * 16 + w * 11 * 64) * 16, hb_l = hb_e + 64 * NT1 * 256; \
  const int s10 = (w == 3) ? 9 * 1024 : 10 * 1024; \
  (void)b; (void)rt; (void)ct; (void)nt0; (void)ok2; (void)tid; (void)c16; (void)kq; (void)wvo; (void)x1_off; (void)hb_e; (void)hb_l; (void)s10;

  u32 dsk;
  const int ldsw = (int)(u32)(size_t)(LAS char*)smem + w_ * 1024;
  {
    constexpr int layer = 0;
    ROLES
    const int d0 = 2 * b;
    u32x4 xf[4], xt[2], wr[4];
    const bool initw = (b == 0) && (w == 3);
    const int init_off = OFF_INIT + (b & 7) * 256;
    const u32 f0 = __builtin_amdgcn_raw_buffer_load_b32(rs, 0, init_off, SC1);
    __builtin_amdgcn_raw_buffer_store_b32(0u, rs, initw ? lane * 256 : OOB, OFF_CA + 6 * 32768, SC1);
    {
      const int ln = b * 4 + lane;
      const int vo = initw ? OFF_CA + 6 * 32768 + 16384 + lane * 256 : (w == 1 && lane < 4 && ln < 832) ? (ln < 768 ? ln : ln + 128) * 256 : OOB;
      __builtin_amdgcn_raw_buffer_store_b32(0u, rs, vo, 0, SC1);
    }
    SB();
    {
      const rsrc_t rx = __builtin_amdgcn_make_buffer_rsrc((void*)x0, 0, L * D * 2, 0x00020000);
      const rsrc_t rg = __builtin_amdgcn_make_buffer_rsrc((void*)wg, 0, D * F * 2, 0x00020000);
      const rsrc_t ru = __builtin_amdgcn_make_buffer_rsrc((void*)wu, 0, D * F * 2, 0x00020000);
      const int tvo = (lane >> 1) * (D * 2) + (lane & 1) * 16, tso = (rt2 * 64 * D + ct * 64 + w * 16) * 2;
      xt[0] = __builtin_amdgcn_raw_buffer_load_b128(rx, tvo, tso, 0);
      xt[1] = __builtin_amdgcn_raw_buffer_load_b128(rx, tvo, tso + 32 * D * 2, 0);
      const int gvo = kq * (F * 2) + (c16 < cnt_ ? c16 : 0) * 16, gso = ((rt * 32 + w * 8) * F + start_ * 8) * 2;
      wr[0] = __builtin_amdgcn_raw_buffer_load_b128(rg, gvo, gso, 0);
      wr[1] = __builtin_amdgcn_raw_buffer_load_b128(ru, gvo, gso, 0);
      wr[2] = __builtin_amdgcn_raw_buffer_load_b128(rg, gvo, gso + 4 * F * 2, 0);
      wr[3] = __builtin_amdgcn_raw_buffer_load_b128(ru, gvo, gso + 4 * F * 2, 0);
      const int fvo = (c16 & 7) * (D * 2) + kq * 16, fso = (b * 8 * D + w * 128) * 2;
#pragma unroll
      for (int i = 0; i < 4; ++i) xf[i] = __builtin_amdgcn_raw_buffer_load_b128(rx, fvo + i * 64, fso, 0);
    }
    SB();
    STAMP();
    {
      u32x4 z = splat4(0u);
      if (tid < 16) *(u32x4*)(smem + (tid >> 1) * KCOPY + (tid & 1) * 16) = z;
      if (tid >= 64 && tid < 128) *(u32x4*)(smem + LD_U + (lane >> 5) * UCH + (lane & 31) * 16) = z;
      if (tid == 128) *(u32x4*)(smem + LD_KT + 760 * 16) = z;
    }
    int ix[12];
#pragma unroll
    for (int i = 0; i < 12; i += 2) {
      const int l0 = tid * 8 - 4 + i;
      const u32 bits = pk_bf16((float)l0, (float)(l0 + 1));
      const int i0 = l0 < 256 ? (l0 < 0 ? 676 : l0) : (int)(bits & 0xffffu) - 17024;
      const int i1 = l0 < 256 ? (l0 < 0 ? 676 : l0 + 1) : (int)(bits >> 16) - 17024;
      ix[i] = LD_KT + (i0 + (i0 >> 3)) * 16; ix[i + 1] = LD_KT + (i1 + (i1 >> 3)) * 16;
      asm volatile("" : "+v"(ix[i]), "+v"(ix[i + 1]));
    }
    SB();
    if (initw) { asm volatile("s_waitcnt vmcnt(10)" ::: "memory"); init_bump(rs); }
    const u32 pn = b * (2 * NS) + tid;
    const u32 p_ar = Are[pn], p_ai = Aim[pn], p_br = Bre[pn], p_bi = Bim[pn];
    SB();
    int kx[2][2][4];
#pragma unroll
    for (int c = 0; c < 2; ++c) {
      const int B = 16 * c + c16;
#pragma unroll
      for (int i = 0; i < 4; ++i) {
        const int i0 = kq < 2 ? 8 * B + 4 * kq + i : kq == 2 ? 256 + 4 * B + i : 384 + (i >> 1) * 64 + 2 * B + (i & 1);
        const int i1 = kq == 0 ? 512 + 32 * i + B : (kq == 1 && i == 0) ? 640 + B : 680;
        kx[0][c][i] = LD_KT + (i0 + (i0 >> 3)) * 16 + w * 4; kx[1][c][i] = LD_KT + (i1 + (i1 >> 3)) * 16 + w * 4;
        asm volatile("" : "+v"(kx[0][c][i]), "+v"(kx[1][c][i]));
      }
    }
    SB();
    const u32 p_cr = Cre[pn], p_ci = Cim[pn];
    const u32 p_ld = logdt[(u32)(d0 + (tid >> 7))];
    dsk = ((const u32*)dskip)[(u32)(b + (lane >> 6))];
    const int chunk = b + 256 * (w ^ 2);
    u32x4 w2r[4];
    {
      const rsrc_t rwd = __builtin_amdgcn_make_buffer_rsrc((void*)wd, 0, F * D * 2, 0x00020000);
#pragma unroll
      for (int i = 0; i < 4; ++i)
        w2r[i] = __builtin_amdgcn_raw_buffer_load_b128(rwd, (lane >> 3) * (D * 2) + (lane & 7) * 16, ((chunk >> 3) * 32 * D + (chunk & 7) * 64 + i * 8 * D) * 2, 0);
    }
    SB();
    STAMP();
    {
      char* img = smem + LD_Y + w * YW;
      *(u32x4*)(img + wvo) = xt[0];
      *(u32x4*)(img + 1024 + wvo) = xt[1];
      const char* tp = img + (kq * 8 + (c16 >> 2)) * 32 + (c16 & 3) * 8;
      u32x4 t0 = tr_frag(tp, 0, 128), t1 = tr_frag(tp, 1024, 1024 + 128);
      const int so = OFF_XT + ((ct * 64 + w * 16) * L + rt2 * 64) * 2;
      st16(rs, c16 * (L * 2) + kq * 16, so, t0);
      st16(rs, c16 * (L * 2) + kq * 16 + 64, so, t1);
    }
    constexpr int WROW = 368;
    if (c16 < 11) {
      char* wdst = smem + LD_XT + (w * 8 + kq) * WROW + c16 * 32;
#pragma unroll
      for (int i = 0; i < 2; ++i) {
        const u32x4 g = wr[2 * i], u = wr[2 * i + 1];
        *(u32x4*)(wdst + i * 4 * WROW) = (u32x4){g[0], u[0], g[1], u[1]};
        *(u32x4*)(wdst + i * 4 * WROW + 16) = (u32x4){g[2], u[2], g[3], u[3]};
      }
    }
    {
      const u32x4 ones = splat4(0x3f803f80u);
      f32x4 sa, qa;
      static_for<4>([&](auto ic) { mfma_v<IC(ic) == 0>(sa, ones, xf[IC(ic)]); mfma_v<IC(ic) == 0>(qa, xf[IC(ic)], xf[IC(ic)]); });
      asm volatile("s_nop 7" : "+v"(sa), "+v"(qa));
      if ((c16 >> 2) == kq && c16 < 8) {
        const int r = c16 & 3;
        float q = r == 0 ? qa[0] : r == 1 ? qa[1] : r == 2 ? qa[2] : qa[3];
        *(f32x2*)(smem + LD_SS + (w * 8 + c16) * 8) = (f32x2){sa[0], q};
      }
    }
    bar();
    asm volatile("" :: "v"(cpf));
    if (initw) init_bump(rs);
    const int so01 = OFF_WGU + nt0 * KS1 * 1024, so2 = so01 + (ok2 ? 2 * KS1 * 1024 : 0);
    {
      const char* wsrc = smem + LD_XT + (8 * kq + (c16 >> 2)) * WROW + nbase_ * 32 + (c16 & 3) * 8;
      u32x4 f0 = tr_frag(wsrc, 0, 4 * WROW), f1 = tr_frag(wsrc, 32, 32 + 4 * WROW), f2 = tr_frag(wsrc, 64, 64 + 4 * WROW);
      if (tid < 8) {
        const f32x2* sp = (const f32x2*)(smem + LD_SS) + tid;
        f32x2 v = (sp[0] + sp[8]) + (sp[16] + sp[24]);
        __builtin_amdgcn_raw_buffer_store_b64(__builtin_bit_cast(u32x2, v), rs, tid * 8, OFF_ST0 + b * 64, SC1);
      }
      st16(rs, wvo, so01 + rt * 1024, f0);
      st16(rs, wvo, so01 + (KS1 + rt) * 1024, f1);
      if (ok2) st16(rs, wvo, so2 + rt * 1024, f2);
    }
    SB();
    STAMP();
    PrepRegs<1> pe, pl;
    u32 pv, pv2;
    const u32 f1 = __builtin_amdgcn_raw_buffer_load_b32(rs, 0, init_off, SC1);
    SB();
    {
      constexpr int TROW = 272;
      char* tab = smem + LD_XT + 32768 - 4096 + w * (32 * TROW);
      char* tar = smem + LD_Y + w * YW;
      char* tar2 = smem + LD_W2 + w * 4096;
      const float dt = rbf(expf(lo_f(p_ld)));
      const float ar = dt * lo_f(p_ar), ai = dt * lo_f(p_ai), br = dt * lo_f(p_br), bi = dt * lo_f(p_bi);
      const float cr = lo_f(p_cr), ci = lo_f(p_ci);
      const float I2PI_HI = 0.15915494309189535f;
      const float I2PI_LO = (float)(0.15915494309189535 - (double)0.15915494309189535f);
      const float a2 = ar * 1.4426950408889634f;
      const float rh = ai * I2PI_HI;
      const float rl = __builtin_fmaf(ai, I2PI_LO, __builtin_fmaf(ai, I2PI_HI, -rh));
      auto cexp = [&](float l) {
        const float e = __builtin_amdgcn_exp2f(a2 * l);
        const float fr = __builtin_fmaf(l, rl, __builtin_amdgcn_fractf(rh * l));
        return (f32x2){__builtin_amdgcn_cosf(fr), __builtin_amdgcn_sinf(fr)} * e;
      };
      const float kr = cr * br - ci * bi, ki = cr * bi + ci * br;
      u32 mx = max(__float_as_uint(kr) & 0x7f800000u, __float_as_uint(ki) & 0x7f800000u);
      mx = max(mx, (u32)__builtin_amdgcn_update_dpp(0, (int)mx, 0xB1, 0xf, 0xf, true));
      mx = max(mx, (u32)__builtin_amdgcn_update_dpp(0, (int)mx, 0x4E, 0xf, 0xf, true));
      mx = max(mx, (u32)__builtin_amdgcn_update_dpp(0, (int)mx, 0x124, 0xf, 0xf, true));
      mx = max(mx, (u32)__builtin_amdgcn_update_dpp(0, (int)mx, 0x128, 0xf, 0xf, true));
      mx = max(max((u32)__builtin_amdgcn_readlane((int)mx, 0), (u32)__builtin_amdgcn_readlane((int)mx, 16)),
               max((u32)__builtin_amdgcn_readlane((int)mx, 32), (u32)__builtin_amdgcn_readlane((int)mx, 48)));
      mx = min(max(mx, 60u << 23), 200u << 23);
      const float up = __uint_as_float((267u << 23) - mx);
      const float kscale = __uint_as_float(mx - (27u << 23));
      const f32x2 e1 = cexp(1.f), e8 = cexp(8.f), e128 = cexp(128.f), e256 = cexp(256.f), e1024 = cexp(1024.f);
      const CM c8 = mkcm((f32x2){e8[0], -e8[1]}), c128 = mkcm((f32x2){e128[0], -e128[1]}), m1 = mkcm(e1), m256 = mkcm(e256);
      u32 hv;
      auto putr = [&](int r, u32 v) { *(u32*)((r < 16 ? tar + r * TROW : tar2 + (r - 16) * TROW) + lane * 4) = v; };
      auto putc = [&](int c, u32 v) { *(u32*)(tab + c * TROW + lane * 4) = v; };
      f32x2 p[8];
      p[0] = (f32x2){16384.f, 0.f}; putr(0, pk_f16(p[0]));
      p[1] = e1 * 16384.f; putr(1, pk_f16(p[1]));
#pragma unroll
      for (int i = 2; i < 8; ++i) { p[i] = cmul_h(p[i - 1], m1, hv); putr(i, hv); }
      f32x2 q0 = e256 * 16384.f; putr(8, pk_f16(q0));
      (void)cmul_h(p[2], m256, hv); putr(9, hv);
      f32x2 q4 = cmul_h(p[4], m256, hv); putr(10, hv);
      (void)cmul_h(p[6], m256, hv); putr(11, hv);
      q0 = cmul_h(q0, m256, hv); putr(12, hv);
      q4 = cmul_h(q4, m256, hv); putr(13, hv);
      (void)cmul_h(q0, m256, hv); putr(14, hv);
      (void)cmul_h(q4, m256, hv); putr(15, hv);
      q0 = e1024 * 16384.f; putr(16, pk_f16(q0));
#pragma unroll
      for (int i = 17; i < 21; ++i) { q0 = cmul_h(q0, m256, hv); putr(i, hv); }
      SB();
      STAMP();
      if (initw) init_bump(rs);
      init_wait(rs, init_off, f0, f1);
      drain_bar<0, false>();
      signal_all(rs, 6, b, w);
      SB();
      STAMP();
      f32x2 v0 = (f32x2){kr, -ki} * up; putc(0, pk_f16(v0));
      f32x2 v1 = cmul_h(v0, c128, hv); putc(16, hv);
#pragma unroll
      for (int i = 1; i < 16; ++i) {
        v0 = cmul_h(v0, c8, hv); putc(i, hv);
        v1 = cmul_h(v1, c8, hv); putc(16 + i, hv);
      }
      f32x4 ka[2][2];
      const char* ra0 = tar + c16 * TROW + kq * 16;
      const char* ra1 = tar2 + (c16 < 5 ? c16 : 0) * TROW + kq * 16;
      const char* rb0 = tab + c16 * TROW + kq * 16;
      static_for<4>([&](auto sc) {
        constexpr int s = IC(sc);
        const u32x4 a0 = *(const u32x4*)(ra0 + s * 64), a1 = *(const u32x4*)(ra1 + s * 64);
        const u32x4 b0 = *(const u32x4*)(rb0 + s * 64), b1 = *(const u32x4*)(rb0 + 16 * TROW + s * 64);
        mfma_h<s == 0>(ka[0][0], a0, b0); mfma_h<s == 0>(ka[0][1], a0, b1);
        mfma_h<s == 0>(ka[1][0], a1, b0); mfma_h<s == 0>(ka[1][1], a1, b1);
      });
      pv = poll_ld(rs, all_off(6, b), true);
      mfma_done4(ka[0][0], ka[0][1], ka[1][0], ka[1][1]);
#pragma unroll
      for (int r = 0; r < 2; ++r)
#pragma unroll
        for (int c = 0; c < 2; ++c) {
          const f32x4 v = ka[r][c] * kscale;
#pragma unroll
          for (int i = 0; i < 4; ++i) *(float*)(smem + kx[r][c][i]) = v[i];
        }
    }
    bar();
    STAMP();
    {
      u32 e[2][6];
#pragma unroll
      for (int i = 0; i < 6; ++i) {
        const f32x4 a = *(const f32x4*)(smem + ix[2 * i]), c = *(const f32x4*)(smem + ix[2 * i + 1]);
        e[0][i] = pk_bf16(a[0] + a[1], c[0] + c[1]);
        e[1][i] = pk_bf16(a[2] + a[3], c[2] + c[3]);
      }
      pv2 = poll_ld(rs, all_off(6, b), true);
#pragma unroll
      for (int c = 0; c < 2; ++c) {
        char* kb = smem + c * KCH + tid * 16 + 32;
        u32 o[5];
#pragma unroll
        for (int i = 0; i < 5; ++i) o[i] = __builtin_amdgcn_alignbit(e[c][i + 1], e[c][i], 16);
        *(u32x4*)(kb) = (u32x4){e[c][2], e[c][3], e[c][4], e[c][5]};
        *(u32x4*)(kb + KCOPY) = (u32x4){o[1], o[2], o[3], o[4]};
        *(u32x4*)(kb + 2 * KCOPY) = (u32x4){e[c][1], e[c][2], e[c][3], e[c][4]};
        *(u32x4*)(kb + 3 * KCOPY) = (u32x4){o[0], o[1], o[2], o[3]};
      }
    }
    SB();
    if (chunk < KS2P * 8) {
      char* img = smem + LD_W2 + 16384 + w * 4096;
#pragma unroll
      for (int i = 0; i < 4; ++i)
        *(u32x4*)(img + i * 1024 + (lane >> 3) * 128 + ((((lane & 7) >> 1) ^ (((lane >> 4) & 1) | ((i & 1) << 1))) * 32) + (lane & 1) * 16) = w2r[i];
      const int fr = ((c16 >> 3) & 1) | ((kq & 1) << 1);
#pragma unroll
      for (int i = 0; i < 4; ++i)
        st16(rs, wvo, OFF_WD + (((chunk & 7) * 4 + i) * KS2P + (chunk >> 3)) * 1024,
             tr_frag(img + (8 * kq + (c16 >> 2)) * 128 + ((i ^ fr) * 32) + (c16 & 3) * 8, 0, 512));
    }
    SB();
    STAMP();
    if (initw) {
#pragma unroll 1
      for (int spins = 0; spins < SPIN_CAP && !__all(pv2 >= 16u); ++spins) { init_bump(rs); pv2 = poll_ld(rs, all_off(6, b), true); }
    }
    poll_wait2(rs, all_off(6, b), true, pv, pv2);
    STAMP();
    prep_load<1>(rs, tid, OFF_XT + d0 * L * 2, OFF_ST0, pe);
    prep_load<1>(rs, tid, OFF_XT + d0 * L * 2 + 2048, OFF_ST0 + 8192, pl);
    f32x4 ze[2], zl[2];
    prep_ln<1>(smem, 0, tid, dsk, pe, ze);
    asm volatile("" : "+v"(pl.pa[0]), "+v"(pl.pb[0]), "+v"(pl.xv[0]), "+v"(pl.xv[1]));
    SB();
    static_for<10>([&](auto ic) { ld_w1<IC(ic)>(rs, wvo, so01, so2); });
    bar();
    STAMP();
    f32x4 acc[4];
    conv_mfma<0, 0, 3, true>(smem, w, lane, acc, [&](auto nc) {
      constexpr int n = IC(nc);
      if constexpr (n % 4 == 3) ld_w1<10 + n / 4>(rs, wvo, so01, so2);
    });
    prep_ln<1>(smem, 1, tid, dsk, pl, zl);
    conv_out(rs, smem, OFF_X1 + d0 * L * 2, w, lane, acc, ze);
    STAMP();
    conv_mfma<4, 0, 7, true>(smem, w, lane, acc, [&](auto nc) {
      constexpr int n = IC(nc);
      if constexpr (n < 40 && n % 5 == 0) ld_w1<20 + n / 5>(rs, wvo, so01, so2);
      if constexpr (n == 40) { drain_bar<8, false>(); signal_all(rs, 0, b, w); }
      if constexpr (n >= 42 && n < 102 && n % 3 == 0) ld_w1<28 + (n - 42) / 3>(rs, wvo, so01, so2);
    });
    STAMP();
    pv = poll_ld(rs, all_off(0, b), true);
    conv_out(rs, smem, OFF_X1 + d0 * L * 2 + 2048, w, lane, acc, zl);
    pv2 = poll_ld(rs, all_off(0, b), true);
    STAMP();
    SB();
    poll_wait2(rs, all_off(0, b), true, pv, pv2);
    STAMP();
    {
      const int gx = xs_voff(w, lane), gs = OFF_X1 + rt * 128;
      const u32x4 nones = splat4(0xbf80bf80u);
      f32x4 c[3];
      static_for<48>([&](auto ic) {
        mfma_aA<4 * IC(ic), (IC(ic) < 3)>(c[IC(ic) % 3], nones); SB();
        if constexpr (IC(ic) < 16 && !(IC(ic) & 1)) { xs_dma<IC(ic) / 2>(rs, gx, gs, ldsw); SB(); }
      });
      asm volatile("s_nop 7" : "+v"(c[0]), "+v"(c[1]), "+v"(c[2]));
#pragma unroll
      for (int i = 0; i < 3; ++i) *(f32x4*)(smem + LD_CS + w * 192 + i * 64 + kq * 16) = c[i];
    }
    drain_bar<0, true>();
    signal_all(rs, 1, b, w);
    STAMP();
  }
  static_for<2>([&](auto lc) {
    constexpr int layer = IC(lc);
    PrepRegs<8> pr;
    u32x4 W2[5][4];
    u32x4 hf[11][2];
    {
      ROLES
      int sw2[4];
#pragma unroll
      for (int i = 0; i < 4; ++i) sw2[i] = OFF_WD + ((ct * 4 + ((w + i) & 3)) * KS2P + w * 11) * 1024;
      const int fr = ((c16 >> 3) & 1) | ((kq & 1) << 1);
      const char* xr[4];
#pragma unroll
      for (int j = 0; j < 4; ++j) xr[j] = smem + LD_XT + (8 * kq + (c16 >> 2)) * 128 + ((((w + j) & 3) ^ fr) * 32) + (c16 & 3) * 8;
      const int gx = xs_voff(w, lane), gs = x1_off + rt * 128;
      u32x4 xs[8];
      char* ssw = smem + ((c16 >> 2) == kq ? LD_SS + w * 128 : LD_Y) + c16 * 8;
      const u32x4 ones = splat4(0x3f803f80u);
      u32x2 xb[2][4][2];
#pragma unroll
      for (int j = 0; j < 4; ++j) { xb[0][j][0] = tr_half(xr[j]); xb[0][j][1] = tr_half(xr[j] + 512); }
      SB();
      static_for<2>([&](auto hc) {
        constexpr int h = IC(hc);
        f32x4 ga[3][4], sa, qa;
        const int nxt_poll = h == 0 ? all_off(layer * 4 + 1, b) : grp_off(layer * 32 + (rt2 >> 1));
        u32 pv, pv2;
        {
          auto fill = [&](auto gc) {
            constexpr int g = IC(gc);
            if constexpr (g < 8) xs_dma<8 + g>(rs, gx, gs + h * 2048, ldsw);
            if constexpr (g == (h == 0 ? SIG_CL : SIG_HE) && (h == 1 || layer == 1)) {
              drain_bar<8, false>();
              if constexpr (h == 1) signal_grp(rs, layer * 32 + rt, w); else signal_all(rs, layer * 4 + 1, b, w);
            }
            if constexpr (g == 27) drain_bar<(h == 1 && layer == 0) ? 12 : 0, true>();
            if constexpr (h == 1) {
              if constexpr (layer == 0) {
                if constexpr (g >= 15 && g < 27) ld_agpr_imm<192 + 16 * ((g - 15) & 3) + 4 * ((g - 15) >> 2), ((g - 15) & 3) * 1024>(rs, wvo, sw2[(g - 15) >> 2]);
                if constexpr (g >= 28 && g < 32) ld_agpr_imm<192 + 16 * (g - 28) + 12, (g - 28) * 1024>(rs, wvo, sw2[3]);
                if constexpr (g >= 32 && g < 40) xs[g - 32] = ld16(rs, wvo + ((g - 32) >> 2) * 1024, sw2[(g - 32) & 3] + 4096);
                if constexpr (g >= 52 && g < 60) *(u32x4*)(smem + LD_W2 + w * 8192 + (g - 52) * 1024 + wvo) = xs[g - 52];
              }
              if constexpr (g >= 28 && g < 48) {
                constexpr int i = (g - 28) / 5, s = (g - 28) % 5;
                W2[s][i] = ld16(rs, wvo + (s & 3) * 1024, sw2[i] + (6 + (s >> 2) * 4) * 1024);
              }
            }
            if constexpr (g == POLL_G - 12) pv = poll_ld(rs, nxt_poll, h == 0);
            if constexpr (g == POLL_G - 5) pv2 = poll_ld(rs, nxt_poll, h == 0);
            if constexpr (g == POLL_G) poll_wait2(rs, nxt_poll, h == 0, pv, pv2);
            if constexpr (g >= POLL_G && g < POLL_G + 8) {
              if constexpr (h == 0) xs_dma<g - POLL_G>(rs, gx, gs + 2048, ldsw);
              else hf[g - POLL_G][0] = ld16(rs, wvo + ((g - POLL_G) & 3) * 1024, hb_e + ((g - POLL_G) >> 2) * 4096);
            }
          };
          static_for<60>([&](auto gc) {
            constexpr int g = IC(gc), ks = g >> 2, j = g & 3;
            const u32x4 x = {xb[ks & 1][j][0][0], xb[ks & 1][j][0][1], xb[ks & 1][j][1][0], xb[ks & 1][j][1][1]};
            mfma_aA<12 * ks, ks == 0>(ga[0][j], x);
            xb[(ks + 1) & 1][j][0] = tr_half(xr[j] + (ks + 1) * 4096);
            SB();
            mfma_aA<12 * ks + 4, ks == 0>(ga[1][j], x);
            xb[(ks + 1) & 1][j][1] = tr_half(xr[j] + (ks + 1) * 4096 + 512);
            SB();
            mfma_aA<12 * ks + 8, ks == 0>(ga[2][j], x);
            SB();
            fill(gc);
            SB();
            if constexpr (j == 0) { mfma_v<ks == 0>(sa, ones, x); SB(); mfma_v<ks == 0>(qa, x, x); SB(); }
          });
          u32x4 x[4];
#pragma unroll
          for (int j = 0; j < 4; ++j) x[j] = (u32x4){xb[1][j][0][0], xb[1][j][0][1], xb[1][j][1][0], xb[1][j][1][1]};
          mfma_vv(sa, ones, x[0]); SB();
          mfma_vv(qa, x[0], x[0]); SB();
#pragma unroll
          for (int j = 0; j < 4; ++j) { mfma_aA<180, false>(ga[0][j], x[j]); SB(); }
#pragma unroll
          for (int j = 0; j < 4; ++j) { mfma_aA<184, false>(ga[1][j], x[j]); SB(); }
          asm volatile("" : "+v"(sa), "+v"(qa));
          {
            const int r = c16 & 3;
            const float q01 = (r & 1) ? qa[1] : qa[0];
            SB();
            mfma_aA<188, false>(ga[2][0], x[0]); SB();
            const float q23 = (r & 1) ? qa[3] : qa[2];
            const float q = (r & 2) ? q23 : q01;
            SB();
            mfma_aA<188, false>(ga[2][1], x[1]); SB();
            *(f32x2*)ssw = (f32x2){sa[0], q};
            SB();
          }
          mfma_aA<188, false>(ga[2][2], x[2]); SB();
          mfma_aA<188, false>(ga[2][3], x[3]); SB();
        }
        STAMP();
        f32x4 cs[3];
#pragma unroll
        for (int i = 0; i < 3; ++i) cs[i] = *(const f32x4*)(smem + LD_CS + w * 192 + i * 64 + kq * 16);
        bar();
        f32x2 st[4];
#pragma unroll
        for (int j = 0; j < 4; ++j) st[j] = *(const f32x2*)(smem + LD_SS + (((w + j) & 3) * 16 + c16) * 8);
        tie4(ga[0][0], ga[0][1], ga[0][2], ga[0][3]);
        tie4(ga[1][0], ga[1][1], ga[1][2], ga[1][3]);
        tie4(ga[2][0], ga[2][1], ga[2][2], ga[2][3]);
        SB();
        u32 hd[3][4];
#pragma unroll
        for (int j = 0; j < 4; ++j) {
          const f32x2 mv = ln_mv(st[j]);
          const float nl = __builtin_amdgcn_rsqf(mv[1]) * -1.4426950408889634f;
#pragma unroll
          for (int i = 0; i < 3; ++i) {
            const f32x4 v = __builtin_elementwise_fma(cs[i], (f32x4){mv[0], mv[0], mv[0], mv[0]}, ga[i][j]);
            const f32x2 g = {v[0], v[1]}, u = {v[2], v[3]};
            const f32x2 a = g * nl;
            const f32x2 e = {__builtin_amdgcn_exp2f(a[0]), __builtin_amdgcn_exp2f(a[1])};
            const f32x2 d = __builtin_elementwise_fma(e, (f32x2){mv[1], mv[1]}, (f32x2){mv[1], mv[1]});
            const f32x2 r = {__builtin_amdgcn_rcpf(d[0]), __builtin_amdgcn_rcpf(d[1])};
            hd[i][j] = pk_bf16((g * u) * r);
            if constexpr (h == 1) {
              const int t = j * 3 + i;
              SB();
              if (t < 3) hf[8 + t][0] = ld16(rs, wvo, hb_e + (t == 2 ? s10 : (8 + t) * 1024));
              else if (t < 8) hf[t - 3][1] = ld16(rs, wvo + ((t - 3) & 3) * 1024, hb_e + NT1 * 256 + ((t - 3) >> 2) * 4096);
              SB();
            }
          }
        }
        const int hb = (((w + kq) & 3) * NT1 * 16 + c16) * 16, hs = h_off + ((h * 64 + rt * 4) * NT1 + nt0) * 256;
#pragma unroll
        for (int i = 0; i < 3; ++i) {
          auto a02 = __builtin_amdgcn_permlane32_swap(hd[i][0], hd[i][2], false, false);
          auto a13 = __builtin_amdgcn_permlane32_swap(hd[i][1], hd[i][3], false, false);
          auto o01 = __builtin_amdgcn_permlane16_swap(a02[0], a13[0], false, false);
          auto o23 = __builtin_amdgcn_permlane16_swap(a02[1], a13[1], false, false);
          u32x4 v = {o01[0], o01[1], o23[0], o23[1]};
          st16(rs, (i < 2 || ok2) ? hb + i * 256 : OOB, hs, v);
        }
        SB();
        if constexpr (h == 0) {
          drain_bar<3, false>();
#pragma unroll
          for (int j = 0; j < 4; ++j) { xb[0][j][0] = tr_half(xr[j]); xb[0][j][1] = tr_half(xr[j] + 512); }
        }
        SB();
        STAMP();
      });
    }
    {
      ROLES
      char* rw[3];
#pragma unroll
      for (int i = 0; i < 3; ++i) rw[i] = smem + LD_XT + (((w + 1 + i) & 3) * 3 + i) * 2048 + wvo;
      const char* rb = smem + LD_XT + w * 6144 + wvo;
      const int rvo = c16 * (L * 2) + kq * 8;
      const int ovo = c16 * (L * 2) + (kq & 1) * 32 + (kq >> 1) * 16;
      char* ow = smem + LD_OT + (w * 16 + c16) * 64 + (kq & 1) * 32 + (kq >> 1) * 16;
      const char* ob = smem + LD_OT + (8 * kq + (c16 >> 2)) * 64 + (w & 1) * 32 + (c16 & 3) * 8;
      const char* wlp = smem + LD_W2 + w * 8192 + wvo;
      static_for<2>([&](auto hc) {
        constexpr int h = IC(hc);
        const int tok0 = h * 1024 + rt2 * 32;
        const int eso = ((ct * 64 + w * 16) * L + tok0) * 2;
        const int hb = h ? hb_l : hb_e;
        f32x4 da[4][2];
        u32x4 wl[2][4];
        u32x2 res[2];
        u32 pv, pv2;
        static_for<88>([&](auto nc) {
          constexpr int n = IC(nc), j = n / 44, s = (n % 44) >> 2, i = (n + 1) & 3;
          if constexpr (s < 4) mfma_aB<192 + 16 * s + 4 * i, s == 0>(da[i][j], hf[s][j]);
          else if constexpr (s < 6) mfma_vv(da[i][j], hf[s][j], wl[s - 4][i]);
          else mfma_vv(da[i][j], hf[s][j], W2[s - 6][i]);
          SB();
          if constexpr (n < 24 && (n & 3) == 3)
            hf[5 + (n >> 2)][1] = ld16(rs, wvo, hb + NT1 * 256 + ((n >> 2) == 5 ? s10 : (5 + (n >> 2)) * 1024));
          if constexpr (n % 44 >= 6 && n % 44 < 14) wl[(n % 44 - 6) >> 2][(n % 44 - 5) & 3] = *(const u32x4*)(wlp + (((n % 44 - 6) & 4) + ((n % 44 - 5) & 3)) * 1024);
          if constexpr (n == 25) res[0] = ld8(rs, rvo, x1_off + eso);
          if constexpr (n == 26) res[1] = ld8(rs, rvo + 32, x1_off + eso);
          if constexpr (h == 0 && n == SIG_HL) { drain_bar<8, false>(); signal_grp(rs, layer * 32 + 16 + rt, w); }
          if constexpr (n == 52 || n == 56 || n == 60) { asm volatile("" : "+v"(da[(n - 48) >> 2][0])); *(f32x4*)(rw[(n - 52) >> 2]) = da[(n - 48) >> 2][0]; }
          if constexpr (h == 1 && layer == 0 && n == 80) pv = poll_ld(rs, all_off(2, b), true);
          SB();
        });
        mfma_done4(da[0][1], da[1][1], da[2][1], da[3][1]);
        SB();
        STAMP();
#pragma unroll
        for (int i = 0; i < 3; ++i) *(f32x4*)(rw[i] + 1024) = da[i + 1][1];
        bar();
        u32x2 xo[2];
#pragma unroll
        for (int j = 0; j < 2; ++j) {
          const f32x4 rf = {lo_f(res[j][0]), hi_f(res[j][0]), lo_f(res[j][1]), hi_f(res[j][1])};
          const f32x4 v = ((da[0][j] + rf) + *(const f32x4*)(rb + j * 1024)) + (*(const f32x4*)(rb + 2048 + j * 1024) + *(const f32x4*)(rb + 4096 + j * 1024));
          xo[j] = (u32x2){pk_bf16(v[0], v[1]), pk_bf16(v[2], v[3])};
        }
        auto s0 = __builtin_amdgcn_permlane16_swap(xo[0][0], xo[1][0], false, false);
        auto s1 = __builtin_amdgcn_permlane16_swap(xo[0][1], xo[1][1], false, false);
        const u32x4 xv = {s0[0], s1[0], s0[1], s1[1]};
        *(u32x4*)ow = xv;
        if constexpr (layer == 0) st16(rs, ovo, OFF_XT + ACT + eso, xv);
        if constexpr (h == 0) pv = poll_ld(rs, grp_off(layer * 32 + 16 + (rt2 >> 1)), false);
        else if constexpr (layer == 0) pv2 = poll_ld(rs, all_off(2, b), true);
        bar();
        if constexpr (layer == 0) {
          if (w < 2) {
            u32x4 f0 = tr_frag(ob, 0, 256), f1 = tr_frag(ob, 2048, 2048 + 256);
            const u32x4 ones = splat4(0x3f803f80u);
            f32x4 sa, qa;
            mfma_v0(sa, ones, f0); mfma_v0(qa, f0, f0); mfma_vv(sa, ones, f1); mfma_vv(qa, f1, f1);
            asm volatile("s_nop 7" : "+v"(sa), "+v"(qa));
            const int r = c16 & 3;
            float q = r == 0 ? qa[0] : r == 1 ? qa[1] : r == 2 ? qa[2] : qa[3];
            u32x2 v = {__float_as_uint(sa[0]), __float_as_uint(q)};
            __builtin_amdgcn_raw_buffer_store_b64(v, rs, (c16 >> 2) == kq ? c16 * 8 : OOB, OFF_ST1 + (ct * L + tok0 + w * 16) * 8, SC1);
          }
        } else {
          u32x4 f = tr_frag(ob, (w >> 1) * 2048, (w >> 1) * 2048 + 256);
          const rsrc_t ro = __builtin_amdgcn_make_buffer_rsrc(out, 0, L * D * 2, 0x00020000);
          __builtin_amdgcn_raw_buffer_store_b128(f, ro, (c16 * D + kq * 8) * 2, ((tok0 + (w & 1) * 16) * D + ct * 64 + (w >> 1) * 32) * 2, 0);
        }
        SB();
        STAMP();
        if constexpr (h == 0) {
          poll_wait(rs, grp_off(layer * 32 + 16 + (rt2 >> 1)), false, pv);
          STAMP();
#pragma unroll
          for (int s = 0; s < 11; ++s) hf[s][0] = ld16(rs, wvo + (s < 8 ? (s & 3) * 1024 : 0), hb_l + (s < 8 ? (s >> 2) * 4096 : s == 10 ? s10 : s * 1024));
          SB();
          if constexpr (layer == 0) { drain_bar<11, false>(); signal_all(rs, 2, b, w); }
          SB();
#pragma unroll
          for (int s = 0; s < 5; ++s) hf[s][1] = ld16(rs, wvo + (s & 3) * 1024, hb_l + NT1 * 256 + (s >> 2) * 4096);
          SB();
        } else {
          if constexpr (layer == 0) {
            poll_wait2(rs, all_off(2, b), true, pv, pv2);
            STAMP();
            prep_load<8>(rs, tid, OFF_XT + ACT + b * 2 * L * 2, OFF_ST1, pr);
            drain_bar<18, false>();
            signal_all(rs, 3, b, w);
          }
        }
      });
    }
    if constexpr (layer == 0) {
      ROLES
      const int xo = b * 2 * L * 2;
      f32x4 z[2], acc[4];
      u32 pv;
      prep_ln<8>(smem, 0, tid, dsk, pr, z);
      bar();
      STAMP();
      conv_mfma<0, 0, 3, true>(smem, w, lane, acc, [&](auto) {});
      conv_out(rs, smem, OFF_X1 + ACT + xo, w, lane, acc, z);
      STAMP();
      conv_mfma<4, 0, 3, true>(smem, w, lane, acc, [&](auto nc) {
        constexpr int n = IC(nc);
        if constexpr (n == 2) pv = poll_ld(rs, all_off(3, b), true);
        if constexpr (n == 26) { drain_bar<0, false>(); signal_all(rs, 4, b, w); poll_wait(rs, all_off(3, b), true, pv); }
        if constexpr (n >= 27 && n < 63 && (n & 1)) prep_ld1<8>(rs, (n - 27) >> 1, tid, OFF_XT + ACT + xo + 2048, OFF_ST1 + 8192, pr);
      });
      STAMP();
      prep_ln<8>(smem, 1, tid, dsk, pr, z);
      bar();
      STAMP();
      conv_mfma<4, 4, 7, false>(smem, w, lane, acc, [&](auto nc) { if constexpr (IC(nc) == 12) pv = poll_ld(rs, all_off(4, b), true); });
      STAMP();
      poll_wait(rs, all_off(4, b), true, pv);
      STAMP();
      const int gx = xs_voff(w, lane), gs = OFF_X1 + ACT + rt * 128;
      static_for<8>([&](auto ic) { xs_dma<IC(ic)>(rs, gx, gs, ldsw); });
      SB();
      conv_out(rs, smem, OFF_X1 + ACT + xo + 2048, w, lane, acc, z);
      drain_bar<1, false>();
      STAMP();
    }
  });
  STAMP_OUT();
  asm volatile(".Lmk_code_end:" ::: "memory");
}

extern "C" void kernel_launch(void* const* d_in, const int* in_sizes, int n_in, void* d_out, int out_size,
                              void* d_ws, size_t ws_size, hipStream_t stream) {
  char* ws = (char*)d_ws;
  (void)hipFuncSetAttribute((const void*)main_kernel, hipFuncAttributeMaxDynamicSharedMemorySize, LDS_BYTES + LDS_EXTRA);
  main_kernel<<<256, 256, LDS_BYTES + LDS_EXTRA, stream>>>(
      (const u16*)d_in[0], (const u16*)d_in[1], (const u16*)d_in[2], (const u16*)d_in[3], (const u16*)d_in[4],
      (const u16*)d_in[5], (const u16*)d_in[6], (const u16*)d_in[7], (const u16*)d_in[8], (const u16*)d_in[10],
      (const u16*)d_in[11], (const u16*)d_in[12], ws, (u16*)d_out);
}
```

```cpp
#include <hip/hip_runtime.h>
#include <stdint.h>
#include <type_traits>

typedef unsigned short u16;
typedef unsigned int u32;
typedef u32 u32x4 __attribute__((ext_vector_type(4)));
typedef u32 u32x2 __attribute__((ext_vector_type(2)));
typedef float f32x4 __attribute__((ext_vector_type(4)));
typedef float f32x2 __attribute__((ext_vector_type(2)));
typedef short s16x4 __attribute__((ext_vector_type(4)));
typedef __bf16 bf16x2 __attribute__((ext_vector_type(2)));
typedef _Float16 f16x2 __attribute__((ext_vector_type(2)));
#define LAS __attribute__((address_space(3)))
#define GAS __attribute__((address_space(1)))
#define DEV __device__ __forceinline__
#define SB() __builtin_amdgcn_sched_barrier(0)
#define IC(x) decltype(x)::value
typedef __amdgpu_buffer_rsrc_t rsrc_t;

constexpr int L = 2048, D = 512, NS = 128, F = 1376;
constexpr int NT1 = 172;
constexpr int KS1 = 16;
constexpr int KS2P = 44;
constexpr float EPS = 1e-5f;
constexpr int SC1 = 16;
constexpr int SIG_CL = 9, SIG_HE = 12, SIG_HL = 48, POLL_G = 52;

constexpr int ACT = D * L * 2;
constexpr int HID = F * L * 2;
constexpr int N_ALL = 7;
constexpr int OFF_CA  = 0;
constexpr int OFF_CH  = OFF_CA + N_ALL * 128 * 256;
constexpr int OFF_INIT = OFF_CH + 64 * 256;
constexpr int CTRL_BYTES = OFF_INIT + 8 * 256;
constexpr int OFF_XT  = CTRL_BYTES;
constexpr int ACT2 = ACT, HID2 = HID;
constexpr int OFF_X1  = OFF_XT + ACT + ACT2;
constexpr int OFF_H   = OFF_X1 + ACT + ACT2;
constexpr int OFF_WGU = OFF_H + HID + HID2;
constexpr int OFF_WD  = OFF_WGU + NT1 * KS1 * 1024;
constexpr int OFF_ST0 = OFF_WD + 32 * KS2P * 1024;
constexpr int OFF_ST1 = OFF_ST0 + L * 8;
constexpr int WS_END  = OFF_ST1 + 8 * L * 8;

#define STAMP_DECL
#define STAMP()
#define STAMP_OUT()
#define LDS_EXTRA 0

DEV float lo_f(u32 v) { return __uint_as_float(v << 16); }
DEV float hi_f(u32 v) { return __uint_as_float(v & 0xffff0000u); }
DEV u32 pk_bf16(float a, float b) {
  f32x2 v = {a, b};
  return __builtin_bit_cast(u32, __builtin_convertvector(v, bf16x2));
}
DEV u32 pk_bf16(f32x2 v) { return __builtin_bit_cast(u32, __builtin_convertvector(v, bf16x2)); }
DEV u32 pk_f16(f32x2 v) { return __builtin_bit_cast(u32, __builtin_convertvector(v, f16x2)); }
DEV f32x2 unpk(u32 v) { return (f32x2){lo_f(v), hi_f(v)}; }
DEV float rbf(float a) { return lo_f(pk_bf16(a, a)); }
DEV f32x2 ln_mv(f32x2 sq) {
  f32x2 t = __builtin_elementwise_fma(sq, (f32x2){1.0f / D, 1.0f / D}, (f32x2){0.f, EPS});
  t[1] = fmaxf(__builtin_fmaf(-t[0], t[0], t[1]), EPS);
  return t;
}
struct CM { f32x2 re, im; };
DEV CM mkcm(f32x2 c) { return CM{(f32x2){c[0], c[0]}, (f32x2){-c[1], c[1]}}; }
DEV f32x2 cmul_h(f32x2 x, const CM& c, u32& h) {
  const f32x2 r = __builtin_elementwise_fma(x.yx, c.im, x * c.re);
  h = pk_f16(r);
  return r;
}

constexpr int KCOPY = 4160, KCH = 4 * KCOPY;
constexpr int LD_U = 2 * KCH;
constexpr int UCH = (256 + L) * 2;
constexpr int LD_XT = LD_U + 2 * UCH;
constexpr int LD_Y = LD_XT + 65536;
constexpr int YW = 5120;
constexpr int LD_SS = LD_Y + 4 * YW;
constexpr int LD_OT = LD_Y;
constexpr int LD_CS = LD_SS + 512;
constexpr int LD_W2 = LD_CS + 768;
constexpr int LDS_BYTES = LD_W2 + 32768;
static_assert(LDS_BYTES <= 163840, "LDS");
constexpr int LD_KT = LD_XT + 16384;
constexpr int RROW = 144, RW = 64 * RROW;

#define ACLOB "a0","a1","a2","a3","a4","a5","a6","a7","a8","a9","a10","a11","a12","a13","a14","a15","a16","a17","a18","a19","a20","a21","a22","a23","a24","a25","a26","a27","a28","a29","a30","a31","a32","a33","a34","a35","a36","a37","a38","a39","a40","a41","a42","a43","a44","a45","a46","a47","a48","a49","a50","a51","a52","a53","a54","a55","a56","a57","a58","a59","a60","a61","a62","a63","a64","a65","a66","a67","a68","a69","a70","a71","a72","a73","a74","a75","a76","a77","a78","a79","a80","a81","a82","a83","a84","a85","a86","a87","a88","a89","a90","a91","a92","a93","a94","a95","a96","a97","a98","a99","a100","a101","a102","a103","a104","a105","a106","a107","a108","a109","a110","a111","a112","a113","a114","a115","a116","a117","a118","a119","a120","a121","a122","a123","a124","a125","a126","a127","a128","a129","a130","a131","a132","a133","a134","a135","a136","a137","a138","a139","a140","a141","a142","a143","a144","a145","a146","a147","a148","a149","a150","a151","a152","a153","a154","a155","a156","a157","a158","a159","a160","a161","a162","a163","a164","a165","a166","a167","a168","a169","a170","a171","a172","a173","a174","a175","a176","a177","a178","a179","a180","a181","a182","a183","a184","a185","a186","a187","a188","a189","a190","a191","a192","a193","a194","a195","a196","a197","a198","a199","a200","a201","a202","a203","a204","a205","a206","a207","a208","a209","a210","a211","a212","a213","a214","a215","a216","a217","a218","a219","a220","a221","a222","a223","a224","a225","a226","a227","a228","a229","a230","a231","a232","a233","a234","a235","a236","a237","a238","a239","a240","a241","a242","a243","a244","a245","a246","a247","a248","a249","a250","a251","a252","a253","a254","a255"
#define MF "v_mfma_f32_16x16x32_bf16 "
DEV void mfma_vv(f32x4& c, u32x4 a, u32x4 b) { asm(MF "%0, %1, %2, %0" : "+v"(c) : "v"(a), "v"(b)); }
DEV void mfma_v0(f32x4& c, u32x4 a, u32x4 b) { asm(MF "%0, %1, %2, 0" : "=&v"(c) : "v"(a), "v"(b)); }
template <bool FIRST> DEV void mfma_v(f32x4& c, u32x4 a, u32x4 b) { if constexpr (FIRST) mfma_v0(c, a, b); else mfma_vv(c, a, b); }
template <int R, bool FIRST> DEV void mfma_aA(f32x4& c, u32x4 b) {
  if constexpr (FIRST) asm(MF "%0, a[%c2:%c3], %1, 0" : "=&v"(c) : "v"(b), "i"(R), "i"(R + 3));
  else asm(MF "%0, a[%c2:%c3], %1, %0" : "+v"(c) : "v"(b), "i"(R), "i"(R + 3));
}
template <int R, bool FIRST> DEV void mfma_aB(f32x4& c, u32x4 a) {
  if constexpr (FIRST) asm(MF "%0, %1, a[%c2:%c3], 0" : "=&v"(c) : "v"(a), "i"(R), "i"(R + 3));
  else asm(MF "%0, %1, a[%c2:%c3], %0" : "+v"(c) : "v"(a), "i"(R), "i"(R + 3));
}
template <bool FIRST> DEV void mfma_h(f32x4& c, u32x4 a, u32x4 b) {
  if constexpr (FIRST) asm("s_nop 1\n\tv_mfma_f32_16x16x32_f16 %0, %1, %2, 0" : "=&v"(c) : "v"(a), "v"(b));
  else asm("s_nop 1\n\tv_mfma_f32_16x16x32_f16 %0, %1, %2, %0" : "+v"(c) : "v"(a), "v"(b));
}
DEV void mfma_done4(f32x4& a, f32x4& b, f32x4& c, f32x4& d) { asm volatile("s_nop 7" : "+v"(a), "+v"(b), "+v"(c), "+v"(d)); }
DEV void tie4(f32x4& a, f32x4& b, f32x4& c, f32x4& d) { asm volatile("" : "+v"(a), "+v"(b), "+v"(c), "+v"(d)); }
DEV void bar() { asm volatile("s_waitcnt lgkmcnt(0)\n\ts_barrier" ::: "memory", ACLOB); }

DEV u32x4 ld16(rsrc_t rs, int voff, int soff) { return __builtin_amdgcn_raw_buffer_load_b128(rs, voff, soff, SC1); }
DEV u32x2 ld8(rsrc_t rs, int voff, int soff) { return __builtin_amdgcn_raw_buffer_load_b64(rs, voff, soff, SC1); }
DEV void st16(rsrc_t rs, int voff, int soff, u32x4 v) { __builtin_amdgcn_raw_buffer_store_b128(v, rs, voff, soff, SC1); }
DEV int fresh_lane() {
  int l;
  asm volatile("v_mbcnt_lo_u32_b32 %0, -1, 0\n\tv_mbcnt_hi_u32_b32 %0, -1, %0" : "=v"(l));
  return l;
}
DEV u32x4 splat4(u32 c) {
  u32x4 r;
  asm volatile("v_mov_b32 %0, %4\n\tv_mov_b32 %1, %4\n\tv_mov_b32 %2, %4\n\tv_mov_b32 %3, %4"
               : "=v"(r[0]), "=v"(r[1]), "=v"(r[2]), "=v"(r[3]) : "i"(c));
  asm volatile("s_nop 1" : "+v"(r));
  return r;
}
DEV int opq(int x) { asm volatile("" : "+s"(x)); return x; }
DEV u32x2 tr_half(const char* p) { return __builtin_bit_cast(u32x2, __builtin_amdgcn_ds_read_tr16_b64_v4i16((LAS s16x4*)p)); }
DEV u32x4 tr_frag(const char* p, int o0, int o1) {
  u32x2 a = tr_half(p + o0), c = tr_half(p + o1);
  return (u32x4){a[0], a[1], c[0], c[1]};
}
template <int N, typename FN, int I = 0>
DEV void static_for(FN&& fn) {
  if constexpr (I < N) { fn(std::integral_constant<int, I>{}); static_for<N, FN, I + 1>(static_cast<FN&&>(fn)); }
}

constexpr int SPIN_CAP = 400000;
DEV int all_off(int edge, int b) { return OFF_CA + (edge * 8 + (b & 7)) * 4096; }
DEV int grp_off(int idx) { return OFF_CH + idx * 256; }
DEV u32 poll_ld(rsrc_t rs, int soff, bool all) {
  const int voff = all ? (fresh_lane() & 15) * 256 : 0;
  return __builtin_amdgcn_raw_buffer_load_b32(rs, voff, soff, SC1);
}
DEV void poll_wait(rsrc_t rs, int soff, bool all, u32 f) {
  if (__builtin_expect(!__all(f >= 16u), 0)) {
    const int voff = all ? (fresh_lane() & 15) * 256 : 0;
    int spins = SPIN_CAP;
    u32 t;
    asm volatile("s_nop 4\n"
                 "1:\n\t"
                 "buffer_load_dword %0, %2, %3, %4 offen sc1\n\t"
                 "s_sub_u32 %1, %1, 1\n\t"
                 "s_cbranch_scc1 2f\n\t"
                 "s_waitcnt vmcnt(0)\n\t"
                 "v_cmp_gt_u32 vcc, 16, %0\n\t"
                 "s_nop 4\n\t"
                 "s_cbranch_vccnz 1b\n"
                 "2:\n\t"
                 "s_waitcnt vmcnt(0)"
                 : "=&v"(t), "+s"(spins) : "v"(voff), "s"(rs), "s"(soff) : "memory", "vcc", "scc");
  }
  asm volatile("" ::: "memory");
}
DEV void poll_wait2(rsrc_t rs, int soff, bool all, u32 f0, u32 f1) {
  if (!__all(f0 >= 16u)) poll_wait(rs, soff, all, f1);
  asm volatile("" ::: "memory");
}
template <int YOUNGER, bool LDSW>
DEV void drain_bar() {
  if constexpr (LDSW) asm volatile("s_waitcnt vmcnt(%c0) lgkmcnt(0)\n\ts_barrier" :: "i"(YOUNGER) : "memory", ACLOB);
  else asm volatile("s_waitcnt vmcnt(%c0)\n\ts_barrier" :: "i"(YOUNGER) : "memory", ACLOB);
}
constexpr int OOB = 0x7ffffff0;
DEV void signal_all(rsrc_t rs, int edge, int b, int w) {
  if (w == 0) {
    const int lane = fresh_lane();
    __builtin_amdgcn_raw_ptr_buffer_atomic_add_i32(1, rs, lane < 8 ? lane * 4096 : OOB, OFF_CA + edge * 32768 + (b & 15) * 256, SC1);
  }
}
DEV void signal_grp(rsrc_t rs, int idx, int w) {
  if (w == 0) {
    const int lane = fresh_lane();
    __builtin_amdgcn_raw_ptr_buffer_atomic_add_i32(1, rs, lane == 0 ? 0 : OOB, OFF_CH + idx * 256, SC1);
  }
}

DEV void init_bump(rsrc_t rs) {
  const int lane = fresh_lane();
  __builtin_amdgcn_raw_ptr_buffer_atomic_add_i32(1, rs, lane < 8 ? lane * 256 : OOB, OFF_INIT, SC1);
}
DEV void init_wait(rsrc_t rs, int soff, u32 f0, u32 f) {
  if (__builtin_expect(__any(f == f0), 0)) {
    int spins = SPIN_CAP;
    u32 t;
    asm volatile("s_nop 4\n"
                 "1:\n\t"
                 "buffer_load_dword %0, off, %3, %4 sc1\n\t"
                 "s_sub_u32 %1, %1, 1\n\t"
                 "s_cbranch_scc1 2f\n\t"
                 "s_waitcnt vmcnt(0)\n\t"
                 "v_cmp_eq_u32 vcc, %2, %0\n\t"
                 "s_nop 4\n\t"
                 "s_cbranch_vccnz 1b\n"
                 "2:\n\t"
                 "s_waitcnt vmcnt(0)"
                 : "=&v"(t), "+s"(spins) : "v"(f0), "s"(rs), "s"(soff) : "memory", "vcc", "scc");
  }
  asm volatile("" ::: "memory");
}

template <int P> struct PrepRegs { u32x4 pa[P], pb[P]; u32x2 xv[2]; };
template <int P>
DEV void prep_ld1(rsrc_t rs, int i, int tid, int xso, int sso, PrepRegs<P>& pr) {
  if (i < 2 * P) {
    if (i & 1) pr.pb[i >> 1] = ld16(rs, tid * 32 + 16, sso + (i >> 1) * (L * 8));
    else pr.pa[i >> 1] = ld16(rs, tid * 32, sso + (i >> 1) * (L * 8));
  } else pr.xv[i - 2 * P] = ld8(rs, tid * 8, xso + (i - 2 * P) * (L * 2));
}
template <int P>
DEV void prep_load(rsrc_t rs, int tid, int xso, int sso, PrepRegs<P>& pr) {
#pragma unroll
  for (int i = 0; i < 2 * P + 2; ++i) prep_ld1<P>(rs, i, tid, xso, sso, pr);
  SB();
}
DEV void kill4(u32x4& x) { asm volatile("" : "=v"(x)); }
DEV void kill2(u32x2& x) { asm volatile("" : "=v"(x)); }
template <int P> DEV void prep_kill(PrepRegs<P>& pr) {
#pragma unroll
  for (int p = 0; p < P; ++p) { kill4(pr.pa[p]); kill4(pr.pb[p]); }
  kill2(pr.xv[0]); kill2(pr.xv[1]);
}
template <int P>
DEV void prep_ln(char* smem, int h, int tid, u32 dsk, const PrepRegs<P>& pr, f32x4 (&z)[2]) {
  f32x4 s0 = __builtin_bit_cast(f32x4, pr.pa[0]), s1 = __builtin_bit_cast(f32x4, pr.pb[0]);
#pragma unroll
  for (int p = 1; p < P; ++p) { s0 += __builtin_bit_cast(f32x4, pr.pa[p]); s1 += __builtin_bit_cast(f32x4, pr.pb[p]); }
  const f32x2 t0 = ln_mv(s0.xy), t1 = ln_mv(s0.zw), t2 = ln_mv(s1.xy), t3 = ln_mv(s1.zw);
  const f32x2 rr[2] = {{__builtin_amdgcn_rsqf(t0[1]), __builtin_amdgcn_rsqf(t1[1])}, {__builtin_amdgcn_rsqf(t2[1]), __builtin_amdgcn_rsqf(t3[1])}};
  const f32x2 mr[2] = {{-t0[0] * rr[0][0], -t1[0] * rr[0][1]}, {-t2[0] * rr[1][0], -t3[0] * rr[1][1]}};
#pragma unroll
  for (int c = 0; c < 2; ++c) {
    const float dk = c ? hi_f(dsk) : lo_f(dsk);
    u32x2 u;
    f32x2 zz[2];
#pragma unroll
    for (int i = 0; i < 2; ++i) {
      const f32x2 x = unpk(pr.xv[c][i]);
      const f32x2 uf = __builtin_elementwise_fma(x, rr[i], mr[i]);
      u[i] = pk_bf16(uf);
      zz[i] = __builtin_elementwise_fma(uf, (f32x2){dk, dk}, x);
    }
    z[c] = (f32x4){zz[0][0], zz[0][1], zz[1][0], zz[1][1]};
    *(u32x2*)(smem + LD_U + c * UCH + (256 + h * 1024) * 2 + tid * 8) = u;
  }
}
template <int M0, int I0, int I1> struct ConvPlan {
  static constexpr int Q0 = (M0 - I1) > 0 ? (M0 - I1) : 0, Q1 = M0 + 3 - I0;
  static constexpr int NQ = Q1 - Q0 + 1, NU = I1 - I0 + 1, NF = NQ + NU;
  static constexpr int count() { int n = 0; for (int i = I0; i <= I1; ++i) for (int m = 0; m < 4; ++m) n += (M0 + m - i >= 0); return n; }
  static constexpr int NM = count();
  static constexpr int im(int t) { int n = 0; for (int i = I0; i <= I1; ++i) for (int m = 0; m < 4; ++m) if (M0 + m - i >= 0) { if (n == t) return i * 4 + m; ++n; } return 0; }
  static constexpr int frag(int f) {
    if (f == 0) return 0;
    if (f <= 4) return -(Q1 - 4 + f + 1);
    int g = f - 5, nlow = NQ - 4;
    if (g < 2 * nlow) return (g & 1) ? -(Q1 - 4 - (g >> 1) + 1) : 1 + (g >> 1);
    return 1 + nlow + (g - 2 * nlow);
  }
};
template <int M0, int I0, int I1, bool FIRST, typename FN>
DEV void conv_mfma(char* smem, int w, int lane, f32x4 (&acc)[4], FN&& fill) {
  using P = ConvPlan<M0, I0, I1>;
  const int c16 = lane & 15, kq = lane >> 4;
  const int cch = w >> 1, wr = w & 1;
  const char* tb = smem + cch * KCH + (c16 >> 2) * KCOPY + (16 + 16 * (1 + 8 * wr) - 8 * kq + 4 * (c16 & 3) - 4) * 2;
  const char* ub = smem + LD_U + cch * UCH + (256 - 16 * (1 + 8 * wr + 6) + 16 * c16 + 8 * kq) * 2;
  u32x4 T[2][P::NQ], U[2][P::NU];
  auto ldf = [&](auto rc, auto fc) {
    constexpr int rr = IC(rc), f = P::frag(IC(fc));
    if constexpr (f >= 0) U[rr & 1][f] = *(const u32x4*)(ub + (3 - rr) * 64 + (I0 + f) * 512);
    else T[rr & 1][-f - 1 - P::Q0] = tr_frag(tb, rr * 64 + (-f - 1) * 512 + 8, rr * 64 + (-f - 1) * 512);
  };
  static_for<P::NF>([&](auto fc) { ldf(std::integral_constant<int, 0>{}, fc); });
  SB();
  static_for<4 * P::NM>([&](auto nc) {
    constexpr int n = IC(nc), rr = n / P::NM, t = n % P::NM, i = P::im(t) >> 2, m = P::im(t) & 3, q = M0 + m - i;
    mfma_v<FIRST && rr == 0 && i == I0>(acc[m], T[rr & 1][q - P::Q0], U[rr & 1][i - I0]);
    if constexpr (rr < 3 && t < P::NF) ldf(std::integral_constant<int, rr + 1>{}, std::integral_constant<int, t>{});
    SB();
    fill(nc);
    SB();
  });
}
DEV void conv_out(rsrc_t rs, char* smem, int xso, int w, int lane, f32x4 (&acc)[4], const f32x4 (&z)[2]) {
  const int tid = w * 64 + lane;
  char* yw = smem + LD_Y + w * YW + (lane & 15) * 80 + (lane >> 4) * 16;
  const char* yb = smem + LD_Y + tid * 16 + (tid >> 2) * 16;
  const int vo = ((tid & 1) * L + (tid >> 1) * 8) * 2;
  mfma_done4(acc[0], acc[1], acc[2], acc[3]);
#pragma unroll
  for (int m = 0; m < 4; ++m) *(f32x4*)(yw + m * 1280) = acc[m];
  bar();
  u32x2 o[2];
#pragma unroll
  for (int c = 0; c < 2; ++c) {
    const f32x4 y = (*(const f32x4*)(yb + (2 * c) * YW) + z[c]) + *(const f32x4*)(yb + (2 * c + 1) * YW);
    o[c] = (u32x2){pk_bf16(y[0], y[1]), pk_bf16(y[2], y[3])};
  }
  const bool odd = tid & 1;
  u32 s0 = odd ? o[0][0] : o[1][0], s1 = odd ? o[0][1] : o[1][1];
  u32 g0 = __builtin_amdgcn_update_dpp(0u, s0, 0xB1, 0xf, 0xf, true);
  u32 g1 = __builtin_amdgcn_update_dpp(0u, s1, 0xB1, 0xf, 0xf, true);
  u32x4 v = odd ? (u32x4){g0, g1, o[1][0], o[1][1]} : (u32x4){o[0][0], o[0][1], g0, g1};
  st16(rs, vo, xso, v);
  SB();
}

template <int R, int IMM>
DEV void ld_agpr(rsrc_t rs, int voff, int sbase) {
  int tmp;
  asm volatile("s_add_u32 %0, %3, %c6\n\tbuffer_load_dwordx4 a[%c4:%c5], %1, %2, %0 offen sc1"
               : "=&s"(tmp) : "v"(voff), "s"(rs), "s"(sbase), "i"(R), "i"(R + 3), "i"(IMM) : "memory", "scc");
}
template <int I> DEV void ld_w1(rsrc_t rs, int voff, int so01, int so2) {
  constexpr int ks = I / 3, i = I % 3;
  if constexpr (i < 2) ld_agpr<4 * I, (i * KS1 + ks) * 1024>(rs, voff, so01);
  else ld_agpr<4 * I, ks * 1024>(rs, voff, so2);
}
template <int R, int IMM>
DEV void ld_agpr_imm(rsrc_t rs, int voff, int soff) {
  asm volatile("buffer_load_dwordx4 a[%c3:%c4], %0, %1, %2 offen offset:%c5 sc1"
               :: "v"(voff), "s"(rs), "s"(soff), "i"(R), "i"(R + 3), "i"(IMM) : "memory");
}

constexpr int XS_STEP = 32 * L * 2;
DEV int xs_voff(int w, int lane) {
  const int fsw = ((lane >> 4) & 1) | ((w & 1) << 1);
  return (w * 8 + (lane >> 3)) * (L * 2) + ((((lane & 7) >> 1) ^ fsw) * 32) + (lane & 1) * 16;
}
template <int KS>
DEV void xs_dma(rsrc_t rs, int voff, int sbase, int ldsw) {
  int tmp;
  asm volatile("s_add_u32 m0, %4, %c5\n\ts_add_u32 %0, %3, %c6\n\tbuffer_load_dwordx4 %1, %2, %0 offen sc1 lds"
               : "=&s"(tmp) : "v"(voff), "s"(rs), "s"(sbase), "s"(ldsw), "i"(LD_XT + KS * 4096), "i"(KS * XS_STEP) : "memory", "scc");
}

__global__ __launch_bounds__(256, 1) void main_kernel(
    const u16* __restrict__ x0, const u16* __restrict__ Are, const u16* __restrict__ Aim,
    const u16* __restrict__ Bre, const u16* __restrict__ Bim, const u16* __restrict__ Cre,
    const u16* __restrict__ Cim, const u16* __restrict__ logdt, const u16* __restrict__ dskip,
    const u16* __restrict__ wg, const u16* __restrict__ wu, const u16* __restrict__ wd,
    char* __restrict__ ws, u16* __restrict__ out) {
  extern __shared__ __attribute__((aligned(16))) char smem[];
  const int w_ = __builtin_amdgcn_readfirstlane(threadIdx.x >> 6);
  const int b_ = blockIdx.x;
  const rsrc_t rs = __builtin_amdgcn_make_buffer_rsrc(ws, 0, WS_END, 0x00020000);
  STAMP_DECL
  STAMP();
  asm volatile("" ::: ACLOB);
  u32 cpf;
  {
    unsigned long long pc; int len;
    asm volatile("s_getpc_b64 %0\n.Lmk_code_begin:\n\ts_mov_b32 %1, .Lmk_code_end-.Lmk_code_begin" : "=s"(pc), "=s"(len));
    const int off = min((int)((min(threadIdx.x & 63, 4u) * 4 + (threadIdx.x >> 6)) * 32 + (blockIdx.x >> 3)) * 128, len - 4) & ~3;
    cpf = *(const GAS u32*)(pc + (unsigned long long)(u32)off);
    SB();
    asm volatile("" :: "s"(x0), "s"(Are), "s"(Aim), "s"(Bre), "s"(Bim), "s"(Cre), "s"(Cim), "s"(logdt), "s"(dskip), "s"(wg), "s"(wu), "s"(wd), "s"(ws));
  }
  const int rt_ = ((b_ & 7) >> 1) * 4 + ((b_ >> 3) & 3), cb_ = (b_ & 1) * 8 + (b_ >> 5);
  const int rt2_ = (b_ & 7) * 4 + ((b_ >> 3) & 3), ct_ = b_ >> 5;
  const int start_ = cb_ * 10 + (cb_ < 12 ? cb_ : 12), cnt_ = cb_ < 12 ? 11 : 10;
  const int nbase_ = (w_ < 3) ? w_ * 3 : cnt_ - 2;
  const int nt0_ = start_ + nbase_;
  const int ok2_ = __builtin_amdgcn_readfirstlane((w_ < 3) && (nbase_ + 2 < cnt_ - 2));
#define ROLES \
  const int w = w_, b = b_, rt = rt_, rt2 = rt2_, ct = ct_, nt0 = nt0_, ok2 = ok2_; \
  const int lane = fresh_lane(), tid = w * 64 + lane, c16 = lane & 15, kq = lane >> 4, wvo = lane * 16; \
  const int x1_off = OFF_X1 + layer * ACT2, h_off = OFF_H + layer * HID2; \
  const int hb_e = h_off + (rt2 * 2 * NT1 * 16 + w * 11 * 64) * 16, hb_l = hb_e + 64 * NT1 * 256; \
  const int s10 = (w == 3) ? 9 * 1024 : 10 * 1024; \
  (void)b; (void)rt; (void)ct; (void)nt0; (void)ok2; (void)tid; (void)c16; (void)kq; (void)wvo; (void)x1_off; (void)hb_e; (void)hb_l; (void)s10;

  u32 dsk;
  const int ldsw = (int)(u32)(size_t)(LAS char*)smem + w_ * 1024;
  {
    constexpr int layer = 0;
    ROLES
    const int d0 = 2 * b;
    u32x4 xf[4], xt[2], wr[4];
    const bool initw = (b == 0) && (w == 3);
    const int init_off = OFF_INIT + (b & 7) * 256;
    const u32 f0 = __builtin_amdgcn_raw_buffer_load_b32(rs, 0, init_off, SC1);
    __builtin_amdgcn_raw_buffer_store_b32(0u, rs, initw ? lane * 256 : OOB, OFF_CA + 6 * 32768, SC1);
    {
      const int ln = b * 4 + lane;
      const int vo = initw ? OFF_CA + 6 * 32768 + 16384 + lane * 256 : (w == 1 && lane < 4 && ln < 832) ? (ln < 768 ? ln : ln + 128) * 256 : OOB;
      __builtin_amdgcn_raw_buffer_store_b32(0u, rs, vo, 0, SC1);
    }
    SB();
    {
      const rsrc_t rx = __builtin_amdgcn_make_buffer_rsrc((void*)x0, 0, L * D * 2, 0x00020000);
      const rsrc_t rg = __builtin_amdgcn_make_buffer_rsrc((void*)wg, 0, D * F * 2, 0x00020000);
      const rsrc_t ru = __builtin_amdgcn_make_buffer_rsrc((void*)wu, 0, D * F * 2, 0x00020000);
      const int tvo = (lane >> 1) * (D * 2) + (lane & 1) * 16, tso = (rt2 * 64 * D + ct * 64 + w * 16) * 2;
      xt[0] = __builtin_amdgcn_raw_buffer_load_b128(rx, tvo, tso, 0);
      xt[1] = __builtin_amdgcn_raw_buffer_load_b128(rx, tvo, tso + 32 * D * 2, 0);
      const int gvo = kq * (F * 2) + (c16 < cnt_ ? c16 : 0) * 16, gso = ((rt * 32 + w * 8) * F + start_ * 8) * 2;
      wr[0] = __builtin_amdgcn_raw_buffer_load_b128(rg, gvo, gso, 0);
      wr[1] = __builtin_amdgcn_raw_buffer_load_b128(ru, gvo, gso, 0);
      wr[2] = __builtin_amdgcn_raw_buffer_load_b128(rg, gvo, gso + 4 * F * 2, 0);
      wr[3] = __builtin_amdgcn_raw_buffer_load_b128(ru, gvo, gso + 4 * F * 2, 0);
      const int fvo = (c16 & 7) * (D * 2) + kq * 16, fso = (b * 8 * D + w * 128) * 2;
#pragma unroll
      for (int i = 0; i < 4; ++i) xf[i] = __builtin_amdgcn_raw_buffer_load_b128(rx, fvo + i * 64, fso, 0);
    }
    SB();
    STAMP();
    {
      u32x4 z = splat4(0u);
      if (tid < 16) *(u32x4*)(smem + (tid >> 1) * KCOPY + (tid & 1) * 16) = z;
      if (tid >= 64 && tid < 128) *(u32x4*)(smem + LD_U + (lane >> 5) * UCH + (lane & 31) * 16) = z;
      if (tid == 128) *(u32x4*)(smem + LD_KT + 760 * 16) = z;
    }
    int ix[12];
#pragma unroll
    for (int i = 0; i < 12; i += 2) {
      const int l0 = tid * 8 - 4 + i;
      const u32 bits = pk_bf16((float)l0, (float)(l0 + 1));
      const int i0 = l0 < 256 ? (l0 < 0 ? 676 : l0) : (int)(bits & 0xffffu) - 17024;
      const int i1 = l0 < 256 ? (l0 < 0 ? 676 : l0 + 1) : (int)(bits >> 16) - 17024;
      ix[i] = LD_KT + (i0 + (i0 >> 3)) * 16; ix[i + 1] = LD_KT + (i1 + (i1 >> 3)) * 16;
      asm volatile("" : "+v"(ix[i]), "+v"(ix[i + 1]));
    }
    SB();
    if (initw) { asm volatile("s_waitcnt vmcnt(10)" ::: "memory"); init_bump(rs); }
    const u32 pn = b * (2 * NS) + tid;
    const u32 p_ar = Are[pn], p_ai = Aim[pn], p_br = Bre[pn], p_bi = Bim[pn];
    SB();
    int kx[2][2][4];
#pragma unroll
    for (int c = 0; c < 2; ++c) {
      const int B = 16 * c + c16;
#pragma unroll
      for (int i = 0; i < 4; ++i) {
        const int i0 = kq < 2 ? 8 * B + 4 * kq + i : kq == 2 ? 256 + 4 * B + i : 384 + (i >> 1) * 64 + 2 * B + (i & 1);
        const int i1 = kq == 0 ? 512 + 32 * i + B : (kq == 1 && i == 0) ? 640 + B : 680;
        kx[0][c][i] = LD_KT + (i0 + (i0 >> 3)) * 16 + w * 4; kx[1][c][i] = LD_KT + (i1 + (i1 >> 3)) * 16 + w * 4;
        asm volatile("" : "+v"(kx[0][c][i]), "+v"(kx[1][c][i]));
      }
    }
    SB();
    const u32 p_cr = Cre[pn], p_ci = Cim[pn];
    const u32 p_ld = logdt[(u32)(d0 + (tid >> 7))];
    dsk = ((const u32*)dskip)[(u32)(b + (lane >> 6))];
    const int chunk = b + 256 * (w ^ 2);
    u32x4 w2r[4];
    {
      const rsrc_t rwd = __builtin_amdgcn_make_buffer_rsrc((void*)wd, 0, F * D * 2, 0x00020000);
#pragma unroll
      for (int i = 0; i < 4; ++i)
        w2r[i] = __builtin_amdgcn_raw_buffer_load_b128(rwd, (lane >> 3) * (D * 2) + (lane & 7) * 16, ((chunk >> 3) * 32 * D + (chunk & 7) * 64 + i * 8 * D) * 2, 0);
    }
    SB();
    STAMP();
    {
      char* img = smem + LD_Y + w * YW;
      *(u32x4*)(img + wvo) = xt[0];
      *(u32x4*)(img + 1024 + wvo) = xt[1];
      const char* tp = img + (kq * 8 + (c16 >> 2)) * 32 + (c16 & 3) * 8;
      u32x4 t0 = tr_frag(tp, 0, 128), t1 = tr_frag(tp, 1024, 1024 + 128);
      const int so = OFF_XT + ((ct * 64 + w * 16) * L + rt2 * 64) * 2;
      st16(rs, c16 * (L * 2) + kq * 16, so, t0);
      st16(rs, c16 * (L * 2) + kq * 16 + 64, so, t1);
    }
    constexpr int WROW = 368;
    if (c16 < 11) {
      char* wdst = smem + LD_XT + (w * 8 + kq) * WROW + c16 * 32;
#pragma unroll
      for (int i = 0; i < 2; ++i) {
        const u32x4 g = wr[2 * i], u = wr[2 * i + 1];
        *(u32x4*)(wdst + i * 4 * WROW) = (u32x4){g[0], u[0], g[1], u[1]};
        *(u32x4*)(wdst + i * 4 * WROW + 16) = (u32x4){g[2], u[2], g[3], u[3]};
      }
    }
    {
      const u32x4 ones = splat4(0x3f803f80u);
      f32x4 sa, qa;
      static_for<4>([&](auto ic) { mfma_v<IC(ic) == 0>(sa, ones, xf[IC(ic)]); mfma_v<IC(ic) == 0>(qa, xf[IC(ic)], xf[IC(ic)]); });
      asm volatile("s_nop 7" : "+v"(sa), "+v"(qa));
      if ((c16 >> 2) == kq && c16 < 8) {
        const int r = c16 & 3;
        float q = r == 0 ? qa[0] : r == 1 ? qa[1] : r == 2 ? qa[2] : qa[3];
        *(f32x2*)(smem + LD_SS + (w * 8 + c16) * 8) = (f32x2){sa[0], q};
      }
    }
    bar();
    asm volatile("" :: "v"(cpf));
    if (initw) init_bump(rs);
    const int so01 = OFF_WGU + nt0 * KS1 * 1024, so2 = so01 + (ok2 ? 2 * KS1 * 1024 : 0);
    {
      const char* wsrc = smem + LD_XT + (8 * kq + (c16 >> 2)) * WROW + nbase_ * 32 + (c16 & 3) * 8;
      u32x4 f0 = tr_frag(wsrc, 0, 4 * WROW), f1 = tr_frag(wsrc, 32, 32 + 4 * WROW), f2 = tr_frag(wsrc, 64, 64 + 4 * WROW);
      if (tid < 8) {
        const f32x2* sp = (const f32x2*)(smem + LD_SS) + tid;
        f32x2 v = (sp[0] + sp[8]) + (sp[16] + sp[24]);
        __builtin_amdgcn_raw_buffer_store_b64(__builtin_bit_cast(u32x2, v), rs, tid * 8, OFF_ST0 + b * 64, SC1);
      }
      st16(rs, wvo, so01 + rt * 1024, f0);
      st16(rs, wvo, so01 + (KS1 + rt) * 1024, f1);
      if (ok2) st16(rs, wvo, so2 + rt * 1024, f2);
    }
    SB();
    STAMP();
    PrepRegs<1> pe, pl;
    u32 pv, pv2;
    const u32 f1 = __builtin_amdgcn_raw_buffer_load_b32(rs, 0, init_off, SC1);
    SB();
    {
      constexpr int TROW = 272;
      char* tab = smem + LD_XT + 32768 - 4096 + w * (32 * TROW);
      char* tar = smem + LD_Y + w * YW;
      char* tar2 = smem + LD_W2 + w * 4096;
      const float dt = rbf(expf(lo_f(p_ld)));
      const float ar = dt * lo_f(p_ar), ai = dt * lo_f(p_ai), br = dt * lo_f(p_br), bi = dt * lo_f(p_bi);
      const float cr = lo_f(p_cr), ci = lo_f(p_ci);
      const float I2PI_HI = 0.15915494309189535f;
      const float I2PI_LO = (float)(0.15915494309189535 - (double)0.15915494309189535f);
      const float a2 = ar * 1.4426950408889634f;
      const float rh = ai * I2PI_HI;
      const float rl = __builtin_fmaf(ai, I2PI_LO, __builtin_fmaf(ai, I2PI_HI, -rh));
      auto cexp = [&](float l) {
        const float e = __builtin_amdgcn_exp2f(a2 * l);
        const float fr = __builtin_fmaf(l, rl, __builtin_amdgcn_fractf(rh * l));
        return (f32x2){__builtin_amdgcn_cosf(fr), __builtin_amdgcn_sinf(fr)} * e;
      };
      const float kr = cr * br - ci * bi, ki = cr * bi + ci * br;
      u32 mx = max(__float_as_uint(kr) & 0x7f800000u, __float_as_uint(ki) & 0x7f800000u);
      mx = max(mx, (u32)__builtin_amdgcn_update_dpp(0, (int)mx, 0xB1, 0xf, 0xf, true));
      mx = max(mx, (u32)__builtin_amdgcn_update_dpp(0, (int)mx, 0x4E, 0xf, 0xf, true));
      mx = max(mx, (u32)__builtin_amdgcn_update_dpp(0, (int)mx, 0x124, 0xf, 0xf, true));
      mx = max(mx, (u32)__builtin_amdgcn_update_dpp(0, (int)mx, 0x128, 0xf, 0xf, true));
      mx = max(max((u32)__builtin_amdgcn_readlane((int)mx, 0), (u32)__builtin_amdgcn_readlane((int)mx, 16)),
               max((u32)__builtin_amdgcn_readlane((int)mx, 32), (u32)__builtin_amdgcn_readlane((int)mx, 48)));
      mx = min(max(mx, 60u << 23), 200u << 23);
      const float up = __uint_as_float((267u << 23) - mx);
      const float kscale = __uint_as_float(mx - (27u << 23));
      const f32x2 e1 = cexp(1.f), e8 = cexp(8.f), e128 = cexp(128.f), e256 = cexp(256.f), e1024 = cexp(1024.f);
      const CM c8 = mkcm((f32x2){e8[0], -e8[1]}), c128 = mkcm((f32x2){e128[0], -e128[1]}), m1 = mkcm(e1), m256 = mkcm(e256);
      u32 hv;
      auto putr = [&](int r, u32 v) { *(u32*)((r < 16 ? tar + r * TROW : tar2 + (r - 16) * TROW) + lane * 4) = v; };
      auto putc = [&](int c, u32 v) { *(u32*)(tab + c * TROW + lane * 4) = v; };
      f32x2 p[8];
      p[0] = (f32x2){16384.f, 0.f}; putr(0, pk_f16(p[0]));
      p[1] = e1 * 16384.f; putr(1, pk_f16(p[1]));
#pragma unroll
      for (int i = 2; i < 8; ++i) { p[i] = cmul_h(p[i - 1], m1, hv); putr(i, hv); }
      f32x2 q0 = e256 * 16384.f; putr(8, pk_f16(q0));
      (void)cmul_h(p[2], m256, hv); putr(9, hv);
      f32x2 q4 = cmul_h(p[4], m256, hv); putr(10, hv);
      (void)cmul_h(p[6], m256, hv); putr(11, hv);
      q0 = cmul_h(q0, m256, hv); putr(12, hv);
      q4 = cmul_h(q4, m256, hv); putr(13, hv);
      (void)cmul_h(q0, m256, hv); putr(14, hv);
      (void)cmul_h(q4, m256, hv); putr(15, hv);
      q0 = e1024 * 16384.f; putr(16, pk_f16(q0));
#pragma unroll
      for (int i = 17; i < 21; ++i) { q0 = cmul_h(q0, m256, hv); putr(i, hv); }
      SB();
      STAMP();
      if (initw) init_bump(rs);
      init_wait(rs, init_off, f0, f1);
      drain_bar<0, false>();
      signal_all(rs, 6, b, w);
      SB();
      STAMP();
      f32x2 v0 = (f32x2){kr, -ki} * up; putc(0, pk_f16(v0));
      f32x2 v1 = cmul_h(v0, c128, hv); putc(16, hv);
#pragma unroll
      for (int i = 1; i < 16; ++i) {
        v0 = cmul_h(v0, c8, hv); putc(i, hv);
        v1 = cmul_h(v1, c8, hv); putc(16 + i, hv);
      }
      f32x4 ka[2][2];
      const char* ra0 = tar + c16 * TROW + kq * 16;
      const char* ra1 = tar2 + (c16 < 5 ? c16 : 0) * TROW + kq * 16;
      const char* rb0 = tab + c16 * TROW + kq * 16;
      static_for<4>([&](auto sc) {
        constexpr int s = IC(sc);
        const u32x4 a0 = *(const u32x4*)(ra0 + s * 64), a1 = *(const u32x4*)(ra1 + s * 64);
        const u32x4 b0 = *(const u32x4*)(rb0 + s * 64), b1 = *(const u32x4*)(rb0 + 16 * TROW + s * 64);
        mfma_h<s == 0>(ka[0][0], a0, b0); mfma_h<s == 0>(ka[0][1], a0, b1);
        mfma_h<s == 0>(ka[1][0], a1, b0); mfma_h<s == 0>(ka[1][1], a1, b1);
      });
      pv = poll_ld(rs, all_off(6, b), true);
      mfma_done4(ka[0][0], ka[0][1], ka[1][0], ka[1][1]);
#pragma unroll
      for (int r = 0; r < 2; ++r)
#pragma unroll
        for (int c = 0; c < 2; ++c) {
          const f32x4 v = ka[r][c] * kscale;
#pragma unroll
          for (int i = 0; i < 4; ++i) *(float*)(smem + kx[r][c][i]) = v[i];
        }
    }
    bar();
    STAMP();
    {
      u32 e[2][6];
#pragma unroll
      for (int i = 0; i < 6; ++i) {
        const f32x4 a = *(const f32x4*)(smem + ix[2 * i]), c = *(const f32x4*)(smem + ix[2 * i + 1]);
        e[0][i] = pk_bf16(a[0] + a[1], c[0] + c[1]);
        e[1][i] = pk_bf16(a[2] + a[3], c[2] + c[3]);
      }
      pv2 = poll_ld(rs, all_off(6, b), true);
#pragma unroll
      for (int c = 0; c < 2; ++c) {
        char* kb = smem + c * KCH + tid * 16 + 32;
        u32 o[5];
#pragma unroll
        for (int i = 0; i < 5; ++i) o[i] = __builtin_amdgcn_alignbit(e[c][i + 1], e[c][i], 16);
        *(u32x4*)(kb) = (u32x4){e[c][2], e[c][3], e[c][4], e[c][5]};
        *(u32x4*)(kb + KCOPY) = (u32x4){o[1], o[2], o[3], o[4]};
        *(u32x4*)(kb + 2 * KCOPY) = (u32x4){e[c][1], e[c][2], e[c][3], e[c][4]};
        *(u32x4*)(kb + 3 * KCOPY) = (u32x4){o[0], o[1], o[2], o[3]};
      }
    }
    SB();
    if (chunk < KS2P * 8) {
      char* img = smem + LD_W2 + 16384 + w * 4096;
#pragma unroll
      for (int i = 0; i < 4; ++i)
        *(u32x4*)(img + i * 1024 + (lane >> 3) * 128 + ((((lane & 7) >> 1) ^ (((lane >> 4) & 1) | ((i & 1) << 1))) * 32) + (lane & 1) * 16) = w2r[i];
      const int fr = ((c16 >> 3) & 1) | ((kq & 1) << 1);
#pragma unroll
      for (int i = 0; i < 4; ++i)
        st16(rs, wvo, OFF_WD + (((chunk & 7) * 4 + i) * KS2P + (chunk >> 3)) * 1024,
             tr_frag(img + (8 * kq + (c16 >> 2)) * 128 + ((i ^ fr) * 32) + (c16 & 3) * 8, 0, 512));
    }
    SB();
    STAMP();
    if (initw) {
#pragma unroll 1
      for (int spins = 0; spins < SPIN_CAP && !__all(pv2 >= 16u); ++spins) { init_bump(rs); pv2 = poll_ld(rs, all_off(6, b), true); }
    }
    poll_wait2(rs, all_off(6, b), true, pv, pv2);
    STAMP();
    prep_load<1>(rs, tid, OFF_XT + d0 * L * 2, OFF_ST0, pe);
    prep_load<1>(rs, tid, OFF_XT + d0 * L * 2 + 2048, OFF_ST0 + 8192, pl);
    f32x4 ze[2], zl[2];
    prep_ln<1>(smem, 0, tid, dsk, pe, ze);
    asm volatile("" : "+v"(pl.pa[0]), "+v"(pl.pb[0]), "+v"(pl.xv[0]), "+v"(pl.xv[1]));
    SB();
    static_for<10>([&](auto ic) { ld_w1<IC(ic)>(rs, wvo, so01, so2); });
    bar();
    STAMP();
    f32x4 acc[4];
    conv_mfma<0, 0, 3, true>(smem, w, lane, acc, [&](auto nc) {
      constexpr int n = IC(nc);
      if constexpr (n % 4 == 3) ld_w1<10 + n / 4>(rs, wvo, so01, so2);
    });
    prep_ln<1>(smem, 1, tid, dsk, pl, zl);
    conv_out(rs, smem, OFF_X1 + d0 * L * 2, w, lane, acc, ze);
    STAMP();
    conv_mfma<4, 0, 7, true>(smem, w, lane, acc, [&](auto nc) {
      constexpr int n = IC(nc);
      if constexpr (n < 40 && n % 5 == 0) ld_w1<20 + n / 5>(rs, wvo, so01, so2);
      if constexpr (n == 40) { drain_bar<8, false>(); signal_all(rs, 0, b, w); }
      if constexpr (n >= 42 && n < 102 && n % 3 == 0) ld_w1<28 + (n - 42) / 3>(rs, wvo, so01, so2);
    });
    STAMP();
    pv = poll_ld(rs, all_off(0, b), true);
    conv_out(rs, smem, OFF_X1 + d0 * L * 2 + 2048, w, lane, acc, zl);
    pv2 = poll_ld(rs, all_off(0, b), true);
    STAMP();
    SB();
    poll_wait2(rs, all_off(0, b), true, pv, pv2);
    STAMP();
    {
      const int gx = xs_voff(w, lane), gs = OFF_X1 + rt * 128;
      const u32x4 nones = splat4(0xbf80bf80u);
      f32x4 c[3];
      static_for<48>([&](auto ic) {
        mfma_aA<4 * IC(ic), (IC(ic) < 3)>(c[IC(ic) % 3], nones); SB();
        if constexpr (IC(ic) < 16 && !(IC(ic) & 1)) { xs_dma<IC(ic) / 2>(rs, gx, gs, ldsw); SB(); }
      });
      asm volatile("s_nop 7" : "+v"(c[0]), "+v"(c[1]), "+v"(c[2]));
#pragma unroll
      for (int i = 0; i < 3; ++i) *(f32x4*)(smem + LD_CS + w * 192 + i * 64 + kq * 16) = c[i];
    }
    drain_bar<0, true>();
    signal_all(rs, 1, b, w);
    STAMP();
  }
  static_for<2>([&](auto lc) {
    constexpr int layer = IC(lc);
    PrepRegs<8> pr;
    u32x4 W2[5][4];
    u32x4 hf[11][2];
    {
      ROLES
      int sw2[4];
#pragma unroll
      for (int i = 0; i < 4; ++i) sw2[i] = OFF_WD + ((ct * 4 + ((w + i) & 3)) * KS2P + w * 11) * 1024;
      const int fr = ((c16 >> 3) & 1) | ((kq & 1) << 1);
      const char* xr[4];
#pragma unroll
      for (int j = 0; j < 4; ++j) xr[j] = smem + LD_XT + (8 * kq + (c16 >> 2)) * 128 + ((((w + j) & 3) ^ fr) * 32) + (c16 & 3) * 8;
      const int gx = xs_voff(w, lane), gs = x1_off + rt * 128;
      u32x4 xs[8];
      char* ssw = smem + ((c16 >> 2) == kq ? LD_SS + w * 128 : LD_Y) + c16 * 8;
      const u32x4 ones = splat4(0x3f803f80u);
      u32x2 xb[2][4][2];
#pragma unroll
      for (int j = 0; j < 4; ++j) { xb[0][j][0] = tr_half(xr[j]); xb[0][j][1] = tr_half(xr[j] + 512); }
      SB();
      static_for<2>([&](auto hc) {
        constexpr int h = IC(hc);
        f32x4 ga[3][4], sa, qa;
        const int nxt_poll = h == 0 ? all_off(layer * 4 + 1, b) : grp_off(layer * 32 + (rt2 >> 1));
        u32 pv, pv2;
        {
          auto fill = [&](auto gc) {
            constexpr int g = IC(gc);
            if constexpr (g < 8) xs_dma<8 + g>(rs, gx, gs + h * 2048, ldsw);
            if constexpr (g == (h == 0 ? SIG_CL : SIG_HE) && (h == 1 || layer == 1)) {
              drain_bar<8, false>();
              if constexpr (h == 1) signal_grp(rs, layer * 32 + rt, w); else signal_all(rs, layer * 4 + 1, b, w);
            }
            if constexpr (g == 27) drain_bar<(h == 1 && layer == 0) ? 12 : 0, true>();
            if constexpr (h == 1) {
              if constexpr (layer == 0) {
                if constexpr (g >= 15 && g < 27) ld_agpr_imm<192 + 16 * ((g - 15) & 3) + 4 * ((g - 15) >> 2), ((g - 15) & 3) * 1024>(rs, wvo, sw2[(g - 15) >> 2]);
                if constexpr (g >= 28 && g < 32) ld_agpr_imm<192 + 16 * (g - 28) + 12, (g - 28) * 1024>(rs, wvo, sw2[3]);
                if constexpr (g >= 32 && g < 40) xs[g - 32] = ld16(rs, wvo + ((g - 32) >> 2) * 1024, sw2[(g - 32) & 3] + 4096);
                if constexpr (g >= 52 && g < 60) *(u32x4*)(smem + LD_W2 + w * 8192 + (g - 52) * 1024 + wvo) = xs[g - 52];
              }
              if constexpr (g >= 28 && g < 48) {
                constexpr int i = (g - 28) / 5, s = (g - 28) % 5;
                W2[s][i] = ld16(rs, wvo + (s & 3) * 1024, sw2[i] + (6 + (s >> 2) * 4) * 1024);
              }
            }
            if constexpr (g == POLL_G - 12) pv = poll_ld(rs, nxt_poll, h == 0);
            if constexpr (g == POLL_G - 5) pv2 = poll_ld(rs, nxt_poll, h == 0);
            if constexpr (g == POLL_G) poll_wait2(rs, nxt_poll, h == 0, pv, pv2);
            if constexpr (g >= POLL_G && g < POLL_G + 8) {
              if constexpr (h == 0) xs_dma<g - POLL_G>(rs, gx, gs + 2048, ldsw);
              else hf[g - POLL_G][0] = ld16(rs, wvo + ((g - POLL_G) & 3) * 1024, hb_e + ((g - POLL_G) >> 2) * 4096);
            }
          };
          static_for<60>([&](auto gc) {
            constexpr int g = IC(gc), ks = g >> 2, j = g & 3;
            const u32x4 x = {xb[ks & 1][j][0][0], xb[ks & 1][j][0][1], xb[ks & 1][j][1][0], xb[ks & 1][j][1][1]};
            mfma_aA<12 * ks, ks == 0>(ga[0][j], x);
            xb[(ks + 1) & 1][j][0] = tr_half(xr[j] + (ks + 1) * 4096);
            SB();
            mfma_aA<12 * ks + 4, ks == 0>(ga[1][j], x);
            xb[(ks + 1) & 1][j][1] = tr_half(xr[j] + (ks + 1) * 4096 + 512);
            SB();
            mfma_aA<12 * ks + 8, ks == 0>(ga[2][j], x);
            SB();
            fill(gc);
            SB();
            if constexpr (j == 0) { mfma_v<ks == 0>(sa, ones, x); SB(); mfma_v<ks == 0>(qa, x, x); SB(); }
          });
          u32x4 x[4];
#pragma unroll
          for (int j = 0; j < 4; ++j) x[j] = (u32x4){xb[1][j][0][0], xb[1][j][0][1], xb[1][j][1][0], xb[1][j][1][1]};
          mfma_vv(sa, ones, x[0]); SB();
          mfma_vv(qa, x[0], x[0]); SB();
#pragma unroll
          for (int j = 0; j < 4; ++j) { mfma_aA<180, false>(ga[0][j], x[j]); SB(); }
#pragma unroll
          for (int j = 0; j < 4; ++j) { mfma_aA<184, false>(ga[1][j], x[j]); SB(); }
          asm volatile("" : "+v"(sa), "+v"(qa));
          {
            const int r = c16 & 3;
            const float q01 = (r & 1) ? qa[1] : qa[0];
            SB();
            mfma_aA<188, false>(ga[2][0], x[0]); SB();
            const float q23 = (r & 1) ? qa[3] : qa[2];
            const float q = (r & 2) ? q23 : q01;
            SB();
            mfma_aA<188, false>(ga[2][1], x[1]); SB();
            *(f32x2*)ssw = (f32x2){sa[0], q};
            SB();
          }
          mfma_aA<188, false>(ga[2][2], x[2]); SB();
          mfma_aA<188, false>(ga[2][3], x[3]); SB();
        }
        STAMP();
        f32x4 cs[3];
#pragma unroll
        for (int i = 0; i < 3; ++i) cs[i] = *(const f32x4*)(smem + LD_CS + w * 192 + i * 64 + kq * 16);
        bar();
        f32x2 st[4];
#pragma unroll
        for (int j = 0; j < 4; ++j) st[j] = *(const f32x2*)(smem + LD_SS + (((w + j) & 3) * 16 + c16) * 8);
        tie4(ga[0][0], ga[0][1], ga[0][2], ga[0][3]);
        tie4(ga[1][0], ga[1][1], ga[1][2], ga[1][3]);
        tie4(ga[2][0], ga[2][1], ga[2][2], ga[2][3]);
        SB();
        u32 hd[3][4];
#pragma unroll
        for (int j = 0; j < 4; ++j) {
          const f32x2 mv = ln_mv(st[j]);
          const float nl = __builtin_amdgcn_rsqf(mv[1]) * -1.4426950408889634f;
#pragma unroll
          for (int i = 0; i < 3; ++i) {
            const f32x4 v = __builtin_elementwise_fma(cs[i], (f32x4){mv[0], mv[0], mv[0], mv[0]}, ga[i][j]);
            const f32x2 g = {v[0], v[1]}, u = {v[2], v[3]};
            const f32x2 a = g * nl;
            const f32x2 e = {__builtin_amdgcn_exp2f(a[0]), __builtin_amdgcn_exp2f(a[1])};
            const f32x2 d = __builtin_elementwise_fma(e, (f32x2){mv[1], mv[1]}, (f32x2){mv[1], mv[1]});
            const f32x2 r = {__builtin_amdgcn_rcpf(d[0]), __builtin_amdgcn_rcpf(d[1])};
            hd[i][j] = pk_bf16((g * u) * r);
            if constexpr (h == 1) {
              const int t = j * 3 + i;
              SB();
              if (t < 3) hf[8 + t][0] = ld16(rs, wvo, hb_e + (t == 2 ? s10 : (8 + t) * 1024));
              else if (t < 8) hf[t - 3][1] = ld16(rs, wvo + ((t - 3) & 3) * 1024, hb_e + NT1 * 256 + ((t - 3) >> 2) * 4096);
              SB();
            }
          }
        }
        const int hb = (((w + kq) & 3) * NT1 * 16 + c16) * 16, hs = h_off + ((h * 64 + rt * 4) * NT1 + nt0) * 256;
#pragma unroll
        for (int i = 0; i < 3; ++i) {
          auto a02 = __builtin_amdgcn_permlane32_swap(hd[i][0], hd[i][2], false, false);
          auto a13 = __builtin_amdgcn_permlane32_swap(hd[i][1], hd[i][3], false, false);
          auto o01 = __builtin_amdgcn_permlane16_swap(a02[0], a13[0], false, false);
          auto o23 = __builtin_amdgcn_permlane16_swap(a02[1], a13[1], false, false);
          u32x4 v = {o01[0], o01[1], o23[0], o23[1]};
          st16(rs, (i < 2 || ok2) ? hb + i * 256 : OOB, hs, v);
        }
        SB();
        if constexpr (h == 0) {
          drain_bar<3, false>();
#pragma unroll
          for (int j = 0; j < 4; ++j) { xb[0][j][0] = tr_half(xr[j]); xb[0][j][1] = tr_half(xr[j] + 512); }
        }
        SB();
        STAMP();
      });
    }
    {
      ROLES
      char* rw[3];
#pragma unroll
      for (int i = 0; i < 3; ++i) rw[i] = smem + LD_XT + (((w + 1 + i) & 3) * 3 + i) * 2048 + wvo;
      const char* rb = smem + LD_XT + w * 6144 + wvo;
      const int rvo = c16 * (L * 2) + kq * 8;
      const int ovo = c16 * (L * 2) + (kq & 1) * 32 + (kq >> 1) * 16;
      char* ow = smem + LD_OT + (w * 16 + c16) * 64 + (kq & 1) * 32 + (kq >> 1) * 16;
      const char* ob = smem + LD_OT + (8 * kq + (c16 >> 2)) * 64 + (w & 1) * 32 + (c16 & 3) * 8;
      const char* wlp = smem + LD_W2 + w * 8192 + wvo;
      static_for<2>([&](auto hc) {
        constexpr int h = IC(hc);
        const int tok0 = h * 1024 + rt2 * 32;
        const int eso = ((ct * 64 + w * 16) * L + tok0) * 2;
        const int hb = h ? hb_l : hb_e;
        f32x4 da[4][2];
        u32x4 wl[2][4];
        u32x2 res[2];
        u32 pv, pv2;
        static_for<88>([&](auto nc) {
          constexpr int n = IC(nc), j = n / 44, s = (n % 44) >> 2, i = (n + 1) & 3;
          if constexpr (s < 4) mfma_aB<192 + 16 * s + 4 * i, s == 0>(da[i][j], hf[s][j]);
          else if constexpr (s < 6) mfma_vv(da[i][j], hf[s][j], wl[s - 4][i]);
          else mfma_vv(da[i][j], hf[s][j], W2[s - 6][i]);
          SB();
          if constexpr (n < 24 && (n & 3) == 3)
            hf[5 + (n >> 2)][1] = ld16(rs, wvo, hb + NT1 * 256 + ((n >> 2) == 5 ? s10 : (5 + (n >> 2)) * 1024));
          if constexpr (n % 44 >= 6 && n % 44 < 14) wl[(n % 44 - 6) >> 2][(n % 44 - 5) & 3] = *(const u32x4*)(wlp + (((n % 44 - 6) & 4) + ((n % 44 - 5) & 3)) * 1024);
          if constexpr (n == 25) res[0] = ld8(rs, rvo, x1_off + eso);
          if constexpr (n == 26) res[1] = ld8(rs, rvo + 32, x1_off + eso);
          if constexpr (h == 0 && n == SIG_HL) { drain_bar<8, false>(); signal_grp(rs, layer * 32 + 16 + rt, w); }
          if constexpr (n == 52 || n == 56 || n == 60) { asm volatile("" : "+v"(da[(n - 48) >> 2][0])); *(f32x4*)(rw[(n - 52) >> 2]) = da[(n - 48) >> 2][0]; }
          if constexpr (h == 1 && layer == 0 && n == 80) pv = poll_ld(rs, all_off(2, b), true);
          SB();
        });
        mfma_done4(da[0][1], da[1][1], da[2][1], da[3][1]);
        SB();
        STAMP();
#pragma unroll
        for (int i = 0; i < 3; ++i) *(f32x4*)(rw[i] + 1024) = da[i + 1][1];
        bar();
        u32x2 xo[2];
#pragma unroll
        for (int j = 0; j < 2; ++j) {
          const f32x4 rf = {lo_f(res[j][0]), hi_f(res[j][0]), lo_f(res[j][1]), hi_f(res[j][1])};
          const f32x4 v = ((da[0][j] + rf) + *(const f32x4*)(rb + j * 1024)) + (*(const f32x4*)(rb + 2048 + j * 1024) + *(const f32x4*)(rb + 4096 + j * 1024));
          xo[j] = (u32x2){pk_bf16(v[0], v[1]), pk_bf16(v[2], v[3])};
        }
        auto s0 = __builtin_amdgcn_permlane16_swap(xo[0][0], xo[1][0], false, false);
        auto s1 = __builtin_amdgcn_permlane16_swap(xo[0][1], xo[1][1], false, false);
        const u32x4 xv = {s0[0], s1[0], s0[1], s1[1]};
        *(u32x4*)ow = xv;
        if constexpr (layer == 0) st16(rs, ovo, OFF_XT + ACT2 + eso, xv);
        if constexpr (h == 0) pv = poll_ld(rs, grp_off(layer * 32 + 16 + (rt2 >> 1)), false);
        else if constexpr (layer == 0) pv2 = poll_ld(rs, all_off(2, b), true);
        bar();
        if constexpr (layer == 0) {
          if (w < 2) {
            u32x4 f0 = tr_frag(ob, 0, 256), f1 = tr_frag(ob, 2048, 2048 + 256);
            const u32x4 ones = splat4(0x3f803f80u);
            f32x4 sa, qa;
            mfma_v0(sa, ones, f0); mfma_v0(qa, f0, f0); mfma_vv(sa, ones, f1); mfma_vv(qa, f1, f1);
            asm volatile("s_nop 7" : "+v"(sa), "+v"(qa));
            const int r = c16 & 3;
            float q = r == 0 ? qa[0] : r == 1 ? qa[1] : r == 2 ? qa[2] : qa[3];
            u32x2 v = {__float_as_uint(sa[0]), __float_as_uint(q)};
            __builtin_amdgcn_raw_buffer_store_b64(v, rs, (c16 >> 2) == kq ? c16 * 8 : OOB, OFF_ST1 + (ct * L + tok0 + w * 16) * 8, SC1);
          }
        } else {
          u32x4 f = tr_frag(ob, (w >> 1) * 2048, (w >> 1) * 2048 + 256);
          const rsrc_t ro = __builtin_amdgcn_make_buffer_rsrc(out, 0, L * D * 2, 0x00020000);
          __builtin_amdgcn_raw_buffer_store_b128(f, ro, (c16 * D + kq * 8) * 2, ((tok0 + (w & 1) * 16) * D + ct * 64 + (w >> 1) * 32) * 2, 0);
        }
        SB();
        STAMP();
        if constexpr (h == 0) {
          poll_wait(rs, grp_off(layer * 32 + 16 + (rt2 >> 1)), false, pv);
          STAMP();
#pragma unroll
          for (int s = 0; s < 11; ++s) hf[s][0] = ld16(rs, wvo + (s < 8 ? (s & 3) * 1024 : 0), hb_l + (s < 8 ? (s >> 2) * 4096 : s == 10 ? s10 : s * 1024));
          SB();
          if constexpr (layer == 0) { drain_bar<11, false>(); signal_all(rs, 2, b, w); }
          SB();
#pragma unroll
          for (int s = 0; s < 5; ++s) hf[s][1] = ld16(rs, wvo + (s & 3) * 1024, hb_l + NT1 * 256 + (s >> 2) * 4096);
          SB();
        } else {
          if constexpr (layer == 0) {
            poll_wait2(rs, all_off(2, b), true, pv, pv2);
            STAMP();
            prep_load<8>(rs, tid, OFF_XT + ACT2 + b * 2 * L * 2, OFF_ST1, pr);
            drain_bar<18, false>();
            signal_all(rs, 3, b, w);
          }
        }
      });
    }
    if constexpr (layer == 0) {
      ROLES
      const int xo = b * 2 * L * 2;
      f32x4 z[2], acc[4];
      u32 pv;
      prep_ln<8>(smem, 0, tid, dsk, pr, z);
      bar();
      STAMP();
      conv_mfma<0, 0, 3, true>(smem, w, lane, acc, [&](auto) {});
      conv_out(rs, smem, OFF_X1 + ACT2 + xo, w, lane, acc, z);
      STAMP();
      conv_mfma<4, 0, 3, true>(smem, w, lane, acc, [&](auto nc) {
        constexpr int n = IC(nc);
        if constexpr (n == 2) pv = poll_ld(rs, all_off(3, b), true);
        if constexpr (n == 26) { drain_bar<0, false>(); signal_all(rs, 4, b, w); poll_wait(rs, all_off(3, b), true, pv); }
        if constexpr (n >= 27 && n < 63 && (n & 1)) prep_ld1<8>(rs, (n - 27) >> 1, tid, OFF_XT + ACT2 + xo + 2048, OFF_ST1 + 8192, pr);
      });
      STAMP();
      prep_ln<8>(smem, 1, tid, dsk, pr, z);
      bar();
      STAMP();
      conv_mfma<4, 4, 7, false>(smem, w, lane, acc, [&](auto nc) { if constexpr (IC(nc) == 12) pv = poll_ld(rs, all_off(4, b), true); });
      STAMP();
      poll_wait(rs, all_off(4, b), true, pv);
      STAMP();
      const int gx = xs_voff(w, lane), gs = OFF_X1 + ACT2 + rt * 128;
      static_for<8>([&](auto ic) { xs_dma<IC(ic)>(rs, gx, gs, ldsw); });
      SB();
      conv_out(rs, smem, OFF_X1 + ACT2 + xo + 2048, w, lane, acc, z);
      drain_bar<1, false>();
      STAMP();
    }
  });
  STAMP_OUT();
  asm volatile(".Lmk_code_end:" ::: "memory");
}

extern "C" void kernel_launch(void* const* d_in, const int* in_sizes, int n_in, void* d_out, int out_size,
                              void* d_ws, size_t ws_size, hipStream_t stream) {
  char* ws = (char*)d_ws;
  (void)hipFuncSetAttribute((const void*)main_kernel, hipFuncAttributeMaxDynamicSharedMemorySize, LDS_BYTES + LDS_EXTRA);
  main_kernel<<<256, 256, LDS_BYTES + LDS_EXTRA, stream>>>(
      (const u16*)d_in[0], (const u16*)d_in[1], (const u16*)d_in[2], (const u16*)d_in[3], (const u16*)d_in[4],
      (const u16*)d_in[5], (const u16*)d_in[6], (const u16*)d_in[7], (const u16*)d_in[8], (const u16*)d_in[10],
      (const u16*)d_in[11], (const u16*)d_in[12], ws, (u16*)d_out);
}
```

```cpp
#include <hip/hip_runtime.h>
#include <stdint.h>
#include <type_traits>

typedef unsigned short u16;
typedef unsigned int u32;
typedef u32 u32x4 __attribute__((ext_vector_type(4)));
typedef u32 u32x2 __attribute__((ext_vector_type(2)));
typedef float f32x4 __attribute__((ext_vector_type(4)));
typedef float f32x2 __attribute__((ext_vector_type(2)));
typedef short s16x4 __attribute__((ext_vector_type(4)));
typedef __bf16 bf16x2 __attribute__((ext_vector_type(2)));
typedef _Float16 f16x2 __attribute__((ext_vector_type(2)));
#define LAS __attribute__((address_space(3)))
#define GAS __attribute__((address_space(1)))
#define DEV __device__ __forceinline__
#define SB() __builtin_amdgcn_sched_barrier(0)
#define IC(x) decltype(x)::value
typedef __amdgpu_buffer_rsrc_t rsrc_t;

constexpr int L = 2048, D = 512, NS = 128, F = 1376;
constexpr int NT1 = 172;
constexpr int KS1 = 16;
constexpr int KS2P = 44;
constexpr float EPS = 1e-5f;
constexpr int SC1 = 16;
constexpr int SIG_CL = 9, SIG_HE = 12, SIG_HL = 44, SIG_XE = 30, POLL_G = 52;

constexpr int ACT = D * L * 2;
constexpr int HID = F * L * 2;
constexpr int N_ALL = 7;
constexpr int OFF_CA  = 0;
constexpr int OFF_CH  = OFF_CA + N_ALL * 128 * 256;
constexpr int OFF_INIT = OFF_CH + 64 * 256;
constexpr int CTRL_BYTES = OFF_INIT + 8 * 256;
constexpr int OFF_XT  = CTRL_BYTES;
constexpr int ACT2 = ACT, HID2 = HID;
constexpr int OFF_X1  = OFF_XT + ACT + ACT2;
constexpr int OFF_H   = OFF_X1 + ACT + ACT2;
constexpr int OFF_WGU = OFF_H + HID + HID2;
constexpr int OFF_WD  = OFF_WGU + (NT1 + 1) * KS1 * 1024;
constexpr int OFF_ST0 = OFF_WD + 32 * KS2P * 1024;
constexpr int OFF_ST1 = OFF_ST0 + L * 8;
constexpr int OFF_AC  = OFF_ST1 + 8 * L * 8;
constexpr int WS_END  = OFF_AC + L * 16;

#define STAMP_DECL
#define STAMP()
#define STAMP_OUT()
#define LDS_EXTRA 0

DEV float lo_f(u32 v) { return __uint_as_float(v << 16); }
DEV float hi_f(u32 v) { return __uint_as_float(v & 0xffff0000u); }
DEV u32 pk_bf16(float a, float b) {
  f32x2 v = {a, b};
  return __builtin_bit_cast(u32, __builtin_convertvector(v, bf16x2));
}
DEV u32 pk_bf16(f32x2 v) { return __builtin_bit_cast(u32, __builtin_convertvector(v, bf16x2)); }
DEV u32 pk_f16(f32x2 v) { return __builtin_bit_cast(u32, __builtin_convertvector(v, f16x2)); }
DEV f32x2 unpk(u32 v) { return (f32x2){lo_f(v), hi_f(v)}; }
DEV float rbf(float a) { return lo_f(pk_bf16(a, a)); }
DEV f32x2 ln_mv(f32x2 sq) {
  f32x2 t = __builtin_elementwise_fma(sq, (f32x2){1.0f / D, 1.0f / D}, (f32x2){0.f, EPS});
  t[1] = fmaxf(__builtin_fmaf(-t[0], t[0], t[1]), EPS);
  return t;
}
struct CM { f32x2 re, im; };
DEV CM mkcm(f32x2 c) { return CM{(f32x2){c[0], c[0]}, (f32x2){-c[1], c[1]}}; }
DEV f32x2 cmul_h(f32x2 x, const CM& c, u32& h) {
  const f32x2 r = __builtin_elementwise_fma(x.yx, c.im, x * c.re);
  h = pk_f16(r);
  return r;
}

constexpr int KCOPY = 4160, KCH = 4 * KCOPY;
constexpr int LD_U = 2 * KCH;
constexpr int UCH = (256 + L) * 2;
constexpr int LD_XT = LD_U + 2 * UCH;
constexpr int LD_Y = LD_XT + 65536;
constexpr int YW = 5120;
constexpr int LD_SS = LD_Y + 4 * YW;
constexpr int LD_OT = LD_Y;
constexpr int LD_CS = LD_SS + 512;
constexpr int LD_W2 = LD_CS + 768;
constexpr int LDS_BYTES = LD_W2 + 32768;
static_assert(LDS_BYTES <= 163840, "LDS");
constexpr int LD_KT = LD_XT + 16384;
constexpr int RROW = 144, RW = 64 * RROW;

#define ACLOB "a0","a1","a2","a3","a4","a5","a6","a7","a8","a9","a10","a11","a12","a13","a14","a15","a16","a17","a18","a19","a20","a21","a22","a23","a24","a25","a26","a27","a28","a29","a30","a31","a32","a33","a34","a35","a36","a37","a38","a39","a40","a41","a42","a43","a44","a45","a46","a47","a48","a49","a50","a51","a52","a53","a54","a55","a56","a57","a58","a59","a60","a61","a62","a63","a64","a65","a66","a67","a68","a69","a70","a71","a72","a73","a74","a75","a76","a77","a78","a79","a80","a81","a82","a83","a84","a85","a86","a87","a88","a89","a90","a91","a92","a93","a94","a95","a96","a97","a98","a99","a100","a101","a102","a103","a104","a105","a106","a107","a108","a109","a110","a111","a112","a113","a114","a115","a116","a117","a118","a119","a120","a121","a122","a123","a124","a125","a126","a127","a128","a129","a130","a131","a132","a133","a134","a135","a136","a137","a138","a139","a140","a141","a142","a143","a144","a145","a146","a147","a148","a149","a150","a151","a152","a153","a154","a155","a156","a157","a158","a159","a160","a161","a162","a163","a164","a165","a166","a167","a168","a169","a170","a171","a172","a173","a174","a175","a176","a177","a178","a179","a180","a181","a182","a183","a184","a185","a186","a187","a188","a189","a190","a191","a192","a193","a194","a195","a196","a197","a198","a199","a200","a201","a202","a203","a204","a205","a206","a207","a208","a209","a210","a211","a212","a213","a214","a215","a216","a217","a218","a219","a220","a221","a222","a223","a224","a225","a226","a227","a228","a229","a230","a231","a232","a233","a234","a235","a236","a237","a238","a239","a240","a241","a242","a243","a244","a245","a246","a247","a248","a249","a250","a251","a252","a253","a254","a255"
#define MF "v_mfma_f32_16x16x32_bf16 "
DEV void mfma_vv(f32x4& c, u32x4 a, u32x4 b) { asm(MF "%0, %1, %2, %0" : "+v"(c) : "v"(a), "v"(b)); }
DEV void mfma_v0(f32x4& c, u32x4 a, u32x4 b) { asm(MF "%0, %1, %2, 0" : "=&v"(c) : "v"(a), "v"(b)); }
template <bool FIRST> DEV void mfma_v(f32x4& c, u32x4 a, u32x4 b) { if constexpr (FIRST) mfma_v0(c, a, b); else mfma_vv(c, a, b); }
template <int R, bool FIRST> DEV void mfma_aA(f32x4& c, u32x4 b) {
  if constexpr (FIRST) asm(MF "%0, a[%c2:%c3], %1, 0" : "=&v"(c) : "v"(b), "i"(R), "i"(R + 3));
  else asm(MF "%0, a[%c2:%c3], %1, %0" : "+v"(c) : "v"(b), "i"(R), "i"(R + 3));
}
template <int R, bool FIRST> DEV void mfma_aB(f32x4& c, u32x4 a) {
  if constexpr (FIRST) asm(MF "%0, %1, a[%c2:%c3], 0" : "=&v"(c) : "v"(a), "i"(R), "i"(R + 3));
  else asm(MF "%0, %1, a[%c2:%c3], %0" : "+v"(c) : "v"(a), "i"(R), "i"(R + 3));
}
template <bool FIRST> DEV void mfma_h(f32x4& c, u32x4 a, u32x4 b) {
  if constexpr (FIRST) asm("s_nop 1\n\tv_mfma_f32_16x16x32_f16 %0, %1, %2, 0" : "=&v"(c) : "v"(a), "v"(b));
  else asm("s_nop 1\n\tv_mfma_f32_16x16x32_f16 %0, %1, %2, %0" : "+v"(c) : "v"(a), "v"(b));
}
DEV void mfma_done4(f32x4& a, f32x4& b, f32x4& c, f32x4& d) { asm volatile("s_nop 7" : "+v"(a), "+v"(b), "+v"(c), "+v"(d)); }
DEV void tie4(f32x4& a, f32x4& b, f32x4& c, f32x4& d) { asm volatile("" : "+v"(a), "+v"(b), "+v"(c), "+v"(d)); }
DEV void bar() { asm volatile("s_waitcnt lgkmcnt(0)\n\ts_barrier" ::: "memory", ACLOB); }

DEV u32x4 ld16(rsrc_t rs, int voff, int soff) { return __builtin_amdgcn_raw_buffer_load_b128(rs, voff, soff, SC1); }
DEV u32x2 ld8(rsrc_t rs, int voff, int soff) { return __builtin_amdgcn_raw_buffer_load_b64(rs, voff, soff, SC1); }
DEV void st16(rsrc_t rs, int voff, int soff, u32x4 v) { __builtin_amdgcn_raw_buffer_store_b128(v, rs, voff, soff, SC1); }
DEV int fresh_lane() {
  int l;
  asm volatile("v_mbcnt_lo_u32_b32 %0, -1, 0\n\tv_mbcnt_hi_u32_b32 %0, -1, %0" : "=v"(l));
  return l;
}
DEV u32x4 splat4(u32 c) {
  u32x4 r;
  asm volatile("v_mov_b32 %0, %4\n\tv_mov_b32 %1, %4\n\tv_mov_b32 %2, %4\n\tv_mov_b32 %3, %4"
               : "=v"(r[0]), "=v"(r[1]), "=v"(r[2]), "=v"(r[3]) : "i"(c));
  asm volatile("s_nop 1" : "+v"(r));
  return r;
}
DEV int opq(int x) { asm volatile("" : "+s"(x)); return x; }
DEV u32x2 tr_half(const char* p) { return __builtin_bit_cast(u32x2, __builtin_amdgcn_ds_read_tr16_b64_v4i16((LAS s16x4*)p)); }
DEV u32x4 tr_frag(const char* p, int o0, int o1) {
  u32x2 a = tr_half(p + o0), c = tr_half(p + o1);
  return (u32x4){a[0], a[1], c[0], c[1]};
}
template <int N, typename FN, int I = 0>
DEV void static_for(FN&& fn) {
  if constexpr (I < N) { fn(std::integral_constant<int, I>{}); static_for<N, FN, I + 1>(static_cast<FN&&>(fn)); }
}

constexpr int OOB = 0x7ffffff0;
constexpr int SPIN_CAP = 400000;
#define POLL_PAUSE "s_nop 4\n\t"
DEV int all_off(int edge, int b) { return OFF_CA + (edge * 8 + (b & 7)) * 4096; }
DEV int grp_off(int idx) { return OFF_CH + idx * 256; }
DEV u32 poll_ld(rsrc_t rs, int soff, bool all, int w) {
  const int voff = (all ? (fresh_lane() & 15) * 256 : 0) | (w == 3 ? 0 : 0x7ff00000);
  return __builtin_amdgcn_raw_buffer_load_b32(rs, voff, soff, SC1);
}
DEV void poll_spin(rsrc_t rs, int soff, bool all, u32 f) {
  if (__builtin_expect(!__all(f >= 16u), 0)) {
    const int voff = all ? (fresh_lane() & 15) * 256 : 0;
    int spins = SPIN_CAP;
    u32 t;
    asm volatile("s_nop 4\n"
                 "1:\n\t"
                 "buffer_load_dword %0, %2, %3, %4 offen sc1\n\t"
                 "s_sub_u32 %1, %1, 1\n\t"
                 "s_cbranch_scc1 2f\n\t"
                 "s_waitcnt vmcnt(0)\n\t"
                 "v_cmp_gt_u32 vcc, 16, %0\n\t"
                 POLL_PAUSE
                 "s_cbranch_vccnz 1b\n"
                 "2:\n\t"
                 "s_waitcnt vmcnt(0)"
                 : "=&v"(t), "+s"(spins) : "v"(voff), "s"(rs), "s"(soff) : "memory", "vcc", "scc");
  }
  asm volatile("" ::: "memory");
}
DEV void poll_wait(rsrc_t rs, int soff, bool all, u32 f, int w) {
  if (w == 3) poll_spin(rs, soff, all, f);
  asm volatile("s_barrier" ::: "memory", ACLOB);
}
DEV void poll_wait2(rsrc_t rs, int soff, bool all, u32 f0, u32 f1, int w) {
  if (w == 3) { if (!__all(f0 >= 16u)) poll_spin(rs, soff, all, f1); }
  asm volatile("s_barrier" ::: "memory", ACLOB);
}
template <int YOUNGER, bool LDSW>
DEV void drain_bar() {
  if constexpr (LDSW) asm volatile("s_waitcnt vmcnt(%c0) lgkmcnt(0)\n\ts_barrier" :: "i"(YOUNGER) : "memory", ACLOB);
  else asm volatile("s_waitcnt vmcnt(%c0)\n\ts_barrier" :: "i"(YOUNGER) : "memory", ACLOB);
}
DEV void signal_all(rsrc_t rs, int edge, int b, int w) {
  if (w == 0) {
    const int lane = fresh_lane();
    __builtin_amdgcn_raw_ptr_buffer_atomic_add_i32(1, rs, lane < 8 ? lane * 4096 : OOB, OFF_CA + edge * 32768 + (b & 15) * 256, SC1);
  }
}
DEV void signal_grp(rsrc_t rs, int idx, int w) {
  if (w == 0) {
    const int lane = fresh_lane();
    __builtin_amdgcn_raw_ptr_buffer_atomic_add_i32(1, rs, lane == 0 ? 0 : OOB, OFF_CH + idx * 256, SC1);
  }
}

DEV void init_bump(rsrc_t rs) {
  const int lane = fresh_lane();
  __builtin_amdgcn_raw_ptr_buffer_atomic_add_i32(1, rs, lane < 8 ? lane * 256 : OOB, OFF_INIT, SC1);
}
DEV void init_wait(rsrc_t rs, int soff, u32 f0, u32 f) {
  if (__builtin_expect(__any(f == f0), 0)) {
    int spins = SPIN_CAP;
    u32 t;
    asm volatile("s_nop 4\n"
                 "1:\n\t"
                 "buffer_load_dword %0, off, %3, %4 sc1\n\t"
                 "s_sub_u32 %1, %1, 1\n\t"
                 "s_cbranch_scc1 2f\n\t"
                 "s_waitcnt vmcnt(0)\n\t"
                 "v_cmp_eq_u32 vcc, %2, %0\n\t"
                 "s_nop 4\n\t"
                 "s_cbranch_vccnz 1b\n"
                 "2:\n\t"
                 "s_waitcnt vmcnt(0)"
                 : "=&v"(t), "+s"(spins) : "v"(f0), "s"(rs), "s"(soff) : "memory", "vcc", "scc");
  }
  asm volatile("" ::: "memory");
}

template <int P> struct PrepRegs { u32x4 pa[P], pb[P]; u32x2 xv[2]; };
template <int P>
DEV void prep_ld1(rsrc_t rs, int i, int tid, int xso, int sso, PrepRegs<P>& pr) {
  if (i < 2 * P) {
    if (i & 1) pr.pb[i >> 1] = ld16(rs, tid * 32 + 16, sso + (i >> 1) * (L * 8));
    else pr.pa[i >> 1] = ld16(rs, tid * 32, sso + (i >> 1) * (L * 8));
  } else pr.xv[i - 2 * P] = ld8(rs, tid * 8, xso + (i - 2 * P) * (L * 2));
}
template <int P>
DEV void prep_load(rsrc_t rs, int tid, int xso, int sso, PrepRegs<P>& pr) {
#pragma unroll
  for (int i = 0; i < 2 * P + 2; ++i) prep_ld1<P>(rs, i, tid, xso, sso, pr);
  SB();
}
DEV void kill4(u32x4& x) { asm volatile("" : "=v"(x)); }
DEV void kill2(u32x2& x) { asm volatile("" : "=v"(x)); }
template <int P> DEV void prep_kill(PrepRegs<P>& pr) {
#pragma unroll
  for (int p = 0; p < P; ++p) { kill4(pr.pa[p]); kill4(pr.pb[p]); }
  kill2(pr.xv[0]); kill2(pr.xv[1]);
}
DEV void ln_apply(char* smem, int h, int tid, u32 dsk, f32x4 s0, f32x4 s1, const u32x2 (&xv)[2], f32x4 (&z)[2]) {
  const f32x2 t0 = ln_mv(s0.xy), t1 = ln_mv(s0.zw), t2 = ln_mv(s1.xy), t3 = ln_mv(s1.zw);
  const f32x2 rr[2] = {{__builtin_amdgcn_rsqf(t0[1]), __builtin_amdgcn_rsqf(t1[1])}, {__builtin_amdgcn_rsqf(t2[1]), __builtin_amdgcn_rsqf(t3[1])}};
  const f32x2 mr[2] = {{-t0[0] * rr[0][0], -t1[0] * rr[0][1]}, {-t2[0] * rr[1][0], -t3[0] * rr[1][1]}};
#pragma unroll
  for (int c = 0; c < 2; ++c) {
    const float dk = c ? hi_f(dsk) : lo_f(dsk);
    u32x2 u;
    f32x2 zz[2];
#pragma unroll
    for (int i = 0; i < 2; ++i) {
      const f32x2 x = unpk(xv[c][i]);
      const f32x2 uf = __builtin_elementwise_fma(x, rr[i], mr[i]);
      u[i] = pk_bf16(uf);
      zz[i] = __builtin_elementwise_fma(uf, (f32x2){dk, dk}, x);
    }
    z[c] = (f32x4){zz[0][0], zz[0][1], zz[1][0], zz[1][1]};
    *(u32x2*)(smem + LD_U + c * UCH + (256 + h * 1024) * 2 + tid * 8) = u;
  }
}
DEV void prep_ln(char* smem, int h, int tid, u32 dsk, const PrepRegs<1>& pr, f32x4 (&z)[2]) {
  ln_apply(smem, h, tid, dsk, __builtin_bit_cast(f32x4, pr.pa[0]), __builtin_bit_cast(f32x4, pr.pb[0]), pr.xv, z);
}
struct AccRegs { u32x4 a[4]; u32x2 xv[2]; };
DEV void acc_ld1(rsrc_t rs, int i, int tid, int xso, int h, AccRegs& r) {
  if (i < 4) r.a[i] = ld16(rs, tid * 16, OFF_AC + h * 16384 + i * 4096);
  else r.xv[i - 4] = ld8(rs, tid * 8, xso + (i - 4) * (L * 2));
}
DEV u32x2 to_fix(float t) {
  const float hi = __builtin_floorf(t * 0x1p-32f);
  return (u32x2){(u32)__builtin_fmaf(hi, -0x1p32f, t), (u32)(int)hi};
}
DEV void acc_ln(rsrc_t rs, char* smem, int h, int tid, u32 dsk, const AccRegs& r, f32x4 (&z)[2]) {
  f32x2 st[4];
#pragma unroll
  for (int i = 0; i < 4; ++i) {
    const f32x2 hi = {(float)(int)r.a[i][1], (float)(int)r.a[i][3]}, lo = {(float)r.a[i][0], (float)r.a[i][2]};
    st[i] = __builtin_elementwise_fma(hi, (f32x2){0x1p2f, 0x1p10f}, lo * (f32x2){0x1p-30f, 0x1p-22f});
  }
  f32x4 s0 = {st[0][0], st[0][1], st[1][0], st[1][1]}, s1 = {st[2][0], st[2][1], st[3][0], st[3][1]};
  const u32 mk = (r.a[0][3] | r.a[1][3]) | (r.a[2][3] | r.a[3][3]);
  if (__builtin_expect(__any(mk >= (1u << 27)), 0)) {
    s0 = s1 = (f32x4){0.f, 0.f, 0.f, 0.f};
#pragma unroll 1
    for (int p = 0; p < 8; ++p) {
      s0 += __builtin_bit_cast(f32x4, ld16(rs, tid * 32, OFF_ST1 + h * 8192 + p * (L * 8)));
      s1 += __builtin_bit_cast(f32x4, ld16(rs, tid * 32 + 16, OFF_ST1 + h * 8192 + p * (L * 8)));
    }
  }
  ln_apply(smem, h, tid, dsk, s0, s1, r.xv, z);
}
template <int M0, int I0, int I1> struct ConvPlan {
  static constexpr int Q0 = (M0 - I1) > 0 ? (M0 - I1) : 0, Q1 = M0 + 3 - I0;
  static constexpr int NQ = Q1 - Q0 + 1, NU = I1 - I0 + 1, NF = NQ + NU;
  static constexpr int count() { int n = 0; for (int i = I0; i <= I1; ++i) for (int m = 0; m < 4; ++m) n += (M0 + m - i >= 0); return n; }
  static constexpr int NM = count();
  static constexpr int im(int t) { int n = 0; for (int i = I0; i <= I1; ++i) for (int m = 0; m < 4; ++m) if (M0 + m - i >= 0) { if (n == t) return i * 4 + m; ++n; } return 0; }
  static constexpr int frag(int f) {
    if (f == 0) return 0;
    if (f <= 4) return -(Q1 - 4 + f + 1);
    int g = f - 5, nlow = NQ - 4;
    if (g < 2 * nlow) return (g & 1) ? -(Q1 - 4 - (g >> 1) + 1) : 1 + (g >> 1);
    return 1 + nlow + (g - 2 * nlow);
  }
};
template <int M0, int I0, int I1, bool FIRST, typename FN>
DEV void conv_mfma(char* smem, int w, int lane, f32x4 (&acc)[4], FN&& fill) {
  using P = ConvPlan<M0, I0, I1>;
  const int c16 = lane & 15, kq = lane >> 4;
  const int cch = w >> 1, wr = w & 1;
  const char* tb = smem + cch * KCH + (c16 >> 2) * KCOPY + (16 + 16 * (1 + 8 * wr) - 8 * kq + 4 * (c16 & 3) - 4) * 2;
  const char* ub = smem + LD_U + cch * UCH + (256 - 16 * (1 + 8 * wr + 6) + 16 * c16 + 8 * kq) * 2;
  u32x4 T[2][P::NQ], U[2][P::NU];
  auto ldf = [&](auto rc, auto fc) {
    constexpr int rr = IC(rc), f = P::frag(IC(fc));
    if constexpr (f >= 0) U[rr & 1][f] = *(const u32x4*)(ub + (3 - rr) * 64 + (I0 + f) * 512);
    else T[rr & 1][-f - 1 - P::Q0] = tr_frag(tb, rr * 64 + (-f - 1) * 512 + 8, rr * 64 + (-f - 1) * 512);
  };
  static_for<P::NF>([&](auto fc) { ldf(std::integral_constant<int, 0>{}, fc); });
  SB();
  static_for<4 * P::NM>([&](auto nc) {
    constexpr int n = IC(nc), rr = n / P::NM, t = n % P::NM, i = P::im(t) >> 2, m = P::im(t) & 3, q = M0 + m - i;
    mfma_v<FIRST && rr == 0 && i == I0>(acc[m], T[rr & 1][q - P::Q0], U[rr & 1][i - I0]);
    if constexpr (rr < 3 && t < P::NF) ldf(std::integral_constant<int, rr + 1>{}, std::integral_constant<int, t>{});
    SB();
    fill(nc);
    SB();
  });
}
DEV void conv_out(rsrc_t rs, char* smem, int xso, int w, int lane, f32x4 (&acc)[4], const f32x4 (&z)[2]) {
  const int tid = w * 64 + lane;
  char* yw = smem + LD_Y + w * YW + (lane & 15) * 80 + (lane >> 4) * 16;
  const char* yb = smem + LD_Y + tid * 16 + (tid >> 2) * 16;
  const int vo = ((tid & 1) * L + (tid >> 1) * 8) * 2;
  mfma_done4(acc[0], acc[1], acc[2], acc[3]);
#pragma unroll
  for (int m = 0; m < 4; ++m) *(f32x4*)(yw + m * 1280) = acc[m];
  bar();
  u32x2 o[2];
#pragma unroll
  for (int c = 0; c < 2; ++c) {
    const f32x4 y = (*(const f32x4*)(yb + (2 * c) * YW) + z[c]) + *(const f32x4*)(yb + (2 * c + 1) * YW);
    o[c] = (u32x2){pk_bf16(y[0], y[1]), pk_bf16(y[2], y[3])};
  }
  const bool odd = tid & 1;
  u32 s0 = odd ? o[0][0] : o[1][0], s1 = odd ? o[0][1] : o[1][1];
  u32 g0 = __builtin_amdgcn_update_dpp(0u, s0, 0xB1, 0xf, 0xf, true);
  u32 g1 = __builtin_amdgcn_update_dpp(0u, s1, 0xB1, 0xf, 0xf, true);
  u32x4 v = odd ? (u32x4){g0, g1, o[1][0], o[1][1]} : (u32x4){o[0][0], o[0][1], g0, g1};
  st16(rs, vo, xso, v);
  SB();
}

template <int R, int IMM>
DEV void ld_agpr(rsrc_t rs, int voff, int sbase) {
  int tmp;
  asm volatile("s_add_u32 %0, %3, %c6\n\tbuffer_load_dwordx4 a[%c4:%c5], %1, %2, %0 offen sc1"
               : "=&s"(tmp) : "v"(voff), "s"(rs), "s"(sbase), "i"(R), "i"(R + 3), "i"(IMM) : "memory", "scc");
}
template <int I> DEV void ld_w1(rsrc_t rs, int voff, int so01, int so2) {
  constexpr int ks = I / 3, i = I % 3;
  if constexpr (i < 2) ld_agpr<4 * I, (i * KS1 + ks) * 1024>(rs, voff, so01);
  else ld_agpr<4 * I, ks * 1024>(rs, voff, so2);
}
template <int R, int IMM>
DEV void ld_agpr_imm(rsrc_t rs, int voff, int soff) {
  asm volatile("buffer_load_dwordx4 a[%c3:%c4], %0, %1, %2 offen offset:%c5 sc1"
               :: "v"(voff), "s"(rs), "s"(soff), "i"(R), "i"(R + 3), "i"(IMM) : "memory");
}

constexpr int XS_STEP = 32 * L * 2;
DEV int xs_voff(int w, int lane) {
  const int fsw = ((lane >> 4) & 1) | ((w & 1) << 1);
  return (w * 8 + (lane >> 3)) * (L * 2) + ((((lane & 7) >> 1) ^ fsw) * 32) + (lane & 1) * 16;
}
template <int KS>
DEV void xs_dma(rsrc_t rs, int voff, int sbase, int ldsw) {
  int tmp;
  asm volatile("s_add_u32 m0, %4, %c5\n\ts_add_u32 %0, %3, %c6\n\tbuffer_load_dwordx4 %1, %2, %0 offen sc1 lds"
               : "=&s"(tmp) : "v"(voff), "s"(rs), "s"(sbase), "s"(ldsw), "i"(LD_XT + KS * 4096), "i"(KS * XS_STEP) : "memory", "scc");
}

__global__ __launch_bounds__(256, 1) void main_kernel(
    const u16* __restrict__ x0, const u16* __restrict__ Are, const u16* __restrict__ Aim,
    const u16* __restrict__ Bre, const u16* __restrict__ Bim, const u16* __restrict__ Cre,
    const u16* __restrict__ Cim, const u16* __restrict__ logdt, const u16* __restrict__ dskip,
    const u16* __restrict__ wg, const u16* __restrict__ wu, const u16* __restrict__ wd,
    char* __restrict__ ws, u16* __restrict__ out) {
  extern __shared__ __attribute__((aligned(16))) char smem[];
  const int w_ = __builtin_amdgcn_readfirstlane(threadIdx.x >> 6);
  const int b_ = blockIdx.x;

  const rsrc_t rs = __builtin_amdgcn_make_buffer_rsrc(ws, 0, WS_END, 0x00020000);
  STAMP_DECL
  STAMP();
  asm volatile("" ::: ACLOB);
  u32 cpf;
  {
    unsigned long long pc; int len;
    asm volatile("s_getpc_b64 %0\n.Lmk_code_begin:\n\ts_mov_b32 %1, .Lmk_code_end-.Lmk_code_begin" : "=s"(pc), "=s"(len));
    const int off = min((int)((min(threadIdx.x & 63, 4u) * 4 + (threadIdx.x >> 6)) * 32 + (blockIdx.x >> 3)) * 128, len - 4) & ~3;
    cpf = *(const GAS u32*)(pc + (unsigned long long)(u32)off);
    SB();
    asm volatile("" :: "s"(x0), "s"(Are), "s"(Aim), "s"(Bre), "s"(Bim), "s"(Cre), "s"(Cim), "s"(logdt), "s"(dskip), "s"(wg), "s"(wu), "s"(wd), "s"(ws));
  }
  const int rt_ = ((b_ & 7) >> 1) * 4 + ((b_ >> 3) & 3), cb_ = (b_ & 1) * 8 + (b_ >> 5);
  const int rt2_ = (b_ & 7) * 4 + ((b_ >> 3) & 3), ct_ = b_ >> 5;
  const int start_ = cb_ * 10 + (cb_ < 12 ? cb_ : 12), cnt_ = cb_ < 12 ? 11 : 10;
  const int nbase_ = (w_ < 3) ? w_ * 3 : cnt_ - 2;
  const int nt0_ = start_ + nbase_;
  const int ok2_ = __builtin_amdgcn_readfirstlane((w_ < 3) && (nbase_ + 2 < cnt_ - 2));
#define ROLES(LANE) \
  const int w = w_, b = b_, rt = rt_, rt2 = rt2_, ct = ct_, nt0 = nt0_, ok2 = ok2_; \
  const int lane = LANE, tid = w * 64 + lane, c16 = lane & 15, kq = lane >> 4, wvo = lane * 16; \
  const int x1_off = OFF_X1 + layer * ACT2, h_off = OFF_H + layer * HID2; \
  const int hb_e = h_off + (rt2 * 2 * NT1 * 16 + w * 11 * 64) * 16, hb_l = hb_e + 64 * NT1 * 256; \
  const int s10 = (w == 3) ? 9 * 1024 : 10 * 1024; \
  (void)b; (void)rt; (void)ct; (void)nt0; (void)ok2; (void)tid; (void)c16; (void)kq; (void)wvo; (void)x1_off; (void)hb_e; (void)hb_l; (void)s10;

  u32 dsk;
  const int ldsw = (int)(u32)(size_t)(LAS char*)smem + w_ * 1024;
  {
    constexpr int layer = 0;
    ROLES(fresh_lane())
    const int d0 = 2 * b;
    u32x4 xf[4], xt[2], wr[4];
    const bool initw = (b == 0) && (w == 3);
    const int init_off = OFF_INIT + (b & 7) * 256;
    const u32 f0 = __builtin_amdgcn_raw_buffer_load_b32(rs, 0, init_off, SC1);
    __builtin_amdgcn_raw_buffer_store_b32(0u, rs, initw ? lane * 256 : OOB, OFF_CA + 6 * 32768, SC1);
    {
      const int ln = b * 4 + lane;
      const int vo = initw ? OFF_CA + 6 * 32768 + 16384 + lane * 256 : (w == 1 && lane < 4 && ln < 832) ? (ln < 768 ? ln : ln + 128) * 256 : OOB;
      __builtin_amdgcn_raw_buffer_store_b32(0u, rs, vo, 0, SC1);
    }
    SB();
    {
      const rsrc_t rx = __builtin_amdgcn_make_buffer_rsrc((void*)x0, 0, L * D * 2, 0x00020000);
      const rsrc_t rg = __builtin_amdgcn_make_buffer_rsrc((void*)wg, 0, D * F * 2, 0x00020000);
      const rsrc_t ru = __builtin_amdgcn_make_buffer_rsrc((void*)wu, 0, D * F * 2, 0x00020000);
      const int tvo = (lane >> 1) * (D * 2) + (lane & 1) * 16, tso = (rt2 * 64 * D + ct * 64 + w * 16) * 2;
      xt[0] = __builtin_amdgcn_raw_buffer_load_b128(rx, tvo, tso, 0);
      xt[1] = __builtin_amdgcn_raw_buffer_load_b128(rx, tvo, tso + 32 * D * 2, 0);
      const int gvo = kq * (F * 2) + (c16 < cnt_ ? c16 : 0) * 16, gso = ((rt * 32 + w * 8) * F + start_ * 8) * 2;
      wr[0] = __builtin_amdgcn_raw_buffer_load_b128(rg, gvo, gso, 0);
      wr[1] = __builtin_amdgcn_raw_buffer_load_b128(ru, gvo, gso, 0);
      wr[2] = __builtin_amdgcn_raw_buffer_load_b128(rg, gvo, gso + 4 * F * 2, 0);
      wr[3] = __builtin_amdgcn_raw_buffer_load_b128(ru, gvo, gso + 4 * F * 2, 0);
      const int fvo = (c16 & 7) * (D * 2) + kq * 16, fso = (b * 8 * D + w * 128) * 2;
#pragma unroll
      for (int i = 0; i < 4; ++i) xf[i] = __builtin_amdgcn_raw_buffer_load_b128(rx, fvo + i * 64, fso, 0);
    }
    SB();
    STAMP();
    {
      u32x4 z = splat4(0u);
      if (tid < 16) *(u32x4*)(smem + (tid >> 1) * KCOPY + (tid & 1) * 16) = z;
      if (tid >= 64 && tid < 128) *(u32x4*)(smem + LD_U + (lane >> 5) * UCH + (lane & 31) * 16) = z;
      if (tid == 128) *(u32x4*)(smem + LD_KT + 760 * 16) = z;
    }
    int ix[12];
#pragma unroll
    for (int i = 0; i < 12; i += 2) {
      const int l0 = tid * 8 - 4 + i;
      const u32 bits = pk_bf16((float)l0, (float)(l0 + 1));
      const int i0 = l0 < 256 ? (l0 < 0 ? 676 : l0) : (int)(bits & 0xffffu) - 17024;
      const int i1 = l0 < 256 ? (l0 < 0 ? 676 : l0 + 1) : (int)(bits >> 16) - 17024;
      ix[i] = LD_KT + (i0 + (i0 >> 3)) * 16; ix[i + 1] = LD_KT + (i1 + (i1 >> 3)) * 16;
      asm volatile("" : "+v"(ix[i]), "+v"(ix[i + 1]));
    }
    SB();
    if (initw) { asm volatile("s_waitcnt vmcnt(10)" ::: "memory"); init_bump(rs); }
    const u32 pn = b * (2 * NS) + tid;
    const u32 p_ar = Are[pn], p_ai = Aim[pn], p_br = Bre[pn], p_bi = Bim[pn];
    SB();
    int kx[2][2][4];
#pragma unroll
    for (int c = 0; c < 2; ++c) {
      const int B = 16 * c + c16;
#pragma unroll
      for (int i = 0; i < 4; ++i) {
        const int i0 = kq < 2 ? 8 * B + 4 * kq + i : kq == 2 ? 256 + 4 * B + i : 384 + (i >> 1) * 64 + 2 * B + (i & 1);
        const int i1 = kq == 0 ? 512 + 32 * i + B : (kq == 1 && i == 0) ? 640 + B : 680;
        kx[0][c][i] = LD_KT + (i0 + (i0 >> 3)) * 16 + w * 4; kx[1][c][i] = LD_KT + (i1 + (i1 >> 3)) * 16 + w * 4;
        asm volatile("" : "+v"(kx[0][c][i]), "+v"(kx[1][c][i]));
      }
    }
    SB();
    const u32 p_cr = Cre[pn], p_ci = Cim[pn];
    const u32 p_ld = logdt[(u32)(d0 + (tid >> 7))];
    dsk = ((const u32*)dskip)[(u32)(b + (lane >> 6))];
    const int chunk = b + 256 * (w ^ 2);
    u32x4 w2r[4];
    {
      const rsrc_t rwd = __builtin_amdgcn_make_buffer_rsrc((void*)wd, 0, F * D * 2, 0x00020000);
#pragma unroll
      for (int i = 0; i < 4; ++i)
        w2r[i] = __builtin_amdgcn_raw_buffer_load_b128(rwd, (lane >> 3) * (D * 2) + (lane & 7) * 16, ((chunk >> 3) * 32 * D + (chunk & 7) * 64 + i * 8 * D) * 2, 0);
    }
    SB();
    STAMP();
    {
      char* img = smem + LD_Y + w * YW;
      *(u32x4*)(img + wvo) = xt[0];
      *(u32x4*)(img + 1024 + wvo) = xt[1];
      const char* tp = img + (kq * 8 + (c16 >> 2)) * 32 + (c16 & 3) * 8;
      u32x4 t0 = tr_frag(tp, 0, 128), t1 = tr_frag(tp, 1024, 1024 + 128);
      const int so = OFF_XT + ((ct * 64 + w * 16) * L + rt2 * 64) * 2;
      st16(rs, c16 * (L * 2) + kq * 16, so, t0);
      st16(rs, c16 * (L * 2) + kq * 16 + 64, so, t1);
    }
    constexpr int WROW = 368;
    if (c16 < 11) {
      char* wdst = smem + LD_XT + (w * 8 + kq) * WROW + c16 * 32;
#pragma unroll
      for (int i = 0; i < 2; ++i) {
        const u32x4 g = wr[2 * i], u = wr[2 * i + 1];
        *(u32x4*)(wdst + i * 4 * WROW) = (u32x4){g[0], u[0], g[1], u[1]};
        *(u32x4*)(wdst + i * 4 * WROW + 16) = (u32x4){g[2], u[2], g[3], u[3]};
      }
    }
    {
      const u32x4 ones = splat4(0x3f803f80u);
      f32x4 sa, qa;
      static_for<4>([&](auto ic) { mfma_v<IC(ic) == 0>(sa, ones, xf[IC(ic)]); mfma_v<IC(ic) == 0>(qa, xf[IC(ic)], xf[IC(ic)]); });
      asm volatile("s_nop 7" : "+v"(sa), "+v"(qa));
      if ((c16 >> 2) == kq && c16 < 8) {
        const int r = c16 & 3;
        float q = r == 0 ? qa[0] : r == 1 ? qa[1] : r == 2 ? qa[2] : qa[3];
        *(f32x2*)(smem + LD_SS + (w * 8 + c16) * 8) = (f32x2){sa[0], q};
      }
    }
    bar();
    asm volatile("" :: "v"(cpf));
    if (initw) init_bump(rs);
    const int so01 = OFF_WGU + nt0 * KS1 * 1024, so2 = ok2 ? so01 + 2 * KS1 * 1024 : w == 3 ? OFF_WGU + NT1 * KS1 * 1024 : WS_END;
    {
      const char* wsrc = smem + LD_XT + (8 * kq + (c16 >> 2)) * WROW + nbase_ * 32 + (c16 & 3) * 8;
      u32x4 f0 = tr_frag(wsrc, 0, 4 * WROW), f1 = tr_frag(wsrc, 32, 32 + 4 * WROW), f2 = tr_frag(wsrc, 64, 64 + 4 * WROW);
      if (tid < 8) {
        const f32x2* sp = (const f32x2*)(smem + LD_SS) + tid;
        f32x2 v = (sp[0] + sp[8]) + (sp[16] + sp[24]);
        __builtin_amdgcn_raw_buffer_store_b64(__builtin_bit_cast(u32x2, v), rs, tid * 8, OFF_ST0 + b * 64, SC1);
      }
      st16(rs, wvo, so01 + rt * 1024, f0);
      st16(rs, wvo, so01 + (KS1 + rt) * 1024, f1);
      st16(rs, (w == 2 && lane < 8) ? wvo : OOB, OFF_AC + b * 128, splat4(0u));
      if (w == 3) f2 = splat4(0x3f803f80u);
      if (w != 3 || cb_ == 15) st16(rs, wvo, so2 + rt * 1024, f2);
    }
    SB();
    STAMP();
    PrepRegs<1> pe, pl;
    u32 pv, pv2;
    const u32 f1 = __builtin_amdgcn_raw_buffer_load_b32(rs, 0, init_off, SC1);
    SB();
    {
      constexpr int TROW = 272;
      char* tab = smem + LD_XT + 32768 - 4096 + w * (32 * TROW);
      char* tar = smem + LD_Y + w * YW;
      char* tar2 = smem + LD_W2 + w * 4096;
      const float dt = rbf(expf(lo_f(p_ld)));
      const float ar = dt * lo_f(p_ar), ai = dt * lo_f(p_ai), br = dt * lo_f(p_br), bi = dt * lo_f(p_bi);
      const float cr = lo_f(p_cr), ci = lo_f(p_ci);
      const float I2PI_HI = 0.15915494309189535f;
      const float I2PI_LO = (float)(0.15915494309189535 - (double)0.15915494309189535f);
      const float a2 = ar * 1.4426950408889634f;
      const float rh = ai * I2PI_HI;
      const float rl = __builtin_fmaf(ai, I2PI_LO, __builtin_fmaf(ai, I2PI_HI, -rh));
      auto cexp = [&](float l) {
        const float e = __builtin_amdgcn_exp2f(a2 * l);
        const float fr = __builtin_fmaf(l, rl, __builtin_amdgcn_fractf(rh * l));
        return (f32x2){__builtin_amdgcn_cosf(fr), __builtin_amdgcn_sinf(fr)} * e;
      };
      const float kr = cr * br - ci * bi, ki = cr * bi + ci * br;
      u32 mx = max(__float_as_uint(kr) & 0x7f800000u, __float_as_uint(ki) & 0x7f800000u);
      mx = max(mx, (u32)__builtin_amdgcn_update_dpp(0, (int)mx, 0xB1, 0xf, 0xf, true));
      mx = max(mx, (u32)__builtin_amdgcn_update_dpp(0, (int)mx, 0x4E, 0xf, 0xf, true));
      mx = max(mx, (u32)__builtin_amdgcn_update_dpp(0, (int)mx, 0x124, 0xf, 0xf, true));
      mx = max(mx, (u32)__builtin_amdgcn_update_dpp(0, (int)mx, 0x128, 0xf, 0xf, true));
      mx = max(max((u32)__builtin_amdgcn_readlane((int)mx, 0), (u32)__builtin_amdgcn_readlane((int)mx, 16)),
               max((u32)__builtin_amdgcn_readlane((int)mx, 32), (u32)__builtin_amdgcn_readlane((int)mx, 48)));
      mx = min(max(mx, 60u << 23), 200u << 23);
      const float up = __uint_as_float((267u << 23) - mx);
      const float kscale = __uint_as_float(mx - (27u << 23));
      const f32x2 e1 = cexp(1.f), e8 = cexp(8.f), e128 = cexp(128.f), e256 = cexp(256.f), e1024 = cexp(1024.f);
      const CM c8 = mkcm((f32x2){e8[0], -e8[1]}), c128 = mkcm((f32x2){e128[0], -e128[1]}), m1 = mkcm(e1), m256 = mkcm(e256);
      u32 hv;
      auto putr = [&](int r, u32 v) { *(u32*)((r < 16 ? tar + r * TROW : tar2 + (r - 16) * TROW) + lane * 4) = v; };
      auto putc = [&](int c, u32 v) { *(u32*)(tab + c * TROW + lane * 4) = v; };
      f32x2 p[8];
      p[0] = (f32x2){16384.f, 0.f}; putr(0, pk_f16(p[0]));
      p[1] = e1 * 16384.f; putr(1, pk_f16(p[1]));
#pragma unroll
      for (int i = 2; i < 8; ++i) { p[i] = cmul_h(p[i - 1], m1, hv); putr(i, hv); }
      f32x2 q0 = e256 * 16384.f; putr(8, pk_f16(q0));
      (void)cmul_h(p[2], m256, hv); putr(9, hv);
      f32x2 q4 = cmul_h(p[4], m256, hv); putr(10, hv);
      (void)cmul_h(p[6], m256, hv); putr(11, hv);
      q0 = cmul_h(q0, m256, hv); putr(12, hv);
      q4 = cmul_h(q4, m256, hv); putr(13, hv);
      (void)cmul_h(q0, m256, hv); putr(14, hv);
      (void)cmul_h(q4, m256, hv); putr(15, hv);
      q0 = e1024 * 16384.f; putr(16, pk_f16(q0));
#pragma unroll
      for (int i = 17; i < 21; ++i) { q0 = cmul_h(q0, m256, hv); putr(i, hv); }
      SB();
      STAMP();
      if (initw) init_bump(rs);
      init_wait(rs, init_off, f0, f1);
      drain_bar<0, false>();
      signal_all(rs, 6, b, w);
      SB();
      STAMP();
      f32x2 v0 = (f32x2){kr, -ki} * up; putc(0, pk_f16(v0));
      f32x2 v1 = cmul_h(v0, c128, hv); putc(16, hv);
#pragma unroll
      for (int i = 1; i < 16; ++i) {
        v0 = cmul_h(v0, c8, hv); putc(i, hv);
        v1 = cmul_h(v1, c8, hv); putc(16 + i, hv);
      }
      f32x4 ka[2][2];
      const char* ra0 = tar + c16 * TROW + kq * 16;
      const char* ra1 = tar2 + (c16 < 5 ? c16 : 0) * TROW + kq * 16;
      const char* rb0 = tab + c16 * TROW + kq * 16;
      static_for<4>([&](auto sc) {
        constexpr int s = IC(sc);
        const u32x4 a0 = *(const u32x4*)(ra0 + s * 64), a1 = *(const u32x4*)(ra1 + s * 64);
        const u32x4 b0 = *(const u32x4*)(rb0 + s * 64), b1 = *(const u32x4*)(rb0 + 16 * TROW + s * 64);
        mfma_h<s == 0>(ka[0][0], a0, b0); mfma_h<s == 0>(ka[0][1], a0, b1);
        mfma_h<s == 0>(ka[1][0], a1, b0); mfma_h<s == 0>(ka[1][1], a1, b1);
      });
      pv = poll_ld(rs, all_off(6, b), true, w);
      mfma_done4(ka[0][0], ka[0][1], ka[1][0], ka[1][1]);
#pragma unroll
      for (int r = 0; r < 2; ++r)
#pragma unroll
        for (int c = 0; c < 2; ++c) {
          const f32x4 v = ka[r][c] * kscale;
#pragma unroll
          for (int i = 0; i < 4; ++i) *(float*)(smem + kx[r][c][i]) = v[i];
        }
    }
    bar();
    STAMP();
    {
      u32 e[2][6];
#pragma unroll
      for (int i = 0; i < 6; ++i) {
        const f32x4 a = *(const f32x4*)(smem + ix[2 * i]), c = *(const f32x4*)(smem + ix[2 * i + 1]);
        e[0][i] = pk_bf16(a[0] + a[1], c[0] + c[1]);
        e[1][i] = pk_bf16(a[2] + a[3], c[2] + c[3]);
      }
      pv2 = poll_ld(rs, all_off(6, b), true, w);
#pragma unroll
      for (int c = 0; c < 2; ++c) {
        char* kb = smem + c * KCH + tid * 16 + 32;
        u32 o[5];
#pragma unroll
        for (int i = 0; i < 5; ++i) o[i] = __builtin_amdgcn_alignbit(e[c][i + 1], e[c][i], 16);
        *(u32x4*)(kb) = (u32x4){e[c][2], e[c][3], e[c][4], e[c][5]};
        *(u32x4*)(kb + KCOPY) = (u32x4){o[1], o[2], o[3], o[4]};
        *(u32x4*)(kb + 2 * KCOPY) = (u32x4){e[c][1], e[c][2], e[c][3], e[c][4]};
        *(u32x4*)(kb + 3 * KCOPY) = (u32x4){o[0], o[1], o[2], o[3]};
      }
    }
    SB();
    if (chunk < KS2P * 8) {
      char* img = smem + LD_W2 + 16384 + w * 4096;
#pragma unroll
      for (int i = 0; i < 4; ++i)
        *(u32x4*)(img + i * 1024 + (lane >> 3) * 128 + ((((lane & 7) >> 1) ^ (((lane >> 4) & 1) | ((i & 1) << 1))) * 32) + (lane & 1) * 16) = w2r[i];
      const int fr = ((c16 >> 3) & 1) | ((kq & 1) << 1);
#pragma unroll
      for (int i = 0; i < 4; ++i)
        st16(rs, wvo, OFF_WD + (((chunk & 7) * 4 + i) * KS2P + (chunk >> 3)) * 1024,
             tr_frag(img + (8 * kq + (c16 >> 2)) * 128 + ((i ^ fr) * 32) + (c16 & 3) * 8, 0, 512));
    }
    SB();
    STAMP();
    if (initw) {
#pragma unroll 1
      for (int spins = 0; spins < SPIN_CAP && !__all(pv2 >= 16u); ++spins) { init_bump(rs); pv2 = poll_ld(rs, all_off(6, b), true, w); }
    }
    poll_wait2(rs, all_off(6, b), true, pv, pv2, w);
    STAMP();
    prep_load<1>(rs, tid, OFF_XT + d0 * L * 2, OFF_ST0, pe);
    prep_load<1>(rs, tid, OFF_XT + d0 * L * 2 + 2048, OFF_ST0 + 8192, pl);
    f32x4 ze[2], zl[2];
    prep_ln(smem, 0, tid, dsk, pe, ze);
    asm volatile("" : "+v"(pl.pa[0]), "+v"(pl.pb[0]), "+v"(pl.xv[0]), "+v"(pl.xv[1]));
    SB();
    static_for<10>([&](auto ic) { ld_w1<IC(ic)>(rs, wvo, so01, so2); });
    bar();
    STAMP();
    f32x4 acc[4];
    conv_mfma<0, 0, 3, true>(smem, w, lane, acc, [&](auto nc) {
      constexpr int n = IC(nc);
      if constexpr (n % 4 == 3) ld_w1<10 + n / 4>(rs, wvo, so01, so2);
    });
    prep_ln(smem, 1, tid, dsk, pl, zl);
    conv_out(rs, smem, OFF_X1 + d0 * L * 2, w, lane, acc, ze);
    STAMP();
    conv_mfma<4, 0, 7, true>(smem, w, lane, acc, [&](auto nc) {
      constexpr int n = IC(nc);
      if constexpr (n < 40 && n % 5 == 0) ld_w1<20 + n / 5>(rs, wvo, so01, so2);
      if constexpr (n == 40) { drain_bar<8, false>(); signal_all(rs, 0, b, w); }
      if constexpr (n >= 42 && n < 102 && n % 3 == 0) ld_w1<28 + (n - 42) / 3>(rs, wvo, so01, so2);
    });
    STAMP();
    pv = poll_ld(rs, all_off(0, b), true, w);
    conv_out(rs, smem, OFF_X1 + d0 * L * 2 + 2048, w, lane, acc, zl);
    pv2 = poll_ld(rs, all_off(0, b), true, w);
    STAMP();
    SB();
    asm volatile("s_waitcnt vmcnt(3)" ::: "memory");
    poll_wait2(rs, all_off(0, b), true, pv, pv2, w);
    STAMP();
    {
      const int gx = xs_voff(w, lane), gs = OFF_X1 + rt * 128;
      const u32x4 nones = splat4(0xbf80bf80u);
      f32x4 c[3];
      static_for<48>([&](auto ic) {
        mfma_aA<4 * IC(ic), (IC(ic) < 3)>(c[IC(ic) % 3], nones); SB();
        if constexpr (IC(ic) < 16 && !(IC(ic) & 1)) { xs_dma<IC(ic) / 2>(rs, gx, gs, ldsw); SB(); }
      });
      asm volatile("s_nop 7" : "+v"(c[0]), "+v"(c[1]), "+v"(c[2]));
#pragma unroll
      for (int i = 0; i < 3; ++i) *(f32x4*)(smem + LD_CS + w * 192 + i * 64 + kq * 16) = c[i];
    }
    drain_bar<0, true>();
    signal_all(rs, 1, b, w);
    STAMP();
  }
  const int lane_g = fresh_lane();
  static_for<2>([&](auto lc) {
    constexpr int layer = IC(lc);
    AccRegs pr;
    u32x4 W2[5][4];
    u32x4 hf[11][2];
    {
      ROLES(lane_g)
      int sw2[4];
#pragma unroll
      for (int i = 0; i < 4; ++i) sw2[i] = OFF_WD + ((ct * 4 + ((w + i) & 3)) * KS2P + w * 11) * 1024;
      const int fr = ((c16 >> 3) & 1) | ((kq & 1) << 1);
      const char* xr[4];
#pragma unroll
      for (int j = 0; j < 4; ++j) xr[j] = smem + LD_XT + (8 * kq + (c16 >> 2)) * 128 + ((((w + j) & 3) ^ fr) * 32) + (c16 & 3) * 8;
      const int gx = xs_voff(w, lane), gs = x1_off + rt * 128;
      u32x4 xs[8];
      char* ssq = smem + ((c16 >> 2) == kq ? LD_SS + w * 128 : LD_Y) + c16 * 8 + 4;
      char* sss = smem + (w == 3 ? LD_SS : LD_Y) + c16 * 8;
      u32x2 xb[2][4][2];
#pragma unroll
      for (int j = 0; j < 4; ++j) { xb[0][j][0] = tr_half(xr[j]); xb[0][j][1] = tr_half(xr[j] + 512); }
      SB();
      static_for<2>([&](auto hc) {
        constexpr int h = IC(hc);
        f32x4 ga[3][4], qa;
        const int nxt_poll = h == 0 ? all_off(layer * 4 + 1, b) : grp_off(layer * 32 + (rt2 >> 1));
        u32 pv, pv2;
        {
          auto fill = [&](auto gc) {
            constexpr int g = IC(gc);
            if constexpr (g < 8) xs_dma<8 + g>(rs, gx, gs + h * 2048, ldsw);
            if constexpr (g == (h == 0 ? SIG_CL : SIG_HE) && (h == 1 || layer == 1)) {
              drain_bar<8, false>();
              if constexpr (h == 1) signal_grp(rs, layer * 32 + rt, w); else signal_all(rs, layer * 4 + 1, b, w);
            }
            if constexpr (g == 27) drain_bar<(h == 0 && layer == 0) ? 16 : (h == 1 ? 7 : 0), true>();
            if constexpr (h == 0 && layer == 0) {
              if constexpr (g >= 10 && g < 26) ld_agpr_imm<192 + 16 * ((g - 10) & 3) + 4 * ((g - 10) >> 2), ((g - 10) & 3) * 1024>(rs, wvo, sw2[(g - 10) >> 2]);
              if constexpr (g >= 28 && g < 36) xs[g - 28] = ld16(rs, wvo + ((g - 28) >> 2) * 1024, sw2[(g - 28) & 3] + 4096);
              if constexpr (g >= 43 && g < 51) *(u32x4*)(smem + LD_W2 + w * 8192 + (g - 43) * 1024 + wvo) = xs[g - 43];
            }
            if constexpr (h == 1) {
              if constexpr (g >= 14 && g < 47 && (g > 27 || !(g & 1)) && (g < 34 || !(g & 1))) {
                constexpr int n = g < 28 ? (g - 14) / 2 : g < 34 ? g - 21 : 13 + (g - 34) / 2, i = n / 5, s = n % 5;
                W2[s][i] = ld16(rs, wvo + (s & 3) * 1024, sw2[i] + (6 + (s >> 2) * 4) * 1024);
              }
            }
            if constexpr (g == POLL_G - 12) pv = poll_ld(rs, nxt_poll, h == 0, w);
            if constexpr (g == POLL_G - 5) pv2 = poll_ld(rs, nxt_poll, h == 0, w);
            if constexpr (g == POLL_G) poll_wait2(rs, nxt_poll, h == 0, pv, pv2, w);
            if constexpr (g >= POLL_G && g < POLL_G + 8) {
              if constexpr (h == 0) xs_dma<g - POLL_G>(rs, gx, gs + 2048, ldsw);
              else hf[g - POLL_G][0] = ld16(rs, wvo + ((g - POLL_G) & 3) * 1024, hb_e + ((g - POLL_G) >> 2) * 4096);
            }
          };
          static_for<60>([&](auto gc) {
            constexpr int g = IC(gc), ks = g >> 2, j = g & 3;
            const u32x4 x = {xb[ks & 1][j][0][0], xb[ks & 1][j][0][1], xb[ks & 1][j][1][0], xb[ks & 1][j][1][1]};
            mfma_aA<12 * ks, ks == 0>(ga[0][j], x);
            xb[(ks + 1) & 1][j][0] = tr_half(xr[j] + (ks + 1) * 4096);
            SB();
            mfma_aA<12 * ks + 4, ks == 0>(ga[1][j], x);
            xb[(ks + 1) & 1][j][1] = tr_half(xr[j] + (ks + 1) * 4096 + 512);
            SB();
            mfma_aA<12 * ks + 8, ks == 0>(ga[2][j], x);
            SB();
            fill(gc);
            SB();
            if constexpr (j == 0) { mfma_v<ks == 0>(qa, x, x); SB(); }
          });
          u32x4 x[4];
#pragma unroll
          for (int j = 0; j < 4; ++j) x[j] = (u32x4){xb[1][j][0][0], xb[1][j][0][1], xb[1][j][1][0], xb[1][j][1][1]};
          mfma_vv(qa, x[0], x[0]); SB();
#pragma unroll
          for (int j = 0; j < 4; ++j) { mfma_aA<188, false>(ga[2][j], x[j]); SB(); }
#pragma unroll
          for (int j = 0; j < 4; ++j) { mfma_aA<180, false>(ga[0][j], x[j]); SB(); }
          asm volatile("" : "+v"(qa));
          {
            const int r = c16 & 3;
            const float q01 = (r & 1) ? qa[1] : qa[0], q23 = (r & 1) ? qa[3] : qa[2];
            SB();
            mfma_aA<184, false>(ga[1][0], x[0]); SB();
            const float q = (r & 2) ? q23 : q01;
            *(float*)ssq = q;
            SB();
            mfma_aA<184, false>(ga[1][1], x[1]); SB();
            tie4(ga[2][0], ga[2][1], ga[2][2], ga[2][3]);
            *(float*)(sss + 384) = ga[2][0][0];
            *(float*)(sss) = ga[2][1][0];
            SB();
            mfma_aA<184, false>(ga[1][2], x[2]); SB();
            *(float*)(sss + 128) = ga[2][2][0];
            *(float*)(sss + 256) = ga[2][3][0];
            SB();
            mfma_aA<184, false>(ga[1][3], x[3]); SB();
          }
        }
        STAMP();
        f32x4 cs[3];
#pragma unroll
        for (int i = 0; i < 3; ++i) cs[i] = *(const f32x4*)(smem + LD_CS + w * 192 + i * 64 + kq * 16);
        bar();
        f32x2 st[4];
#pragma unroll
        for (int j = 0; j < 4; ++j) st[j] = *(const f32x2*)(smem + LD_SS + (((w + j) & 3) * 16 + c16) * 8);
        tie4(ga[0][0], ga[0][1], ga[0][2], ga[0][3]);
        tie4(ga[1][0], ga[1][1], ga[1][2], ga[1][3]);
        tie4(ga[2][0], ga[2][1], ga[2][2], ga[2][3]);
        SB();
        u32 hd[3][4];
#pragma unroll
        for (int j = 0; j < 4; ++j) {
          const f32x2 mv = ln_mv(st[j]);
          const float nl = __builtin_amdgcn_rsqf(mv[1]) * -1.4426950408889634f;
#pragma unroll
          for (int i = 0; i < 3; ++i) {
            const f32x4 v = __builtin_elementwise_fma(cs[i], (f32x4){mv[0], mv[0], mv[0], mv[0]}, ga[i][j]);
            const f32x2 g = {v[0], v[1]}, u = {v[2], v[3]};
            const f32x2 a = g * nl;
            const f32x2 e = {__builtin_amdgcn_exp2f(a[0]), __builtin_amdgcn_exp2f(a[1])};
            const f32x2 d = __builtin_elementwise_fma(e, (f32x2){mv[1], mv[1]}, (f32x2){mv[1], mv[1]});
            const f32x2 r = {__builtin_amdgcn_rcpf(d[0]), __builtin_amdgcn_rcpf(d[1])};
            hd[i][j] = pk_bf16((g * u) * r);
            if constexpr (h == 1) {
              const int t = j * 3 + i;
              if (t < 3) hf[8 + t][0] = ld16(rs, wvo, hb_e + (t == 2 ? s10 : (8 + t) * 1024));
              else hf[t - 3][1] = ld16(rs, wvo + ((t - 3) & 3) * 1024, hb_e + NT1 * 256 + ((t - 3) >> 2) * 4096);
            }
          }
          if constexpr (h == 1) SB();
        }
        const int hb = (((w + kq) & 3) * NT1 * 16 + c16) * 16, hs = h_off + ((h * 64 + rt * 4) * NT1 + nt0) * 256;
#pragma unroll
        for (int i = 0; i < 3; ++i) {
          auto a02 = __builtin_amdgcn_permlane32_swap(hd[i][0], hd[i][2], false, false);
          auto a13 = __builtin_amdgcn_permlane32_swap(hd[i][1], hd[i][3], false, false);
          auto o01 = __builtin_amdgcn_permlane16_swap(a02[0], a13[0], false, false);
          auto o23 = __builtin_amdgcn_permlane16_swap(a02[1], a13[1], false, false);
          u32x4 v = {o01[0], o01[1], o23[0], o23[1]};
          st16(rs, (i < 2 || ok2) ? hb + i * 256 : OOB, hs, v);
        }
        SB();
        if constexpr (h == 0) {
          drain_bar<3, false>();
#pragma unroll
          for (int j = 0; j < 4; ++j) { xb[0][j][0] = tr_half(xr[j]); xb[0][j][1] = tr_half(xr[j] + 512); }
        }
        SB();
        STAMP();
      });
    }
    {
      ROLES(fresh_lane())
      char* rw[3];
#pragma unroll
      for (int i = 0; i < 3; ++i) rw[i] = smem + LD_XT + (((w + 1 + i) & 3) * 3 + i) * 2048 + wvo;
      const char* rb = smem + LD_XT + w * 6144 + wvo;
      const int rvo = c16 * (L * 2) + kq * 8;
      const int ovo = c16 * (L * 2) + (kq & 1) * 32 + (kq >> 1) * 16;
      char* ow = smem + LD_OT + (w * 16 + c16) * 64 + (kq & 1) * 32 + (kq >> 1) * 16;
      const char* ob = smem + LD_OT + (8 * kq + (c16 >> 2)) * 64 + (w & 1) * 32 + (c16 & 3) * 8;
      const char* wlp = smem + LD_W2 + w * 8192 + wvo;
      static_for<2>([&](auto hc) {
        constexpr int h = IC(hc);
        const int tok0 = h * 1024 + rt2 * 32;
        const int eso = ((ct * 64 + w * 16) * L + tok0) * 2;
        const int hb = h ? hb_l : hb_e;
        f32x4 da[4][2];
        u32x4 wl[2][4];
        u32x2 res[2];
        u32 pv, pv2;
        static_for<88>([&](auto nc) {
          constexpr int n = IC(nc), j = n / 44, s = (n % 44) >> 2, i = (n + 1) & 3;
          if constexpr (s < 4) mfma_aB<192 + 16 * s + 4 * i, s == 0>(da[i][j], hf[s][j]);
          else if constexpr (s < 6) mfma_vv(da[i][j], hf[s][j], wl[s - 4][i]);
          else mfma_vv(da[i][j], hf[s][j], W2[s - 6][i]);
          SB();
          if constexpr (h == 0) {
            if constexpr (n == 3 || n == 7) hf[9 + (n >> 2)][1] = ld16(rs, wvo, hb + NT1 * 256 + (n == 7 ? s10 : 9 * 1024));
            if constexpr (n == 10) res[0] = ld8(rs, rvo, x1_off + eso);
            if constexpr (n == 13) res[1] = ld8(rs, rvo + 32, x1_off + eso);
            if constexpr (n == SIG_HL) { drain_bar<4, false>(); signal_grp(rs, layer * 32 + 16 + rt, w); }
            if constexpr (n == 86) pv = poll_ld(rs, grp_off(layer * 32 + 16 + (rt2 >> 1)), false, w);
          } else {
            if constexpr (n < 33 && n % 3 == 2) hf[n / 3][1] = ld16(rs, wvo + (n / 3 < 8 ? ((n / 3) & 3) * 1024 : 0), hb + NT1 * 256 + (n / 3 < 8 ? (n / 12) * 4096 : n / 3 == 10 ? s10 : (n / 3) * 1024));
            if constexpr (layer == 0 && n == SIG_XE) { drain_bar<(SIG_XE + 1) / 3, false>(); signal_all(rs, 2, b, w); }
            if constexpr (n == 35) res[0] = ld8(rs, rvo, x1_off + eso);
            if constexpr (n == 38) res[1] = ld8(rs, rvo + 32, x1_off + eso);
          }
          if constexpr (n % 44 >= 6 && n % 44 < 14) wl[(n % 44 - 6) >> 2][(n % 44 - 5) & 3] = *(const u32x4*)(wlp + (((n % 44 - 6) & 4) + ((n % 44 - 5) & 3)) * 1024);
          if constexpr (n == 52 || n == 56 || n == 60) { asm volatile("" : "+v"(da[(n - 48) >> 2][0])); *(f32x4*)(rw[(n - 52) >> 2]) = da[(n - 48) >> 2][0]; }
          SB();
        });
        mfma_done4(da[0][1], da[1][1], da[2][1], da[3][1]);
        SB();
        STAMP();
#pragma unroll
        for (int i = 0; i < 3; ++i) *(f32x4*)(rw[i] + 1024) = da[i + 1][1];
        if constexpr (h == 0) pv2 = poll_ld(rs, grp_off(layer * 32 + 16 + (rt2 >> 1)), false, w);
        bar();
        u32x2 xo[2];
#pragma unroll
        for (int j = 0; j < 2; ++j) {
          const f32x4 rf = {lo_f(res[j][0]), hi_f(res[j][0]), lo_f(res[j][1]), hi_f(res[j][1])};
          const f32x4 v = ((da[0][j] + rf) + *(const f32x4*)(rb + j * 1024)) + (*(const f32x4*)(rb + 2048 + j * 1024) + *(const f32x4*)(rb + 4096 + j * 1024));
          xo[j] = (u32x2){pk_bf16(v[0], v[1]), pk_bf16(v[2], v[3])};
        }
        auto s0 = __builtin_amdgcn_permlane16_swap(xo[0][0], xo[1][0], false, false);
        auto s1 = __builtin_amdgcn_permlane16_swap(xo[0][1], xo[1][1], false, false);
        const u32x4 xv = {s0[0], s1[0], s0[1], s1[1]};
        *(u32x4*)ow = xv;
        if constexpr (layer == 0) st16(rs, ovo, OFF_XT + ACT2 + eso, xv);
        SB();
        if constexpr (h == 0) {
          poll_wait2(rs, grp_off(layer * 32 + 16 + (rt2 >> 1)), false, pv, pv2, w);
#pragma unroll
          for (int s = 0; s < 6; ++s) hf[s][0] = ld16(rs, wvo + (s & 3) * 1024, hb_l + (s >> 2) * 4096);
        } else if constexpr (layer == 0) pv = poll_ld(rs, all_off(2, b), true, w);
        SB();
        bar();
        if constexpr (h == 0) {
#pragma unroll
          for (int s = 6; s < 11; ++s) hf[s][0] = ld16(rs, wvo + (s < 8 ? (s & 3) * 1024 : 0), hb_l + (s < 8 ? 4096 : s == 10 ? s10 : s * 1024));
          SB();
        }
        if constexpr (layer == 0) {
          if (w < 2) {
            u32x4 f0 = tr_frag(ob, 0, 256), f1 = tr_frag(ob, 2048, 2048 + 256);
            const u32x4 ones = splat4(0x3f803f80u);
            f32x4 sa, qa;
            mfma_v0(sa, ones, f0); mfma_v0(qa, f0, f0); mfma_vv(sa, ones, f1); mfma_vv(qa, f1, f1);
            asm volatile("s_nop 7" : "+v"(sa), "+v"(qa));
            const int r = c16 & 3;
            float q = r == 0 ? qa[0] : r == 1 ? qa[1] : r == 2 ? qa[2] : qa[3];
            u32x2 v = {__float_as_uint(sa[0]), __float_as_uint(q)};
            const bool diag = (c16 >> 2) == kq;
            const bool fits = q < 0x1p33f;
            const u32x2 fs = to_fix(fits ? sa[0] * 0x1p30f : 0.f), fq = to_fix(fits ? q * 0x1p22f : 0x1p59f);
            const int avo = diag ? (c16 & 3) * 4096 + kq * 16 : OOB, aso = OFF_AC + h * 16384 + (rt2 * 8 + w * 4) * 16;
            asm volatile("s_nop 4\n\tbuffer_atomic_add_x2 %0, %2, %3, %4 offen sc1\n\tbuffer_atomic_add_x2 %1, %2, %3, %4 offen offset:8 sc1"
                         :: "v"(fs), "v"(fq), "v"(avo), "s"(rs), "s"(aso) : "memory");
            __builtin_amdgcn_raw_buffer_store_b64(v, rs, diag ? c16 * 8 : OOB, OFF_ST1 + (ct * L + tok0 + w * 16) * 8, SC1);
          }
        } else {
          u32x4 f = tr_frag(ob, (w >> 1) * 2048, (w >> 1) * 2048 + 256);
          const rsrc_t ro = __builtin_amdgcn_make_buffer_rsrc(out, 0, L * D * 2, 0x00020000);
          __builtin_amdgcn_raw_buffer_store_b128(f, ro, (c16 * D + kq * 8) * 2, ((tok0 + (w & 1) * 16) * D + ct * 64 + (w >> 1) * 32) * 2, 0);
        }
        SB();
        STAMP();
        if constexpr (h == 1 && layer == 0) {
          poll_wait(rs, all_off(2, b), true, pv, w);
          STAMP();
#pragma unroll
          for (int i = 0; i < 6; ++i) acc_ld1(rs, i, tid, OFF_XT + ACT2 + b * 2 * L * 2, 0, pr);
          SB();
        }
      });
    }
    if constexpr (layer == 0) {
      ROLES(fresh_lane())
      const int xo = b * 2 * L * 2;
      f32x4 z[2], acc[4];
      u32 pv, pv2;
      acc_ln(rs, smem, 0, tid, dsk, pr, z);
      drain_bar<0, true>();
      signal_all(rs, 3, b, w);
      STAMP();
      conv_mfma<0, 0, 3, true>(smem, w, lane, acc, [&](auto) {});
      conv_out(rs, smem, OFF_X1 + ACT2 + xo, w, lane, acc, z);
      STAMP();
      conv_mfma<4, 0, 3, true>(smem, w, lane, acc, [&](auto nc) {
        constexpr int n = IC(nc);
        if constexpr (n == 22) pv = poll_ld(rs, all_off(3, b), true, w);
        if constexpr (n == 36) { drain_bar<1, false>(); signal_all(rs, 4, b, w); pv2 = poll_ld(rs, all_off(3, b), true, w); }
        if constexpr (n == 52) poll_wait2(rs, all_off(3, b), true, pv, pv2, w);
        if constexpr (n >= 52 && !(n & 1)) acc_ld1(rs, (n - 52) >> 1, tid, OFF_XT + ACT2 + xo + 2048, 1, pr);
      });
      STAMP();
      pv = poll_ld(rs, all_off(4, b), true, w);
      acc_ln(rs, smem, 1, tid, dsk, pr, z);
      bar();
      STAMP();
      const int gx = xs_voff(w, lane), gs = OFF_X1 + ACT2 + rt * 128;
      conv_mfma<4, 4, 7, false>(smem, w, lane, acc, [&](auto nc) {
        constexpr int n = IC(nc);
        if constexpr (n == 2) pv2 = poll_ld(rs, all_off(4, b), true, w);
        if constexpr (n == 22) poll_wait2(rs, all_off(4, b), true, pv, pv2, w);
        if constexpr (n >= 22 && n < 38 && !(n & 1)) xs_dma<(n - 22) / 2>(rs, gx, gs, ldsw);
      });
      STAMP();
      conv_out(rs, smem, OFF_X1 + ACT2 + xo + 2048, w, lane, acc, z);
      drain_bar<1, false>();
      STAMP();
    }
  });
  STAMP_OUT();
  asm volatile(".Lmk_code_end:" ::: "memory");
}

extern "C" void kernel_launch(void* const* d_in, const int* in_sizes, int n_in, void* d_out, int out_size,
                              void* d_ws, size_t ws_size, hipStream_t stream) {
  char* ws = (char*)d_ws;
  (void)hipFuncSetAttribute((const void*)main_kernel, hipFuncAttributeMaxDynamicSharedMemorySize, LDS_BYTES + LDS_EXTRA);
  main_kernel<<<256, 256, LDS_BYTES + LDS_EXTRA, stream>>>(
      (const u16*)d_in[0], (const u16*)d_in[1], (const u16*)d_in[2], (const u16*)d_in[3], (const u16*)d_in[4],
      (const u16*)d_in[5], (const u16*)d_in[6], (const u16*)d_in[7], (const u16*)d_in[8], (const u16*)d_in[10],
      (const u16*)d_in[11], (const u16*)d_in[12], ws, (u16*)d_out);
}
```

```cpp
#include <hip/hip_runtime.h>
#include <stdint.h>
#include <type_traits>

typedef unsigned short u16;
typedef unsigned int u32;
typedef u32 u32x4 __attribute__((ext_vector_type(4)));
typedef u32 u32x2 __attribute__((ext_vector_type(2)));
typedef float f32x4 __attribute__((ext_vector_type(4)));
typedef float f32x2 __attribute__((ext_vector_type(2)));
typedef short s16x4 __attribute__((ext_vector_type(4)));
typedef __bf16 bf16x2 __attribute__((ext_vector_type(2)));
typedef _Float16 f16x2 __attribute__((ext_vector_type(2)));
#define LAS __attribute__((address_space(3)))
#define GAS __attribute__((address_space(1)))
#define DEV __device__ __forceinline__
#define SB() __builtin_amdgcn_sched_barrier(0)
#define IC(x) decltype(x)::value
typedef __amdgpu_buffer_rsrc_t rsrc_t;

constexpr int L = 2048, D = 512, NS = 128, F = 1376;
constexpr int NT1 = 172;
constexpr int KS1 = 16;
constexpr int KS2P = 44;
constexpr float EPS = 1e-5f;
constexpr int SC1 = 16;
#define P_SIG_CL 9
#define P_SIG_HE 12
#define P_SIG_HL 44
#define P_SIG_XE 30
#define P_POLL_G 52
#define CE_P 14
#define X0_P 13
#define ACC_E 74
#define ACC_L 20
constexpr int SIG_CL = P_SIG_CL, SIG_HE = P_SIG_HE, SIG_HL = P_SIG_HL, SIG_XE = P_SIG_XE, POLL_G = P_POLL_G;

#define XPAD 64
constexpr int LP = L + XPAD;
constexpr int ACT = D * LP * 2;
constexpr int HID = F * L * 2;
constexpr int N_ALL = 7;
constexpr int OFF_CA  = 0;
constexpr int OFF_CH  = OFF_CA + N_ALL * 128 * 256;
constexpr int OFF_INIT = OFF_CH + 64 * 256;
constexpr int CTRL_BYTES = OFF_INIT + 8 * 256;
constexpr int OFF_XT  = CTRL_BYTES;
constexpr int ACT2 = ACT, HID2 = HID;
constexpr int OFF_X1  = OFF_XT + ACT + ACT2;
constexpr int OFF_H   = OFF_X1 + ACT + ACT2;
constexpr int OFF_WGU = OFF_H + HID + HID2;
constexpr int OFF_WD  = OFF_WGU + (NT1 + 1) * KS1 * 1024;
constexpr int OFF_ST0 = OFF_WD + 32 * KS2P * 1024;
constexpr int OFF_ST1 = OFF_ST0 + L * 8;
constexpr int OFF_AC  = OFF_ST1 + 8 * L * 8;
constexpr int WS_END  = OFF_AC + L * 16;

#define STAMP_DECL
#define STAMP()
#define STAMPE()
#define STAMP_OUT()
#define LDS_EXTRA 0

DEV float lo_f(u32 v) { return __uint_as_float(v << 16); }
DEV float hi_f(u32 v) { return __uint_as_float(v & 0xffff0000u); }
DEV u32 pk_bf16(float a, float b) {
  f32x2 v = {a, b};
  return __builtin_bit_cast(u32, __builtin_convertvector(v, bf16x2));
}
DEV u32 pk_bf16(f32x2 v) { return __builtin_bit_cast(u32, __builtin_convertvector(v, bf16x2)); }
DEV u32 pk_f16(f32x2 v) { return __builtin_bit_cast(u32, __builtin_convertvector(v, f16x2)); }
DEV f32x2 unpk(u32 v) { return (f32x2){lo_f(v), hi_f(v)}; }
DEV float rbf(float a) { return lo_f(pk_bf16(a, a)); }
DEV f32x2 ln_mv(f32x2 sq) {
  f32x2 t = __builtin_elementwise_fma(sq, (f32x2){1.0f / D, 1.0f / D}, (f32x2){0.f, EPS});
  t[1] = fmaxf(__builtin_fmaf(-t[0], t[0], t[1]), EPS);
  return t;
}
struct CM { f32x2 re, im; };
DEV CM mkcm(f32x2 c) { return CM{(f32x2){c[0], c[0]}, (f32x2){-c[1], c[1]}}; }
DEV f32x2 cmul_h(f32x2 x, const CM& c, u32& h) {
  const f32x2 r = __builtin_elementwise_fma(x.yx, c.im, x * c.re);
  h = pk_f16(r);
  return r;
}

constexpr int KCOPY = 4160, KCH = 4 * KCOPY;
constexpr int LD_U = 2 * KCH;
constexpr int UCH = (256 + L) * 2;
constexpr int LD_XT = LD_U + 2 * UCH;
constexpr int LD_Y = LD_XT + 65536;
constexpr int YW = 5120;
constexpr int LD_SS = LD_Y + 4 * YW;
constexpr int LD_OT = LD_Y;
constexpr int LD_CS = LD_SS + 512;
constexpr int LD_W2 = LD_CS + 768;
constexpr int LDS_BYTES = LD_W2 + 32768;
static_assert(LDS_BYTES <= 163840, "LDS");
constexpr int LD_KT = LD_XT + 16384;
constexpr int RROW = 144, RW = 64 * RROW;

#define ACLOB "a0","a1","a2","a3","a4","a5","a6","a7","a8","a9","a10","a11","a12","a13","a14","a15","a16","a17","a18","a19","a20","a21","a22","a23","a24","a25","a26","a27","a28","a29","a30","a31","a32","a33","a34","a35","a36","a37","a38","a39","a40","a41","a42","a43","a44","a45","a46","a47","a48","a49","a50","a51","a52","a53","a54","a55","a56","a57","a58","a59","a60","a61","a62","a63","a64","a65","a66","a67","a68","a69","a70","a71","a72","a73","a74","a75","a76","a77","a78","a79","a80","a81","a82","a83","a84","a85","a86","a87","a88","a89","a90","a91","a92","a93","a94","a95","a96","a97","a98","a99","a100","a101","a102","a103","a104","a105","a106","a107","a108","a109","a110","a111","a112","a113","a114","a115","a116","a117","a118","a119","a120","a121","a122","a123","a124","a125","a126","a127","a128","a129","a130","a131","a132","a133","a134","a135","a136","a137","a138","a139","a140","a141","a142","a143","a144","a145","a146","a147","a148","a149","a150","a151","a152","a153","a154","a155","a156","a157","a158","a159","a160","a161","a162","a163","a164","a165","a166","a167","a168","a169","a170","a171","a172","a173","a174","a175","a176","a177","a178","a179","a180","a181","a182","a183","a184","a185","a186","a187","a188","a189","a190","a191","a192","a193","a194","a195","a196","a197","a198","a199","a200","a201","a202","a203","a204","a205","a206","a207","a208","a209","a210","a211","a212","a213","a214","a215","a216","a217","a218","a219","a220","a221","a222","a223","a224","a225","a226","a227","a228","a229","a230","a231","a232","a233","a234","a235","a236","a237","a238","a239","a240","a241","a242","a243","a244","a245","a246","a247","a248","a249","a250","a251","a252","a253","a254","a255"
#define MF "v_mfma_f32_16x16x32_bf16 "
DEV void mfma_vv(f32x4& c, u32x4 a, u32x4 b) { asm(MF "%0, %1, %2, %0" : "+v"(c) : "v"(a), "v"(b)); }
DEV void mfma_v0(f32x4& c, u32x4 a, u32x4 b) { asm(MF "%0, %1, %2, 0" : "=&v"(c) : "v"(a), "v"(b)); }
template <bool FIRST> DEV void mfma_v(f32x4& c, u32x4 a, u32x4 b) { if constexpr (FIRST) mfma_v0(c, a, b); else mfma_vv(c, a, b); }
template <int R, bool FIRST> DEV void mfma_aA(f32x4& c, u32x4 b) {
  if constexpr (FIRST) asm(MF "%0, a[%c2:%c3], %1, 0" : "=&v"(c) : "v"(b), "i"(R), "i"(R + 3));
  else asm(MF "%0, a[%c2:%c3], %1, %0" : "+v"(c) : "v"(b), "i"(R), "i"(R + 3));
}
template <int R, bool FIRST> DEV void mfma_aB(f32x4& c, u32x4 a) {
  if constexpr (FIRST) asm(MF "%0, %1, a[%c2:%c3], 0" : "=&v"(c) : "v"(a), "i"(R), "i"(R + 3));
  else asm(MF "%0, %1, a[%c2:%c3], %0" : "+v"(c) : "v"(a), "i"(R), "i"(R + 3));
}
template <bool FIRST> DEV void mfma_h(f32x4& c, u32x4 a, u32x4 b) {
  if constexpr (FIRST) asm("v_mfma_f32_16x16x32_f16 %0, %1, %2, 0" : "=&v"(c) : "v"(a), "v"(b));
  else asm("v_mfma_f32_16x16x32_f16 %0, %1, %2, %0" : "+v"(c) : "v"(a), "v"(b));
}
DEV void mfma_done4(f32x4& a, f32x4& b, f32x4& c, f32x4& d) { asm volatile("s_nop 7" : "+v"(a), "+v"(b), "+v"(c), "+v"(d)); }
DEV void tie4(f32x4& a, f32x4& b, f32x4& c, f32x4& d) { asm volatile("" : "+v"(a), "+v"(b), "+v"(c), "+v"(d)); }
DEV void bar() { asm volatile("s_waitcnt lgkmcnt(0)\n\ts_barrier" ::: "memory", ACLOB); }

DEV u32x4 ld16(rsrc_t rs, int voff, int soff) { return __builtin_amdgcn_raw_buffer_load_b128(rs, voff, soff, SC1); }
DEV u32x2 ld8(rsrc_t rs, int voff, int soff) { return __builtin_amdgcn_raw_buffer_load_b64(rs, voff, soff, SC1); }
DEV void st16(rsrc_t rs, int voff, int soff, u32x4 v) { __builtin_amdgcn_raw_buffer_store_b128(v, rs, voff, soff, SC1); }
DEV int fresh_lane() {
  int l;
  asm volatile("v_mbcnt_lo_u32_b32 %0, -1, 0\n\tv_mbcnt_hi_u32_b32 %0, -1, %0" : "=v"(l));
  return l;
}
DEV u32x4 splat4(u32 c) {
  u32x4 r;
  asm volatile("v_mov_b32 %0, %4\n\tv_mov_b32 %1, %4\n\tv_mov_b32 %2, %4\n\tv_mov_b32 %3, %4"
               : "=v"(r[0]), "=v"(r[1]), "=v"(r[2]), "=v"(r[3]) : "i"(c));
  asm volatile("s_nop 1" : "+v"(r));
  return r;
}
DEV int opq(int x) { asm volatile("" : "+s"(x)); return x; }
DEV u32x2 tr_half(const char* p) { return __builtin_bit_cast(u32x2, __builtin_amdgcn_ds_read_tr16_b64_v4i16((LAS s16x4*)p)); }
DEV u32x4 tr_frag(const char* p, int o0, int o1) {
  u32x2 a = tr_half(p + o0), c = tr_half(p + o1);
  return (u32x4){a[0], a[1], c[0], c[1]};
}
template <int N, typename FN, int I = 0>
DEV void static_for(FN&& fn) {
  if constexpr (I < N) { fn(std::integral_constant<int, I>{}); static_for<N, FN, I + 1>(static_cast<FN&&>(fn)); }
}

constexpr int OOB = 0x7ffffff0;
constexpr int SPIN_CAP = 400000;
#define ACC_SLEEP 2
#define POLL_PAUSE "s_nop 4\n\t"
DEV int all_off(int edge, int b) { return OFF_CA + (edge * 8 + (b & 7)) * 4096; }
DEV int grp_off(int idx) { return OFF_CH + idx * 256; }
DEV u32 poll_ld(rsrc_t rs, int soff, bool all, int w) {
  const int voff = (all ? (fresh_lane() & 15) * 256 : 0) | (w == 3 ? 0 : 0x7ff00000);
  return __builtin_amdgcn_raw_buffer_load_b32(rs, voff, soff, SC1);
}
DEV void poll_spin(rsrc_t rs, int soff, bool all, u32 f) {
  if (__builtin_expect(!__all(f >= 16u), 0)) {
    const int voff = all ? (fresh_lane() & 15) * 256 : 0;
    int spins = SPIN_CAP;
    u32 t;
    asm volatile("s_nop 4\n"
                 "1:\n\t"
                 "buffer_load_dword %0, %2, %3, %4 offen sc1\n\t"
                 "s_sub_u32 %1, %1, 1\n\t"
                 "s_cbranch_scc1 2f\n\t"
                 "s_waitcnt vmcnt(0)\n\t"
                 "v_cmp_gt_u32 vcc, 16, %0\n\t"
                 POLL_PAUSE
                 "s_cbranch_vccnz 1b\n"
                 "2:\n\t"
                 "s_waitcnt vmcnt(0)"
                 : "=&v"(t), "+s"(spins) : "v"(voff), "s"(rs), "s"(soff) : "memory", "vcc", "scc");
  }
  asm volatile("" ::: "memory");
}
DEV void poll_wait(rsrc_t rs, int soff, bool all, u32 f, int w) {
  if (w == 3) poll_spin(rs, soff, all, f);
  asm volatile("s_barrier" ::: "memory", ACLOB);
}
DEV void poll_wait2(rsrc_t rs, int soff, bool all, u32 f0, u32 f1, int w) {
  if (w == 3) { if (!__all(f0 >= 16u)) poll_spin(rs, soff, all, f1); }
  asm volatile("s_barrier" ::: "memory", ACLOB);
}
template <int YOUNGER, bool LDSW>
DEV void drain_bar() {
  if constexpr (LDSW) asm volatile("s_waitcnt vmcnt(%c0) lgkmcnt(0)\n\ts_barrier" :: "i"(YOUNGER) : "memory", ACLOB);
  else asm volatile("s_waitcnt vmcnt(%c0)\n\ts_barrier" :: "i"(YOUNGER) : "memory", ACLOB);
}
DEV void signal_all(rsrc_t rs, int edge, int b, int w) {
  if (w == 0) {
    const int lane = fresh_lane();
    __builtin_amdgcn_raw_ptr_buffer_atomic_add_i32(1, rs, lane < 8 ? lane * 4096 : OOB, OFF_CA + edge * 32768 + (b & 15) * 256, SC1);
  }
}
DEV void signal_grp(rsrc_t rs, int idx, int w) {
  if (w == 0) {
    const int lane = fresh_lane();
    __builtin_amdgcn_raw_ptr_buffer_atomic_add_i32(1, rs, lane == 0 ? 0 : OOB, OFF_CH + idx * 256, SC1);
  }
}

DEV void init_bump(rsrc_t rs) {
  const int lane = fresh_lane();
  __builtin_amdgcn_raw_ptr_buffer_atomic_add_i32(1, rs, lane < 8 ? lane * 256 : OOB, OFF_INIT, SC1);
}
DEV void init_wait(rsrc_t rs, int soff, u32 f0, u32 f) {
  if (__builtin_expect(__any(f == f0), 0)) {
    int spins = SPIN_CAP;
    u32 t;
    asm volatile("s_nop 4\n"
                 "1:\n\t"
                 "buffer_load_dword %0, off, %3, %4 sc1\n\t"
                 "s_sub_u32 %1, %1, 1\n\t"
                 "s_cbranch_scc1 2f\n\t"
                 "s_waitcnt vmcnt(0)\n\t"
                 "v_cmp_eq_u32 vcc, %2, %0\n\t"
                 "s_nop 4\n\t"
                 "s_cbranch_vccnz 1b\n"
                 "2:\n\t"
                 "s_waitcnt vmcnt(0)"
                 : "=&v"(t), "+s"(spins) : "v"(f0), "s"(rs), "s"(soff) : "memory", "vcc", "scc");
  }
  asm volatile("" ::: "memory");
}

template <int P> struct PrepRegs { u32x4 pa[P], pb[P]; u32x2 xv[2]; };
template <int P>
DEV void prep_ld1(rsrc_t rs, int i, int tid, int xso, int sso, PrepRegs<P>& pr) {
  if (i < 2 * P) {
    if (i & 1) pr.pb[i >> 1] = ld16(rs, tid * 32 + 16, sso + (i >> 1) * (L * 8));
    else pr.pa[i >> 1] = ld16(rs, tid * 32, sso + (i >> 1) * (L * 8));
  } else pr.xv[i - 2 * P] = ld8(rs, tid * 8, xso + (i - 2 * P) * (LP * 2));
}
template <int P>
DEV void prep_load(rsrc_t rs, int tid, int xso, int sso, PrepRegs<P>& pr) {
#pragma unroll
  for (int i = 0; i < 2 * P + 2; ++i) prep_ld1<P>(rs, i, tid, xso, sso, pr);
  SB();
}
DEV void kill4(u32x4& x) { asm volatile("" : "=v"(x)); }
DEV void kill2(u32x2& x) { asm volatile("" : "=v"(x)); }
template <int P> DEV void prep_kill(PrepRegs<P>& pr) {
#pragma unroll
  for (int p = 0; p < P; ++p) { kill4(pr.pa[p]); kill4(pr.pb[p]); }
  kill2(pr.xv[0]); kill2(pr.xv[1]);
}
DEV void ln_apply(char* smem, int h, int tid, u32 dsk, f32x4 s0, f32x4 s1, const u32x2 (&xv)[2], f32x4 (&z)[2]) {
  const f32x2 t0 = ln_mv(s0.xy), t1 = ln_mv(s0.zw), t2 = ln_mv(s1.xy), t3 = ln_mv(s1.zw);
  const f32x2 rr[2] = {{__builtin_amdgcn_rsqf(t0[1]), __builtin_amdgcn_rsqf(t1[1])}, {__builtin_amdgcn_rsqf(t2[1]), __builtin_amdgcn_rsqf(t3[1])}};
  const f32x2 mr[2] = {{-t0[0] * rr[0][0], -t1[0] * rr[0][1]}, {-t2[0] * rr[1][0], -t3[0] * rr[1][1]}};
#pragma unroll
  for (int c = 0; c < 2; ++c) {
    const float dk = c ? hi_f(dsk) : lo_f(dsk);
    u32x2 u;
    f32x2 zz[2];
#pragma unroll
    for (int i = 0; i < 2; ++i) {
      const f32x2 x = unpk(xv[c][i]);
      const f32x2 uf = __builtin_elementwise_fma(x, rr[i], mr[i]);
      u[i] = pk_bf16(uf);
      zz[i] = __builtin_elementwise_fma(uf, (f32x2){dk, dk}, x);
    }
    z[c] = (f32x4){zz[0][0], zz[0][1], zz[1][0], zz[1][1]};
    *(u32x2*)(smem + LD_U + c * UCH + (256 + h * 1024) * 2 + tid * 8) = u;
  }
}
DEV void prep_ln(char* smem, int h, int tid, u32 dsk, const PrepRegs<1>& pr, f32x4 (&z)[2]) {
  ln_apply(smem, h, tid, dsk, __builtin_bit_cast(f32x4, pr.pa[0]), __builtin_bit_cast(f32x4, pr.pb[0]), pr.xv, z);
}
struct AccRegs { u32x4 a[4]; u32x2 xv[2]; };
DEV void acc_ld1(rsrc_t rs, int i, int tid, int xso, int h, AccRegs& r) {
  if (i < 4) r.a[i] = ld16(rs, tid * 16, OFF_AC + h * 16384 + i * 4096);
  else r.xv[i - 4] = ld8(rs, tid * 8, xso + (i - 4) * (LP * 2));
}
DEV u32x2 to_fix(float t) {
  const float hi = __builtin_floorf(t * 0x1p-32f);
  const u32 lo = (u32)fminf(__builtin_fmaf(hi, -0x1p32f, t), 4294967040.f);
  return (u32x2){(lo & ~15u) | 1u, (u32)(int)hi};
}
DEV bool acc_ready(const AccRegs& r) {
  u32 o = 0u, n = ~0u;
#pragma unroll
  for (int i = 0; i < 4; ++i) { o |= r.a[i][0] | r.a[i][2]; n &= r.a[i][0] & r.a[i][2]; }
  const u32 mx = max(max(r.xv[0][0], r.xv[0][1]), max(r.xv[1][0], r.xv[1][1]));
  return __all(((o & 7u) | (~n & 8u)) == 0u && mx != ~0u);
}
DEV void acc_wait(rsrc_t rs, int tid, int xso, int h, int poll_off, AccRegs& r, bool ready) {
  if (__builtin_expect(!ready, 0)) {
#pragma unroll 1
    for (int spins = 0; spins < SPIN_CAP; ++spins) {
      bool done = false;
      if ((spins & 7) == 7) done = __all(__builtin_amdgcn_raw_buffer_load_b32(rs, (tid & 15) * 256, poll_off, SC1) >= 16u);
      asm volatile("" ::: "memory");
#pragma unroll
      for (int i = 0; i < 6; ++i) acc_ld1(rs, i, tid, xso, h, r);
      if (done || acc_ready(r)) break;
      __builtin_amdgcn_s_sleep(ACC_SLEEP);
    }
    __builtin_amdgcn_s_waitcnt(0x0F70);
  }
}
DEV void acc_ln(rsrc_t rs, char* smem, int h, int tid, u32 dsk, const AccRegs& r, f32x4 (&z)[2]) {
  f32x2 st[4];
#pragma unroll
  for (int i = 0; i < 4; ++i) {
    const f32x2 hi = {(float)(int)r.a[i][1], (float)(int)r.a[i][3]}, lo = {(float)r.a[i][0], (float)r.a[i][2]};
    st[i] = __builtin_elementwise_fma(hi, (f32x2){0x1p2f, 0x1p6f}, lo * (f32x2){0x1p-30f, 0x1p-26f});
  }
  f32x4 s0 = {st[0][0], st[0][1], st[1][0], st[1][1]}, s1 = {st[2][0], st[2][1], st[3][0], st[3][1]};
  const u32 mk = (r.a[0][3] | r.a[1][3]) | (r.a[2][3] | r.a[3][3]);
  if (__builtin_expect(__any(mk >= (1u << 27)), 0)) {
    poll_spin(rs, all_off(2 + h, blockIdx.x), true, 0u);
    s0 = s1 = (f32x4){0.f, 0.f, 0.f, 0.f};
#pragma unroll 1
    for (int p = 0; p < 8; ++p) {
      s0 += __builtin_bit_cast(f32x4, ld16(rs, tid * 32, OFF_ST1 + h * 8192 + p * (L * 8)));
      s1 += __builtin_bit_cast(f32x4, ld16(rs, tid * 32 + 16, OFF_ST1 + h * 8192 + p * (L * 8)));
    }
  }
  ln_apply(smem, h, tid, dsk, s0, s1, r.xv, z);
}
template <int M0, int I0, int I1> struct ConvPlan {
  static constexpr int Q0 = (M0 - I1) > 0 ? (M0 - I1) : 0, Q1 = M0 + 3 - I0;
  static constexpr int NQ = Q1 - Q0 + 1, NU = I1 - I0 + 1, NF = NQ + NU;
  static constexpr int count() { int n = 0; for (int i = I0; i <= I1; ++i) for (int m = 0; m < 4; ++m) n += (M0 + m - i >= 0); return n; }
  static constexpr int NM = count();
  static constexpr int im(int t) { int n = 0; for (int i = I0; i <= I1; ++i) for (int m = 0; m < 4; ++m) if (M0 + m - i >= 0) { if (n == t) return i * 4 + m; ++n; } return 0; }
  static constexpr int frag(int f) {
    if (f == 0) return 0;
    if (f <= 4) return -(Q1 - 4 + f + 1);
    int g = f - 5, nlow = NQ - 4;
    if (g < 2 * nlow) return (g & 1) ? -(Q1 - 4 - (g >> 1) + 1) : 1 + (g >> 1);
    return 1 + nlow + (g - 2 * nlow);
  }
};
template <int M0, int I0, int I1, bool FIRST, typename FN>
DEV void conv_mfma(char* smem, int w, int lane, f32x4 (&acc)[4], FN&& fill) {
  using P = ConvPlan<M0, I0, I1>;
  const int c16 = lane & 15, kq = lane >> 4;
  const int cch = w >> 1, wr = w & 1;
  const char* tb = smem + cch * KCH + (c16 >> 2) * KCOPY + (16 + 16 * (1 + 8 * wr) - 8 * kq + 4 * (c16 & 3) - 4) * 2;
  const char* ub = smem + LD_U + cch * UCH + (256 - 16 * (1 + 8 * wr + 6) + 16 * c16 + 8 * kq) * 2;
  u32x4 T[2][P::NQ], U[2][P::NU];
  auto ldf = [&](auto rc, auto fc) {
    constexpr int rr = IC(rc), f = P::frag(IC(fc));
    if constexpr (f >= 0) U[rr & 1][f] = *(const u32x4*)(ub + (3 - rr) * 64 + (I0 + f) * 512);
    else T[rr & 1][-f - 1 - P::Q0] = tr_frag(tb, rr * 64 + (-f - 1) * 512 + 8, rr * 64 + (-f - 1) * 512);
  };
  static_for<P::NF>([&](auto fc) { ldf(std::integral_constant<int, 0>{}, fc); });
  SB();
  static_for<4 * P::NM>([&](auto nc) {
    constexpr int n = IC(nc), rr = n / P::NM, t = n % P::NM, i = P::im(t) >> 2, m = P::im(t) & 3, q = M0 + m - i;
    mfma_v<FIRST && rr == 0 && i == I0>(acc[m], T[rr & 1][q - P::Q0], U[rr & 1][i - I0]);
    if constexpr (rr < 3 && t < P::NF) ldf(std::integral_constant<int, rr + 1>{}, std::integral_constant<int, t>{});
    SB();
    fill(nc);
    SB();
  });
}
DEV void conv_out(rsrc_t rs, char* smem, int xso, int w, int lane, f32x4 (&acc)[4], const f32x4 (&z)[2]) {
  const int tid = w * 64 + lane;
  char* yw = smem + LD_Y + w * YW + (lane & 15) * 80 + (lane >> 4) * 16;
  const char* yb = smem + LD_Y + tid * 16 + (tid >> 2) * 16;
  const int vo = ((tid & 1) * LP + (tid >> 1) * 8) * 2;
  mfma_done4(acc[0], acc[1], acc[2], acc[3]);
#pragma unroll
  for (int m = 0; m < 4; ++m) *(f32x4*)(yw + m * 1280) = acc[m];
  bar();
  u32x2 o[2];
#pragma unroll
  for (int c = 0; c < 2; ++c) {
    const f32x4 y = (*(const f32x4*)(yb + (2 * c) * YW) + z[c]) + *(const f32x4*)(yb + (2 * c + 1) * YW);
    o[c] = (u32x2){pk_bf16(y[0], y[1]), pk_bf16(y[2], y[3])};
  }
  const bool odd = tid & 1;
  u32 s0 = odd ? o[0][0] : o[1][0], s1 = odd ? o[0][1] : o[1][1];
  u32 g0 = __builtin_amdgcn_update_dpp(0u, s0, 0xB1, 0xf, 0xf, true);
  u32 g1 = __builtin_amdgcn_update_dpp(0u, s1, 0xB1, 0xf, 0xf, true);
  u32x4 v = odd ? (u32x4){g0, g1, o[1][0], o[1][1]} : (u32x4){o[0][0], o[0][1], g0, g1};
  st16(rs, vo, xso, v);
  SB();
}

template <int R, int IMM>
DEV void ld_agpr(rsrc_t rs, int voff, int sbase) {
  int tmp;
  asm volatile("s_add_u32 %0, %3, %c6\n\tbuffer_load_dwordx4 a[%c4:%c5], %1, %2, %0 offen sc1"
               : "=&s"(tmp) : "v"(voff), "s"(rs), "s"(sbase), "i"(R), "i"(R + 3), "i"(IMM) : "memory", "scc");
}
template <int I> DEV void ld_w1(rsrc_t rs, int voff, int so01, int so2) {
  constexpr int ks = I / 3, i = I % 3;
  if constexpr (i < 2) ld_agpr<4 * I, (i * KS1 + ks) * 1024>(rs, voff, so01);
  else ld_agpr<4 * I, ks * 1024>(rs, voff, so2);
}
template <int R, int IMM>
DEV void ld_agpr_imm(rsrc_t rs, int voff, int soff) {
  asm volatile("buffer_load_dwordx4 a[%c3:%c4], %0, %1, %2 offen offset:%c5 sc1"
               :: "v"(voff), "s"(rs), "s"(soff), "i"(R), "i"(R + 3), "i"(IMM) : "memory");
}

constexpr int XS_STEP = 32 * LP * 2;
DEV int xs_voff(int w, int lane) {
  const int fsw = ((lane >> 4) & 1) | ((w & 1) << 1);
  return (w * 8 + (lane >> 3)) * (LP * 2) + ((((lane & 7) >> 1) ^ fsw) * 32) + (lane & 1) * 16;
}
template <int KS>
DEV void xs_dma(rsrc_t rs, int voff, int sbase, int ldsw) {
  int tmp;
  asm volatile("s_add_u32 m0, %4, %c5\n\ts_add_u32 %0, %3, %c6\n\tbuffer_load_dwordx4 %1, %2, %0 offen sc1 lds"
               : "=&s"(tmp) : "v"(voff), "s"(rs), "s"(sbase), "s"(ldsw), "i"(LD_XT + KS * 4096), "i"(KS * XS_STEP) : "memory", "scc");
}

__global__ __launch_bounds__(256, 1) void main_kernel(
    const u16* __restrict__ x0, const u16* __restrict__ Are, const u16* __restrict__ Aim,
    const u16* __restrict__ Bre, const u16* __restrict__ Bim, const u16* __restrict__ Cre,
    const u16* __restrict__ Cim, const u16* __restrict__ logdt, const u16* __restrict__ dskip,
    const u16* __restrict__ wg, const u16* __restrict__ wu, const u16* __restrict__ wd,
    char* __restrict__ ws, u16* __restrict__ out) {
  extern __shared__ __attribute__((aligned(16))) char smem[];
  const int w_ = __builtin_amdgcn_readfirstlane(threadIdx.x >> 6);
  const int b_ = blockIdx.x;

  const rsrc_t rs = __builtin_amdgcn_make_buffer_rsrc(ws, 0, WS_END, 0x00020000);
  STAMP_DECL
  STAMPE();
  asm volatile("" ::: ACLOB);
  u32 cpf;
  {
    unsigned long long pc; int len;
    asm volatile("s_getpc_b64 %0\n.Lmk_code_begin:\n\ts_mov_b32 %1, .Lmk_code_end-.Lmk_code_begin" : "=s"(pc), "=s"(len));
    const int off = min((int)((min(threadIdx.x & 63, 4u) * 4 + (threadIdx.x >> 6)) * 32 + (blockIdx.x >> 3)) * 128, len - 4) & ~3;
    cpf = *(const GAS u32*)(pc + (unsigned long long)(u32)off);
    SB();
    asm volatile("" :: "s"(x0), "s"(Are), "s"(Aim), "s"(Bre), "s"(Bim), "s"(Cre), "s"(Cim), "s"(logdt), "s"(dskip), "s"(wg), "s"(wu), "s"(wd), "s"(ws));
  }
#define MAPG1 1
  const int rt_ = ((b_ & 7) >> 2) * 8 + ((b_ >> 3) & 7), cb_ = (b_ & 3) * 4 + (b_ >> 6);
  const int rt2_ = (b_ & 7) * 4 + ((b_ >> 3) & 3), ct_ = b_ >> 5;
  const int start_ = cb_ * 10 + (cb_ < 12 ? cb_ : 12), cnt_ = cb_ < 12 ? 11 : 10;
  const int nbase_ = (w_ < 3) ? w_ * 3 : cnt_ - 2;
  const int nt0_ = start_ + nbase_;
  const int ok2_ = __builtin_amdgcn_readfirstlane((w_ < 3) && (nbase_ + 2 < cnt_ - 2));
#define ROLES(LANE) \
  const int w = w_, b = b_, rt = rt_, rt2 = rt2_, ct = ct_, nt0 = nt0_, ok2 = ok2_; \
  const int lane = LANE, tid = w * 64 + lane, c16 = lane & 15, kq = lane >> 4, wvo = lane * 16; \
  const int x1_off = OFF_X1 + layer * ACT2, h_off = OFF_H + layer * HID2; \
  const int hb_e = h_off + (rt2 * 2 * NT1 * 16 + w * 11 * 64) * 16, hb_l = hb_e + 64 * NT1 * 256; \
  const int s10 = (w == 3) ? 9 * 1024 : 10 * 1024; \
  (void)b; (void)rt; (void)ct; (void)nt0; (void)ok2; (void)tid; (void)c16; (void)kq; (void)wvo; (void)x1_off; (void)hb_e; (void)hb_l; (void)s10;

  u32 dsk;
  const int ldsb = (int)(u32)(size_t)(LAS char*)smem, ldsw = ldsb + w_ * 1024;
  {
    constexpr int layer = 0;
    ROLES(fresh_lane())
    const int d0 = 2 * b;
    u32x4 xf[4], xt[2], wr[4];
    const bool initw = (b == 0) && (w == 3);
    const int init_off = OFF_INIT + (b & 7) * 256;
    const u32 f0 = __builtin_amdgcn_raw_buffer_load_b32(rs, 0, init_off, SC1);
    __builtin_amdgcn_raw_buffer_store_b32(0u, rs, initw ? lane * 256 : OOB, OFF_CA + 6 * 32768, SC1);
    {
      const int ln = b * 4 + lane;
      const int vo = initw ? OFF_CA + 6 * 32768 + 16384 + lane * 256 : (w == 1 && lane < 4 && ln < 832) ? (ln < 768 ? ln : ln + 128) * 256 : OOB;
      __builtin_amdgcn_raw_buffer_store_b32(0u, rs, vo, 0, SC1);
    }
    SB();
    {
      const rsrc_t rx = __builtin_amdgcn_make_buffer_rsrc((void*)x0, 0, L * D * 2, 0x00020000);
      const rsrc_t rg = __builtin_amdgcn_make_buffer_rsrc((void*)wg, 0, D * F * 2, 0x00020000);
      const rsrc_t ru = __builtin_amdgcn_make_buffer_rsrc((void*)wu, 0, D * F * 2, 0x00020000);
      const int tvo = (lane >> 1) * (D * 2) + (lane & 1) * 16, tso = (rt2 * 64 * D + ct * 64 + w * 16) * 2;
      xt[0] = __builtin_amdgcn_raw_buffer_load_b128(rx, tvo, tso, 0);
      xt[1] = __builtin_amdgcn_raw_buffer_load_b128(rx, tvo, tso + 32 * D * 2, 0);
      const int gvo = kq * (F * 2) + (c16 < cnt_ ? c16 : 0) * 16, gso = ((rt * 32 + w * 8) * F + start_ * 8) * 2;
      wr[0] = __builtin_amdgcn_raw_buffer_load_b128(rg, gvo, gso, 0);
      wr[1] = __builtin_amdgcn_raw_buffer_load_b128(ru, gvo, gso, 0);
      wr[2] = __builtin_amdgcn_raw_buffer_load_b128(rg, gvo, gso + 4 * F * 2, 0);
      wr[3] = __builtin_amdgcn_raw_buffer_load_b128(ru, gvo, gso + 4 * F * 2, 0);
      const int fvo = (c16 & 7) * (D * 2) + kq * 16, fso = (b * 8 * D + w * 128) * 2;
#pragma unroll
      for (int i = 0; i < 4; ++i) xf[i] = __builtin_amdgcn_raw_buffer_load_b128(rx, fvo + i * 64, fso, 0);
    }
    SB();
    STAMP();
    {
      u32x4 z = splat4(0u);
      if (tid < 16) *(u32x4*)(smem + (tid >> 1) * KCOPY + (tid & 1) * 16) = z;
      if (tid >= 64 && tid < 128) *(u32x4*)(smem + LD_U + (lane >> 5) * UCH + (lane & 31) * 16) = z;
      if (tid == 128) *(u32x4*)(smem + LD_KT + 760 * 16) = z;
    }
    int ix[12];
#pragma unroll
    for (int i = 0; i < 12; i += 2) {
      const int l0 = tid * 8 - 4 + i;
      const u32 bits = pk_bf16((float)l0, (float)(l0 + 1));
      const int i0 = l0 < 256 ? (l0 < 0 ? 676 : l0) : (int)(bits & 0xffffu) - 17024;
      const int i1 = l0 < 256 ? (l0 < 0 ? 676 : l0 + 1) : (int)(bits >> 16) - 17024;
      ix[i] = ldsb + LD_KT + (i0 + (i0 >> 3)) * 16; ix[i + 1] = ldsb + LD_KT + (i1 + (i1 >> 3)) * 16;
      asm volatile("" : "+v"(ix[i]), "+v"(ix[i + 1]));
    }
    SB();
    if (initw) { asm volatile("s_waitcnt vmcnt(10)" ::: "memory"); init_bump(rs); }
    const u32 pn = b * (2 * NS) + tid;
    const u32 p_ar = Are[pn], p_ai = Aim[pn], p_br = Bre[pn], p_bi = Bim[pn];
    SB();
    const int ktw = ldsb + LD_KT + (w & 1) * 8 + (w >> 1) * 4;
    int kx[2][2][4];
#pragma unroll
    for (int c = 0; c < 2; ++c) {
      const int B = 16 * c + c16;
#pragma unroll
      for (int i = 0; i < 4; ++i) {
        const int i0 = kq < 2 ? 8 * B + 4 * kq + i : kq == 2 ? 256 + 4 * B + i : 384 + (i >> 1) * 64 + 2 * B + (i & 1);
        const int i1 = kq == 0 ? 512 + 32 * i + B : (kq == 1 && i == 0) ? 640 + B : 680;
        kx[0][c][i] = ktw + (i0 + (i0 >> 3)) * 16; kx[1][c][i] = ktw + (i1 + (i1 >> 3)) * 16;
        asm volatile("" : "+v"(kx[0][c][i]), "+v"(kx[1][c][i]));
      }
    }
    SB();
    const u32 p_cr = Cre[pn], p_ci = Cim[pn];
    const u32 p_ld = logdt[(u32)(d0 + (tid >> 7))];
    dsk = ((const u32*)dskip)[(u32)(b + (lane >> 6))];
    const int chunk = b + 256 * (w ^ 2);
    u32x4 w2r[4];
    {
      const rsrc_t rwd = __builtin_amdgcn_make_buffer_rsrc((void*)wd, 0, F * D * 2, 0x00020000);
#pragma unroll
      for (int i = 0; i < 4; ++i)
        w2r[i] = __builtin_amdgcn_raw_buffer_load_b128(rwd, (lane >> 3) * (D * 2) + (lane & 7) * 16, ((chunk >> 3) * 32 * D + (chunk & 7) * 64 + i * 8 * D) * 2, 0);
    }
    SB();
    STAMP();
    {
      char* img = smem + LD_Y + w * YW;
      *(u32x4*)(img + wvo) = xt[0];
      *(u32x4*)(img + 1024 + wvo) = xt[1];
      const char* tp = img + (kq * 8 + (c16 >> 2)) * 32 + (c16 & 3) * 8;
      u32x4 t0 = tr_frag(tp, 0, 128), t1 = tr_frag(tp, 1024, 1024 + 128);
      const int so = OFF_XT + ((ct * 64 + w * 16) * LP + rt2 * 64) * 2;
      st16(rs, c16 * (LP * 2) + kq * 16, so, t0);
      st16(rs, c16 * (LP * 2) + kq * 16 + 64, so, t1);
    }
    constexpr int WROW = 368;
    if (c16 < 11) {
      char* wdst = smem + LD_XT + (w * 8 + kq) * WROW + c16 * 32;
#pragma unroll
      for (int i = 0; i < 2; ++i) {
        const u32x4 g = wr[2 * i], u = wr[2 * i + 1];
        *(u32x4*)(wdst + i * 4 * WROW) = (u32x4){g[0], u[0], g[1], u[1]};
        *(u32x4*)(wdst + i * 4 * WROW + 16) = (u32x4){g[2], u[2], g[3], u[3]};
      }
    }
    {
      const u32x4 ones = splat4(0x3f803f80u);
      f32x4 sa, qa;
      static_for<4>([&](auto ic) { mfma_v<IC(ic) == 0>(sa, ones, xf[IC(ic)]); mfma_v<IC(ic) == 0>(qa, xf[IC(ic)], xf[IC(ic)]); });
      asm volatile("s_nop 7" : "+v"(sa), "+v"(qa));
      if ((c16 >> 2) == kq && c16 < 8) {
        const int r = c16 & 3;
        float q = r == 0 ? qa[0] : r == 1 ? qa[1] : r == 2 ? qa[2] : qa[3];
        *(f32x2*)(smem + LD_SS + (w * 8 + c16) * 8) = (f32x2){sa[0], q};
      }
    }
    bar();
    asm volatile("" :: "v"(cpf));
    if (initw) init_bump(rs);
    const int so01 = OFF_WGU + nt0 * KS1 * 1024, so2 = ok2 ? so01 + 2 * KS1 * 1024 : w == 3 ? OFF_WGU + NT1 * KS1 * 1024 : WS_END;
    {
      const char* wsrc = smem + LD_XT + (8 * kq + (c16 >> 2)) * WROW + nbase_ * 32 + (c16 & 3) * 8;
      u32x4 f0 = tr_frag(wsrc, 0, 4 * WROW), f1 = tr_frag(wsrc, 32, 32 + 4 * WROW), f2 = tr_frag(wsrc, 64, 64 + 4 * WROW);
      if (tid < 8) {
        const f32x2* sp = (const f32x2*)(smem + LD_SS) + tid;
        f32x2 v = (sp[0] + sp[8]) + (sp[16] + sp[24]);
        __builtin_amdgcn_raw_buffer_store_b64(__builtin_bit_cast(u32x2, v), rs, tid * 8, OFF_ST0 + b * 64, SC1);
      }
      st16(rs, wvo, so01 + rt * 1024, f0);
      st16(rs, wvo, so01 + (KS1 + rt) * 1024, f1);
      st16(rs, (w == 2 && lane < 8) ? wvo : OOB, OFF_AC + b * 128, splat4(0u));
      const u32x4 fillp = splat4(~0u);
      st16(rs, tid * 16, OFF_XT + ACT2 + b * 4 * LP, fillp);
      st16(rs, tid * 16, OFF_XT + ACT2 + b * 4 * LP + LP * 2, fillp);
      if (w == 3) f2 = splat4(0x3f803f80u);
      if (w != 3 || cb_ == 15) st16(rs, wvo, so2 + rt * 1024, f2);
    }
    SB();
    STAMP();
    PrepRegs<1> pe, pl;
    u32 pv, pv2;
    const u32 f1 = __builtin_amdgcn_raw_buffer_load_b32(rs, 0, init_off, SC1);
    SB();
    {
      constexpr int TROW = 272;
      char* tab = smem + LD_XT + 32768 - 4096 + w * (32 * TROW);
      char* tar = smem + LD_Y + w * YW;
      char* tar2 = smem + LD_W2 + w * 4096;
      const float dt = rbf(expf(lo_f(p_ld)));
      const float ar = dt * lo_f(p_ar), ai = dt * lo_f(p_ai), br = dt * lo_f(p_br), bi = dt * lo_f(p_bi);
      const float cr = lo_f(p_cr), ci = lo_f(p_ci);
      const float I2PI_HI = 0.15915494309189535f;
      const float I2PI_LO = (float)(0.15915494309189535 - (double)0.15915494309189535f);
      const float a2 = ar * 1.4426950408889634f;
      const float rh = ai * I2PI_HI;
      const float rl = __builtin_fmaf(ai, I2PI_LO, __builtin_fmaf(ai, I2PI_HI, -rh));
      auto cexp = [&](float l) {
        const float e = __builtin_amdgcn_exp2f(a2 * l);
        const float fr = __builtin_fmaf(l, rl, __builtin_amdgcn_fractf(rh * l));
        return (f32x2){__builtin_amdgcn_cosf(fr), __builtin_amdgcn_sinf(fr)} * e;
      };
      const float kr = cr * br - ci * bi, ki = cr * bi + ci * br;
      u32 mx = max(__float_as_uint(kr) & 0x7f800000u, __float_as_uint(ki) & 0x7f800000u);
      mx = max(mx, (u32)__builtin_amdgcn_update_dpp(0, (int)mx, 0xB1, 0xf, 0xf, true));
      mx = max(mx, (u32)__builtin_amdgcn_update_dpp(0, (int)mx, 0x4E, 0xf, 0xf, true));
      mx = max(mx, (u32)__builtin_amdgcn_update_dpp(0, (int)mx, 0x124, 0xf, 0xf, true));
      mx = max(mx, (u32)__builtin_amdgcn_update_dpp(0, (int)mx, 0x128, 0xf, 0xf, true));
      mx = max(max((u32)__builtin_amdgcn_readlane((int)mx, 0), (u32)__builtin_amdgcn_readlane((int)mx, 16)),
               max((u32)__builtin_amdgcn_readlane((int)mx, 32), (u32)__builtin_amdgcn_readlane((int)mx, 48)));
      mx = min(max(mx, 60u << 23), 200u << 23);
      const float up = __uint_as_float((267u << 23) - mx);
      const float kscale = __uint_as_float(mx - (27u << 23));
      const f32x2 e1 = cexp(1.f), e8 = cexp(8.f), e128 = cexp(128.f), e256 = cexp(256.f), e1024 = cexp(1024.f);
      const CM c8 = mkcm((f32x2){e8[0], -e8[1]}), c128 = mkcm((f32x2){e128[0], -e128[1]}), m1 = mkcm(e1), m256 = mkcm(e256);
      u32 hv;
      auto putr = [&](int r, u32 v) { *(u32*)((r < 16 ? tar + r * TROW : tar2 + (r - 16) * TROW) + lane * 4) = v; };
      auto putc = [&](int c, u32 v) { *(u32*)(tab + c * TROW + lane * 4) = v; };
      f32x2 p[8];
      p[0] = (f32x2){16384.f, 0.f}; putr(0, pk_f16(p[0]));
      p[1] = e1 * 16384.f; putr(1, pk_f16(p[1]));
#pragma unroll
      for (int i = 2; i < 8; ++i) { p[i] = cmul_h(p[i - 1], m1, hv); putr(i, hv); }
      f32x2 q0 = e256 * 16384.f; putr(8, pk_f16(q0));
      (void)cmul_h(p[2], m256, hv); putr(9, hv);
      f32x2 q4 = cmul_h(p[4], m256, hv); putr(10, hv);
      (void)cmul_h(p[6], m256, hv); putr(11, hv);
      q0 = cmul_h(q0, m256, hv); putr(12, hv);
      q4 = cmul_h(q4, m256, hv); putr(13, hv);
      (void)cmul_h(q0, m256, hv); putr(14, hv);
      (void)cmul_h(q4, m256, hv); putr(15, hv);
      q0 = e1024 * 16384.f; putr(16, pk_f16(q0));
#pragma unroll
      for (int i = 17; i < 21; ++i) { q0 = cmul_h(q0, m256, hv); putr(i, hv); }
      SB();
      STAMP();
      if (initw) init_bump(rs);
      init_wait(rs, init_off, f0, f1);
      drain_bar<0, false>();
      signal_all(rs, 6, b, w);
      SB();
      STAMP();
      u32x4 fa[4][2];
      {
        const char* ra0 = tar + c16 * TROW + kq * 16;
        const char* ra1 = tar2 + (c16 < 5 ? c16 : 0) * TROW + kq * 16;
#pragma unroll
        for (int s = 0; s < 4; ++s) { fa[s][0] = *(const u32x4*)(ra0 + s * 64); fa[s][1] = *(const u32x4*)(ra1 + s * 64); }
      }
      SB();
      f32x2 v0 = (f32x2){kr, -ki} * up; putc(0, pk_f16(v0));
      f32x2 v1 = cmul_h(v0, c128, hv); putc(16, hv);
#pragma unroll
      for (int i = 1; i < 16; ++i) {
        v0 = cmul_h(v0, c8, hv); putc(i, hv);
        v1 = cmul_h(v1, c8, hv); putc(16 + i, hv);
      }
      f32x4 ka[2][2];
      const char* rb0 = tab + c16 * TROW + kq * 16;
      u32x4 fb[4][2];
#pragma unroll
      for (int s = 0; s < 4; ++s) { fb[s][0] = *(const u32x4*)(rb0 + s * 64); fb[s][1] = *(const u32x4*)(rb0 + 16 * TROW + s * 64); }
      SB();
      static_for<4>([&](auto sc) {
        constexpr int s = IC(sc);
        mfma_h<s == 0>(ka[0][0], fa[s][0], fb[s][0]); mfma_h<s == 0>(ka[0][1], fa[s][0], fb[s][1]);
        mfma_h<s == 0>(ka[1][0], fa[s][1], fb[s][0]); mfma_h<s == 0>(ka[1][1], fa[s][1], fb[s][1]);
      });
      pv = poll_ld(rs, all_off(6, b), true, w);
      mfma_done4(ka[0][0], ka[0][1], ka[1][0], ka[1][1]);
#pragma unroll
      for (int r = 0; r < 2; ++r)
#pragma unroll
        for (int c = 0; c < 2; ++c) {
          const f32x4 v = ka[r][c] * kscale;
#pragma unroll
          for (int i = 0; i < 4; ++i) *(LAS float*)(size_t)(u32)kx[r][c][i] = v[i];
        }
    }
    bar();
    STAMP();
    {
      f32x4 tv[12];
#pragma unroll
      for (int i = 0; i < 12; ++i) tv[i] = *(const LAS f32x4*)(size_t)(u32)ix[i];
      SB();
      u32 e[2][6];
#pragma unroll
      for (int i = 0; i < 6; ++i) {
        const f32x2 a = tv[2 * i].xy + tv[2 * i].zw, c = tv[2 * i + 1].xy + tv[2 * i + 1].zw;
        e[0][i] = pk_bf16(a[0], c[0]);
        e[1][i] = pk_bf16(a[1], c[1]);
      }
#pragma unroll
      for (int c = 0; c < 2; ++c) {
        char* kb = smem + c * KCH + tid * 16 + 32;
        u32 o[5];
#pragma unroll
        for (int i = 0; i < 5; ++i) o[i] = __builtin_amdgcn_alignbit(e[c][i + 1], e[c][i], 16);
        *(u32x4*)(kb) = (u32x4){e[c][2], e[c][3], e[c][4], e[c][5]};
        *(u32x4*)(kb + KCOPY) = (u32x4){o[1], o[2], o[3], o[4]};
        *(u32x4*)(kb + 2 * KCOPY) = (u32x4){e[c][1], e[c][2], e[c][3], e[c][4]};
        *(u32x4*)(kb + 3 * KCOPY) = (u32x4){o[0], o[1], o[2], o[3]};
      }
    }
    SB();
    if (chunk < KS2P * 8) {
      char* img = smem + LD_W2 + 16384 + w * 4096;
#pragma unroll
      for (int i = 0; i < 4; ++i)
        *(u32x4*)(img + i * 1024 + (lane >> 3) * 128 + ((((lane & 7) >> 1) ^ (((lane >> 4) & 1) | ((i & 1) << 1))) * 32) + (lane & 1) * 16) = w2r[i];
      const int fr = ((c16 >> 3) & 1) | ((kq & 1) << 1);
#pragma unroll
      for (int i = 0; i < 4; ++i)
        st16(rs, wvo, OFF_WD + (((chunk & 7) * 4 + i) * KS2P + (chunk >> 3)) * 1024,
             tr_frag(img + (8 * kq + (c16 >> 2)) * 128 + ((i ^ fr) * 32) + (c16 & 3) * 8, 0, 512));
    }
    SB();
    STAMP();
    if (initw) {
#pragma unroll 1
      for (int spins = 0; spins < SPIN_CAP && !__all(pv >= 16u); ++spins) { init_bump(rs); pv = poll_ld(rs, all_off(6, b), true, w); }
    }
    poll_wait(rs, all_off(6, b), true, pv, w);
    prep_load<1>(rs, tid, OFF_XT + d0 * LP * 2, OFF_ST0, pe);
    prep_load<1>(rs, tid, OFF_XT + d0 * LP * 2 + 2048, OFF_ST0 + 8192, pl);
    STAMP();
    f32x4 ze[2], zl[2];
    prep_ln(smem, 0, tid, dsk, pe, ze);
    asm volatile("" : "+v"(pl.pa[0]), "+v"(pl.pb[0]), "+v"(pl.xv[0]), "+v"(pl.xv[1]));
    SB();
    static_for<4>([&](auto ic) { ld_w1<IC(ic)>(rs, wvo, so01, so2); });
    bar();
    STAMP();
    f32x4 acc[4];
    conv_mfma<0, 0, 3, true>(smem, w, lane, acc, [&](auto nc) {
      constexpr int n = IC(nc);
      if constexpr (n % 3 == 0 && n < 36) ld_w1<4 + n / 3>(rs, wvo, so01, so2);
    });
    conv_out(rs, smem, OFF_X1 + d0 * LP * 2, w, lane, acc, ze);
    STAMP();
    static_for<4>([&](auto ic) { ld_w1<16 + IC(ic)>(rs, wvo, so01, so2); });
    prep_ln(smem, 1, tid, dsk, pl, zl);
    SB();
    static_for<4>([&](auto ic) { ld_w1<20 + IC(ic)>(rs, wvo, so01, so2); });
    drain_bar<8, true>();
    signal_all(rs, 0, b, w);
    conv_mfma<4, 0, 7, true>(smem, w, lane, acc, [&](auto nc) {
      constexpr int n = IC(nc);
      if constexpr (n % 4 == 0 && n < 96) ld_w1<24 + n / 4>(rs, wvo, so01, so2);
    });
    STAMP();
    pv = poll_ld(rs, all_off(0, b), true, w);
    conv_out(rs, smem, OFF_X1 + d0 * LP * 2 + 2048, w, lane, acc, zl);
    pv2 = poll_ld(rs, all_off(0, b), true, w);
    STAMP();
    SB();
    asm volatile("s_waitcnt vmcnt(3)" ::: "memory");
    poll_wait2(rs, all_off(0, b), true, pv, pv2, w);
    STAMP();
    {
      const int gx = xs_voff(w, lane), gs = OFF_X1 + rt * 128;
      const u32x4 nones = splat4(0xbf80bf80u);
      f32x4 c[3];
      static_for<48>([&](auto ic) {
        mfma_aA<4 * IC(ic), (IC(ic) < 3)>(c[IC(ic) % 3], nones); SB();
        if constexpr (IC(ic) < 16 && !(IC(ic) & 1)) { xs_dma<IC(ic) / 2>(rs, gx, gs, ldsw); SB(); }
      });
      asm volatile("s_nop 7" : "+v"(c[0]), "+v"(c[1]), "+v"(c[2]));
#pragma unroll
      for (int i = 0; i < 3; ++i) *(f32x4*)(smem + LD_CS + w * 192 + i * 64 + kq * 16) = c[i];
    }
    drain_bar<0, true>();
    signal_all(rs, 1, b, w);
    STAMP();
  }
  const int lane_g = fresh_lane();
  static_for<2>([&](auto lc) {
    constexpr int layer = IC(lc);
    AccRegs pr;
    bool rdy = false;
    u32x4 W2[5][4];
    u32x4 hf[11][2];
    {
      ROLES(lane_g)
      int sw2[4];
#pragma unroll
      for (int i = 0; i < 4; ++i) sw2[i] = OFF_WD + ((ct * 4 + ((w + i) & 3)) * KS2P + w * 11) * 1024;
      const int fr = ((c16 >> 3) & 1) | ((kq & 1) << 1);
      const char* xr[4];
#pragma unroll
      for (int j = 0; j < 4; ++j) xr[j] = smem + LD_XT + (8 * kq + (c16 >> 2)) * 128 + ((((w + j) & 3) ^ fr) * 32) + (c16 & 3) * 8;
      const int gx = xs_voff(w, lane), gs = x1_off + rt * 128;
      u32x4 xs[8];
      char* ssq = smem + ((c16 >> 2) == kq ? LD_SS + w * 128 : LD_Y) + c16 * 8 + 4;
      char* sss = smem + (w == 3 ? LD_SS : LD_Y) + c16 * 8;
      u32x2 xb[2][4][2];
#pragma unroll
      for (int j = 0; j < 4; ++j) { xb[0][j][0] = tr_half(xr[j]); xb[0][j][1] = tr_half(xr[j] + 512); }
      SB();
      static_for<2>([&](auto hc) {
        constexpr int h = IC(hc);
        f32x4 ga[3][4], qa;
        const int nxt_poll = h == 0 ? all_off(layer * 4 + 1, b) : grp_off(layer * 32 + (rt2 >> 1));
        u32 pv, pv2;
        {
          auto fill = [&](auto gc) {
            constexpr int g = IC(gc);
            if constexpr (g < 8) xs_dma<8 + g>(rs, gx, gs + h * 2048, ldsw);
            if constexpr (g == (h == 0 ? SIG_CL : SIG_HE) && (h == 1 || layer == 1)) {
              drain_bar<8, false>();
              if constexpr (h == 1) signal_grp(rs, layer * 32 + rt, w); else signal_all(rs, layer * 4 + 1, b, w);
            }
            if constexpr (g == 27) drain_bar<(h == 0 && layer == 0) ? 16 : (h == 1 ? 7 : 0), true>();
            if constexpr (h == 0 && layer == 0) {
              if constexpr (g >= 10 && g < 26) ld_agpr_imm<192 + 16 * ((g - 10) & 3) + 4 * ((g - 10) >> 2), ((g - 10) & 3) * 1024>(rs, wvo, sw2[(g - 10) >> 2]);
              if constexpr (g >= 28 && g < 36) xs[g - 28] = ld16(rs, wvo + ((g - 28) >> 2) * 1024, sw2[(g - 28) & 3] + 4096);
              if constexpr (g >= 43 && g < 51) *(u32x4*)(smem + LD_W2 + w * 8192 + (g - 43) * 1024 + wvo) = xs[g - 43];
            }
            if constexpr (h == 1) {
              if constexpr (g >= 14 && g < 47 && (g > 27 || !(g & 1)) && (g < 34 || !(g & 1))) {
                constexpr int n = g < 28 ? (g - 14) / 2 : g < 34 ? g - 21 : 13 + (g - 34) / 2, i = n / 5, s = n % 5;
                W2[s][i] = ld16(rs, wvo + (s & 3) * 1024, sw2[i] + (6 + (s >> 2) * 4) * 1024);
              }
            }
            if constexpr (g == POLL_G - 12) pv = poll_ld(rs, nxt_poll, h == 0, w);
            if constexpr (g == POLL_G - 5) pv2 = poll_ld(rs, nxt_poll, h == 0, w);
            if constexpr (g == POLL_G) poll_wait2(rs, nxt_poll, h == 0, pv, pv2, w);
            if constexpr (g >= POLL_G && g < POLL_G + 8) {
              if constexpr (h == 0) xs_dma<g - POLL_G>(rs, gx, gs + 2048, ldsw);
              else hf[g - POLL_G][0] = ld16(rs, wvo + ((g - POLL_G) & 3) * 1024, hb_e + ((g - POLL_G) >> 2) * 4096);
            }
          };
          static_for<60>([&](auto gc) {
            constexpr int g = IC(gc), ks = g >> 2, j = g & 3;
            const u32x4 x = {xb[ks & 1][j][0][0], xb[ks & 1][j][0][1], xb[ks & 1][j][1][0], xb[ks & 1][j][1][1]};
            mfma_aA<12 * ks, ks == 0>(ga[0][j], x);
            xb[(ks + 1) & 1][j][0] = tr_half(xr[j] + (ks + 1) * 4096);
            SB();
            mfma_aA<12 * ks + 4, ks == 0>(ga[1][j], x);
            xb[(ks + 1) & 1][j][1] = tr_half(xr[j] + (ks + 1) * 4096 + 512);
            SB();
            mfma_aA<12 * ks + 8, ks == 0>(ga[2][j], x);
            SB();
            fill(gc);
            SB();
            if constexpr (j == 0) { mfma_v<ks == 0>(qa, x, x); SB(); }
          });
          u32x4 x[4];
#pragma unroll
          for (int j = 0; j < 4; ++j) x[j] = (u32x4){xb[1][j][0][0], xb[1][j][0][1], xb[1][j][1][0], xb[1][j][1][1]};
          mfma_vv(qa, x[0], x[0]); SB();
#pragma unroll
          for (int j = 0; j < 4; ++j) { mfma_aA<188, false>(ga[2][j], x[j]); SB(); }
#pragma unroll
          for (int j = 0; j < 4; ++j) { mfma_aA<180, false>(ga[0][j], x[j]); SB(); }
          asm volatile("" : "+v"(qa));
          {
            const int r = c16 & 3;
            const float q01 = (r & 1) ? qa[1] : qa[0], q23 = (r & 1) ? qa[3] : qa[2];
            SB();
            mfma_aA<184, false>(ga[1][0], x[0]); SB();
            const float q = (r & 2) ? q23 : q01;
            *(float*)ssq = q;
            SB();
            mfma_aA<184, false>(ga[1][1], x[1]); SB();
            tie4(ga[2][0], ga[2][1], ga[2][2], ga[2][3]);
            *(float*)(sss + 384) = ga[2][0][0];
            *(float*)(sss) = ga[2][1][0];
            SB();
            mfma_aA<184, false>(ga[1][2], x[2]); SB();
            *(float*)(sss + 128) = ga[2][2][0];
            *(float*)(sss + 256) = ga[2][3][0];
            SB();
            mfma_aA<184, false>(ga[1][3], x[3]); SB();
          }
        }
        STAMP();
        f32x4 cs[3];
#pragma unroll
        for (int i = 0; i < 3; ++i) cs[i] = *(const f32x4*)(smem + LD_CS + w * 192 + i * 64 + kq * 16);
        bar();
        f32x2 st[4];
#pragma unroll
        for (int j = 0; j < 4; ++j) st[j] = *(const f32x2*)(smem + LD_SS + (((w + j) & 3) * 16 + c16) * 8);
        tie4(ga[0][0], ga[0][1], ga[0][2], ga[0][3]);
        tie4(ga[1][0], ga[1][1], ga[1][2], ga[1][3]);
        tie4(ga[2][0], ga[2][1], ga[2][2], ga[2][3]);
        SB();
        u32 hd[3][4];
#pragma unroll
        for (int j = 0; j < 4; ++j) {
          const f32x2 mv = ln_mv(st[j]);
          const float nl = __builtin_amdgcn_rsqf(mv[1]) * -1.4426950408889634f;
#pragma unroll
          for (int i = 0; i < 3; ++i) {
            const f32x4 v = __builtin_elementwise_fma(cs[i], (f32x4){mv[0], mv[0], mv[0], mv[0]}, ga[i][j]);
            const f32x2 g = {v[0], v[1]}, u = {v[2], v[3]};
            const f32x2 a = g * nl;
            const f32x2 e = {__builtin_amdgcn_exp2f(a[0]), __builtin_amdgcn_exp2f(a[1])};
            const f32x2 d = __builtin_elementwise_fma(e, (f32x2){mv[1], mv[1]}, (f32x2){mv[1], mv[1]});
            const f32x2 r = {__builtin_amdgcn_rcpf(d[0]), __builtin_amdgcn_rcpf(d[1])};
            hd[i][j] = pk_bf16((g * u) * r);
            if constexpr (h == 1) {
              const int t = j * 3 + i;
              if (t < 3) hf[8 + t][0] = ld16(rs, wvo, hb_e + (t == 2 ? s10 : (8 + t) * 1024));
              else hf[t - 3][1] = ld16(rs, wvo + ((t - 3) & 3) * 1024, hb_e + NT1 * 256 + ((t - 3) >> 2) * 4096);
            }
          }
          if constexpr (h == 1) SB();
        }
        const int hb = (((w + kq) & 3) * NT1 * 16 + c16) * 16, hs = h_off + ((h * 64 + rt * 4) * NT1 + nt0) * 256;
#pragma unroll
        for (int i = 0; i < 3; ++i) {
          auto a02 = __builtin_amdgcn_permlane32_swap(hd[i][0], hd[i][2], false, false);
          auto a13 = __builtin_amdgcn_permlane32_swap(hd[i][1], hd[i][3], false, false);
          auto o01 = __builtin_amdgcn_permlane16_swap(a02[0], a13[0], false, false);
          auto o23 = __builtin_amdgcn_permlane16_swap(a02[1], a13[1], false, false);
          u32x4 v = {o01[0], o01[1], o23[0], o23[1]};
          st16(rs, (i < 2 || ok2) ? hb + i * 256 : OOB, hs, v);
        }
        SB();
        if constexpr (h == 0) {
          drain_bar<3, false>();
#pragma unroll
          for (int j = 0; j < 4; ++j) { xb[0][j][0] = tr_half(xr[j]); xb[0][j][1] = tr_half(xr[j] + 512); }
        }
        SB();
        STAMP();
      });
    }
    {
      ROLES(fresh_lane())
      char* rw[3];
#pragma unroll
      for (int i = 0; i < 3; ++i) rw[i] = smem + LD_XT + (((w + 1 + i) & 3) * 3 + i) * 2048 + wvo;
      const char* rb = smem + LD_XT + w * 6144 + wvo;
      const int rvo = c16 * (LP * 2) + kq * 8;
      const int ovo = c16 * (LP * 2) + (kq & 1) * 32 + (kq >> 1) * 16;
      char* ow = smem + LD_OT + (w * 16 + c16) * 64 + (kq & 1) * 32 + (kq >> 1) * 16;
      const char* ob = smem + LD_OT + (8 * kq + (c16 >> 2)) * 64 + (w & 1) * 32 + (c16 & 3) * 8;
      const char* wlp = smem + LD_W2 + w * 8192 + wvo;
      static_for<2>([&](auto hc) {
        constexpr int h = IC(hc);
        const int tok0 = h * 1024 + rt2 * 32;
        const int eso = ((ct * 64 + w * 16) * LP + tok0) * 2;
        const int hb = h ? hb_l : hb_e;
        f32x4 da[4][2];
        u32x4 wl[2][4];
        u32x2 res[2];
        u32 pv, pv2;
        static_for<88>([&](auto nc) {
          constexpr int n = IC(nc), j = n / 44, s = (n % 44) >> 2, i = (n + 1) & 3;
          if constexpr (s < 4) mfma_aB<192 + 16 * s + 4 * i, s == 0>(da[i][j], hf[s][j]);
          else if constexpr (s < 6) mfma_vv(da[i][j], hf[s][j], wl[s - 4][i]);
          else mfma_vv(da[i][j], hf[s][j], W2[s - 6][i]);
          SB();
          if constexpr (h == 0) {
            if constexpr (n == 3 || n == 7) hf[9 + (n >> 2)][1] = ld16(rs, wvo, hb + NT1 * 256 + (n == 7 ? s10 : 9 * 1024));
            if constexpr (n == 10) res[0] = ld8(rs, rvo, x1_off + eso);
            if constexpr (n == 13) res[1] = ld8(rs, rvo + 32, x1_off + eso);
            if constexpr (n == SIG_HL) { drain_bar<4, false>(); signal_grp(rs, layer * 32 + 16 + rt, w); }
            if constexpr (n == 86) pv = poll_ld(rs, grp_off(layer * 32 + 16 + (rt2 >> 1)), false, w);
          } else {
            if constexpr (n < 33 && n % 3 == 2) hf[n / 3][1] = ld16(rs, wvo + (n / 3 < 8 ? ((n / 3) & 3) * 1024 : 0), hb + NT1 * 256 + (n / 3 < 8 ? (n / 12) * 4096 : n / 3 == 10 ? s10 : (n / 3) * 1024));
            if constexpr (layer == 0 && n == SIG_XE) { drain_bar<(SIG_XE + 1) / 3, false>(); signal_all(rs, 2, b, w); }
            if constexpr (n == 35) res[0] = ld8(rs, rvo, x1_off + eso);
            if constexpr (n == 38) res[1] = ld8(rs, rvo + 32, x1_off + eso);
            if constexpr (layer == 0 && n >= ACC_E && n < ACC_E + 12 && !((n - ACC_E) & 1)) acc_ld1(rs, (n - ACC_E) >> 1, tid, OFF_XT + ACT2 + b * 2 * LP * 2, 0, pr);
          }
          if constexpr (n % 44 >= 6 && n % 44 < 14) wl[(n % 44 - 6) >> 2][(n % 44 - 5) & 3] = *(const u32x4*)(wlp + (((n % 44 - 6) & 4) + ((n % 44 - 5) & 3)) * 1024);
          if constexpr (n == 52 || n == 56 || n == 60) { asm volatile("" : "+v"(da[(n - 48) >> 2][0])); *(f32x4*)(rw[(n - 52) >> 2]) = da[(n - 48) >> 2][0]; }
          SB();
        });
        mfma_done4(da[0][1], da[1][1], da[2][1], da[3][1]);
        SB();
        STAMP();
#pragma unroll
        for (int i = 0; i < 3; ++i) *(f32x4*)(rw[i] + 1024) = da[i + 1][1];
        if constexpr (h == 0) pv2 = poll_ld(rs, grp_off(layer * 32 + 16 + (rt2 >> 1)), false, w);
        bar();
        u32x2 xo[2];
#pragma unroll
        for (int j = 0; j < 2; ++j) {
          const f32x4 rf = {lo_f(res[j][0]), hi_f(res[j][0]), lo_f(res[j][1]), hi_f(res[j][1])};
          const f32x4 v = ((da[0][j] + rf) + *(const f32x4*)(rb + j * 1024)) + (*(const f32x4*)(rb + 2048 + j * 1024) + *(const f32x4*)(rb + 4096 + j * 1024));
          xo[j] = (u32x2){pk_bf16(v[0], v[1]), pk_bf16(v[2], v[3])};
        }
        auto s0 = __builtin_amdgcn_permlane16_swap(xo[0][0], xo[1][0], false, false);
        auto s1 = __builtin_amdgcn_permlane16_swap(xo[0][1], xo[1][1], false, false);
        const u32x4 xv = {s0[0], s1[0], s0[1], s1[1]};
        *(u32x4*)ow = xv;
        if constexpr (layer == 0 && h == 1) { SB(); rdy = acc_ready(pr); SB(); }
        if constexpr (layer == 0) st16(rs, ovo, OFF_XT + ACT2 + eso, xv);
        SB();
        if constexpr (h == 0) {
          poll_wait2(rs, grp_off(layer * 32 + 16 + (rt2 >> 1)), false, pv, pv2, w);
#pragma unroll
          for (int s = 0; s < 6; ++s) hf[s][0] = ld16(rs, wvo + (s & 3) * 1024, hb_l + (s >> 2) * 4096);
        }
        SB();
        bar();
        if constexpr (h == 0) {
#pragma unroll
          for (int s = 6; s < 11; ++s) hf[s][0] = ld16(rs, wvo + (s < 8 ? (s & 3) * 1024 : 0), hb_l + (s < 8 ? 4096 : s == 10 ? s10 : s * 1024));
          SB();
        }
        if constexpr (layer == 0) {
          if (w < 2) {
            u32x4 f0 = tr_frag(ob, 0, 256), f1 = tr_frag(ob, 2048, 2048 + 256);
            const u32x4 ones = splat4(0x3f803f80u);
            f32x4 sa, qa;
            mfma_v0(sa, ones, f0); mfma_v0(qa, f0, f0); mfma_vv(sa, ones, f1); mfma_vv(qa, f1, f1);
            asm volatile("s_nop 7" : "+v"(sa), "+v"(qa));
            const int r = c16 & 3;
            float q = r == 0 ? qa[0] : r == 1 ? qa[1] : r == 2 ? qa[2] : qa[3];
            u32x2 v = {__float_as_uint(sa[0]), __float_as_uint(q)};
            const bool diag = (c16 >> 2) == kq;
            const bool fits = q < 0x1p29f;
            const u32x2 fs = to_fix(fits ? sa[0] * 0x1p30f : 0.f), fq = to_fix(fits ? q * 0x1p26f : 0x1p59f);
            const int avo = diag ? (c16 & 3) * 4096 + kq * 16 : OOB, aso = OFF_AC + h * 16384 + (rt2 * 8 + w * 4) * 16;
            asm volatile("s_nop 4\n\tbuffer_atomic_add_x2 %0, %2, %3, %4 offen sc1\n\tbuffer_atomic_add_x2 %1, %2, %3, %4 offen offset:8 sc1"
                         :: "v"(fs), "v"(fq), "v"(avo), "s"(rs), "s"(aso) : "memory");
            __builtin_amdgcn_raw_buffer_store_b64(v, rs, diag ? c16 * 8 : OOB, OFF_ST1 + (ct * L + tok0 + w * 16) * 8, SC1);
          }
        } else {
          u32x4 f = tr_frag(ob, (w >> 1) * 2048, (w >> 1) * 2048 + 256);
          const rsrc_t ro = __builtin_amdgcn_make_buffer_rsrc(out, 0, L * D * 2, 0x00020000);
          __builtin_amdgcn_raw_buffer_store_b128(f, ro, (c16 * D + kq * 8) * 2, ((tok0 + (w & 1) * 16) * D + ct * 64 + (w >> 1) * 32) * 2, 0);
        }
        SB();
        STAMP();
      });
    }
    if constexpr (layer == 0) {
      ROLES(fresh_lane())
      const int xo = b * 2 * LP * 2;
      f32x4 z[2], acc[4];
      u32 pv, pv2;
      acc_wait(rs, tid, OFF_XT + ACT2 + xo, 0, all_off(2, b), pr, rdy);
      STAMP();
      acc_ln(rs, smem, 0, tid, dsk, pr, z);
      bar();
      STAMP();
      conv_mfma<0, 0, 3, true>(smem, w, lane, acc, [&](auto nc) { if constexpr (IC(nc) == 30) { drain_bar<0, false>(); signal_all(rs, 3, b, w); } });
      conv_out(rs, smem, OFF_X1 + ACT2 + xo, w, lane, acc, z);
      STAMP();
      conv_mfma<4, 0, 3, true>(smem, w, lane, acc, [&](auto nc) {
        constexpr int n = IC(nc);
        if constexpr (n >= ACC_L && n < ACC_L + 12 && !((n - ACC_L) & 1)) acc_ld1(rs, (n - ACC_L) >> 1, tid, OFF_XT + ACT2 + xo + 2048, 1, pr);
        if constexpr (n == 36) { drain_bar<6, false>(); signal_all(rs, 4, b, w); }
      });
      STAMP();
      acc_wait(rs, tid, OFF_XT + ACT2 + xo + 2048, 1, all_off(3, b), pr, acc_ready(pr));
      acc_ln(rs, smem, 1, tid, dsk, pr, z);
      bar();
      STAMP();
      const int gx = xs_voff(w, lane), gs = OFF_X1 + ACT2 + rt * 128;
      conv_mfma<4, 4, 7, false>(smem, w, lane, acc, [&](auto nc) {
        constexpr int n = IC(nc);
        if constexpr (n == CE_P) pv = poll_ld(rs, all_off(4, b), true, w);
        if constexpr (n == CE_P + 14) pv2 = poll_ld(rs, all_off(4, b), true, w);
      });
      STAMP();
      poll_wait2(rs, all_off(4, b), true, pv, pv2, w);
      static_for<8>([&](auto ic) { xs_dma<IC(ic)>(rs, gx, gs, ldsw); });
      STAMP();
      conv_out(rs, smem, OFF_X1 + ACT2 + xo + 2048, w, lane, acc, z);
      drain_bar<1, false>();
      STAMP();
    }
  });
  STAMPE();
  STAMP_OUT();
  asm volatile(".Lmk_code_end:" ::: "memory");
}

extern "C" void kernel_launch(void* const* d_in, const int* in_sizes, int n_in, void* d_out, int out_size,
                              void* d_ws, size_t ws_size, hipStream_t stream) {
  char* ws = (char*)d_ws;
  (void)hipFuncSetAttribute((const void*)main_kernel, hipFuncAttributeMaxDynamicSharedMemorySize, LDS_BYTES + LDS_EXTRA);
  main_kernel<<<256, 256, LDS_BYTES + LDS_EXTRA, stream>>>(
      (const u16*)d_in[0], (const u16*)d_in[1], (const u16*)d_in[2], (const u16*)d_in[3], (const u16*)d_in[4],
      (const u16*)d_in[5], (const u16*)d_in[6], (const u16*)d_in[7], (const u16*)d_in[8], (const u16*)d_in[10],
      (const u16*)d_in[11], (const u16*)d_in[12], ws, (u16*)d_out);
}
```

```cpp
#include <hip/hip_runtime.h>
#include <stdint.h>
#include <type_traits>

typedef unsigned short u16;
typedef unsigned int u32;
typedef u32 u32x4 __attribute__((ext_vector_type(4)));
typedef u32 u32x2 __attribute__((ext_vector_type(2)));
typedef float f32x4 __attribute__((ext_vector_type(4)));
typedef float f32x2 __attribute__((ext_vector_type(2)));
typedef short s16x4 __attribute__((ext_vector_type(4)));
typedef __bf16 bf16x2 __attribute__((ext_vector_type(2)));
typedef _Float16 f16x2 __attribute__((ext_vector_type(2)));
#define LAS __attribute__((address_space(3)))
#define GAS __attribute__((address_space(1)))
#define DEV __device__ __forceinline__
#define SB() __builtin_amdgcn_sched_barrier(0)
#define IC(x) decltype(x)::value
typedef __amdgpu_buffer_rsrc_t rsrc_t;

constexpr int L = 2048, D = 512, NS = 128, F = 1376;
constexpr int NT1 = 172;
constexpr int KS1 = 16;
constexpr int KS2P = 44;
constexpr float EPS = 1e-5f;
constexpr int SC1 = 16;
constexpr int PUB_CL = 8;

constexpr int ACT = D * L * 2;
constexpr int HID = F * L * 2;
constexpr int N_ALL = 7;
constexpr int OFF_CA  = 0;
constexpr int OFF_CH  = OFF_CA + N_ALL * 128 * 256;
constexpr int CTRL_BYTES = OFF_CH + 64 * 256;
constexpr int OFF_XT  = CTRL_BYTES;
constexpr int OFF_X1  = OFF_XT + 2 * ACT;
constexpr int OFF_H   = OFF_X1 + 2 * ACT;
constexpr int OFF_WGU = OFF_H + 2 * HID;
constexpr int OFF_WD  = OFF_WGU + NT1 * KS1 * 1024;
constexpr int OFF_ST0 = OFF_WD + 32 * KS2P * 1024;
constexpr int OFF_ST1 = OFF_ST0 + L * 8;
constexpr int WS_END  = OFF_ST1 + 8 * L * 8;

#define STAMP_DECL
#define STAMP()
#define STAMP_OUT()
#define LDS_EXTRA 0

DEV float lo_f(u32 v) { return __uint_as_float(v << 16); }
DEV float hi_f(u32 v) { return __uint_as_float(v & 0xffff0000u); }
DEV u32 pk_bf16(float a, float b) {
  f32x2 v = {a, b};
  return __builtin_bit_cast(u32, __builtin_convertvector(v, bf16x2));
}
DEV u32 pk_bf16(f32x2 v) { return __builtin_bit_cast(u32, __builtin_convertvector(v, bf16x2)); }
DEV u32 pk_f16(f32x2 v) { return __builtin_bit_cast(u32, __builtin_convertvector(v, f16x2)); }
DEV f32x2 unpk(u32 v) { return (f32x2){lo_f(v), hi_f(v)}; }
DEV float rbf(float a) { return lo_f(pk_bf16(a, a)); }
DEV f32x2 ln_mv(f32x2 sq) {
  f32x2 t = __builtin_elementwise_fma(sq, (f32x2){1.0f / D, 1.0f / D}, (f32x2){0.f, EPS});
  t[1] = fmaxf(__builtin_fmaf(-t[0], t[0], t[1]), EPS);
  return t;
}
struct CM { f32x2 re, im; };
DEV CM mkcm(f32x2 c) { return CM{(f32x2){c[0], c[0]}, (f32x2){-c[1], c[1]}}; }
DEV f32x2 cmul_h(f32x2 x, const CM& c, u32& h) {
  const f32x2 r = __builtin_elementwise_fma(x.yx, c.im, x * c.re);
  h = pk_f16(r);
  return r;
}

constexpr int KCOPY = 4160, KCH = 4 * KCOPY;
constexpr int LD_U = 2 * KCH;
constexpr int UCH = (256 + L) * 2;
constexpr int LD_XT = LD_U + 2 * UCH;
constexpr int LD_Y = LD_XT + 65536;
constexpr int YW = 5120;
constexpr int LD_SS = LD_Y + 4 * YW;
constexpr int LD_OT = LD_Y;
constexpr int LD_CS = LD_SS + 512;
constexpr int LD_W2 = LD_CS + 768;
constexpr int LDS_BYTES = LD_W2 + 32768;
static_assert(LDS_BYTES <= 163840, "LDS");
constexpr int LD_KT = LD_XT + 16384;
constexpr int RROW = 144, RW = 64 * RROW;

#define ACLOB "a0","a1","a2","a3","a4","a5","a6","a7","a8","a9","a10","a11","a12","a13","a14","a15","a16","a17","a18","a19","a20","a21","a22","a23","a24","a25","a26","a27","a28","a29","a30","a31","a32","a33","a34","a35","a36","a37","a38","a39","a40","a41","a42","a43","a44","a45","a46","a47","a48","a49","a50","a51","a52","a53","a54","a55","a56","a57","a58","a59","a60","a61","a62","a63","a64","a65","a66","a67","a68","a69","a70","a71","a72","a73","a74","a75","a76","a77","a78","a79","a80","a81","a82","a83","a84","a85","a86","a87","a88","a89","a90","a91","a92","a93","a94","a95","a96","a97","a98","a99","a100","a101","a102","a103","a104","a105","a106","a107","a108","a109","a110","a111","a112","a113","a114","a115","a116","a117","a118","a119","a120","a121","a122","a123","a124","a125","a126","a127","a128","a129","a130","a131","a132","a133","a134","a135","a136","a137","a138","a139","a140","a141","a142","a143","a144","a145","a146","a147","a148","a149","a150","a151","a152","a153","a154","a155","a156","a157","a158","a159","a160","a161","a162","a163","a164","a165","a166","a167","a168","a169","a170","a171","a172","a173","a174","a175","a176","a177","a178","a179","a180","a181","a182","a183","a184","a185","a186","a187","a188","a189","a190","a191","a192","a193","a194","a195","a196","a197","a198","a199","a200","a201","a202","a203","a204","a205","a206","a207","a208","a209","a210","a211","a212","a213","a214","a215","a216","a217","a218","a219","a220","a221","a222","a223","a224","a225","a226","a227","a228","a229","a230","a231","a232","a233","a234","a235","a236","a237","a238","a239","a240","a241","a242","a243","a244","a245","a246","a247","a248","a249","a250","a251","a252","a253","a254","a255"
#define MF "v_mfma_f32_16x16x32_bf16 "
DEV void mfma_vv(f32x4& c, u32x4 a, u32x4 b) { asm(MF "%0, %1, %2, %0" : "+v"(c) : "v"(a), "v"(b)); }
DEV void mfma_v0(f32x4& c, u32x4 a, u32x4 b) { asm(MF "%0, %1, %2, 0" : "=&v"(c) : "v"(a), "v"(b)); }
template <bool FIRST> DEV void mfma_v(f32x4& c, u32x4 a, u32x4 b) { if constexpr (FIRST) mfma_v0(c, a, b); else mfma_vv(c, a, b); }
template <int R, bool FIRST> DEV void mfma_aA(f32x4& c, u32x4 b) {
  if constexpr (FIRST) asm(MF "%0, a[%c2:%c3], %1, 0" : "=&v"(c) : "v"(b), "i"(R), "i"(R + 3));
  else asm(MF "%0, a[%c2:%c3], %1, %0" : "+v"(c) : "v"(b), "i"(R), "i"(R + 3));
}
template <int R, bool FIRST> DEV void mfma_aB(f32x4& c, u32x4 a) {
  if constexpr (FIRST) asm(MF "%0, %1, a[%c2:%c3], 0" : "=&v"(c) : "v"(a), "i"(R), "i"(R + 3));
  else asm(MF "%0, %1, a[%c2:%c3], %0" : "+v"(c) : "v"(a), "i"(R), "i"(R + 3));
}
template <bool FIRST> DEV void mfma_h(f32x4& c, u32x4 a, u32x4 b) {
  if constexpr (FIRST) asm("s_nop 1\n\tv_mfma_f32_16x16x32_f16 %0, %1, %2, 0" : "=&v"(c) : "v"(a), "v"(b));
  else asm("s_nop 1\n\tv_mfma_f32_16x16x32_f16 %0, %1, %2, %0" : "+v"(c) : "v"(a), "v"(b));
}
DEV void mfma_done4(f32x4& a, f32x4& b, f32x4& c, f32x4& d) { asm volatile("s_nop 7" : "+v"(a), "+v"(b), "+v"(c), "+v"(d)); }
DEV void tie4(f32x4& a, f32x4& b, f32x4& c, f32x4& d) { asm volatile("" : "+v"(a), "+v"(b), "+v"(c), "+v"(d)); }
DEV void bar() { asm volatile("s_waitcnt lgkmcnt(0)\n\ts_barrier" ::: "memory", ACLOB); }

DEV u32x4 ld16(rsrc_t rs, int voff, int soff) { return __builtin_amdgcn_raw_buffer_load_b128(rs, voff, soff, SC1); }
DEV u32x2 ld8(rsrc_t rs, int voff, int soff) { return __builtin_amdgcn_raw_buffer_load_b64(rs, voff, soff, SC1); }
DEV void st16(rsrc_t rs, int voff, int soff, u32x4 v) { __builtin_amdgcn_raw_buffer_store_b128(v, rs, voff, soff, SC1); }
DEV int fresh_lane() {
  int l;
  asm volatile("v_mbcnt_lo_u32_b32 %0, -1, 0\n\tv_mbcnt_hi_u32_b32 %0, -1, %0" : "=v"(l));
  return l;
}
DEV u32x4 splat4(u32 c) {
  u32x4 r;
  asm volatile("v_mov_b32 %0, %4\n\tv_mov_b32 %1, %4\n\tv_mov_b32 %2, %4\n\tv_mov_b32 %3, %4"
               : "=v"(r[0]), "=v"(r[1]), "=v"(r[2]), "=v"(r[3]) : "i"(c));
  asm volatile("s_nop 1" : "+v"(r));
  return r;
}
DEV int opq(int x) { asm volatile("" : "+s"(x)); return x; }
DEV u32x2 tr_half(const char* p) { return __builtin_bit_cast(u32x2, __builtin_amdgcn_ds_read_tr16_b64_v4i16((LAS s16x4*)p)); }
DEV u32x4 tr_frag(const char* p, int o0, int o1) {
  u32x2 a = tr_half(p + o0), c = tr_half(p + o1);
  return (u32x4){a[0], a[1], c[0], c[1]};
}
template <int N, typename FN, int I = 0>
DEV void static_for(FN&& fn) {
  if constexpr (I < N) { fn(std::integral_constant<int, I>{}); static_for<N, FN, I + 1>(static_cast<FN&&>(fn)); }
}

constexpr int SPIN_CAP = 400000;
DEV int all_off(int edge, int b) { return OFF_CA + (edge * 8 + (b & 7)) * 4096; }
DEV int grp_off(int idx) { return OFF_CH + idx * 256; }
DEV u32 poll_ld(rsrc_t rs, int soff, bool all) {
  const int voff = all ? (fresh_lane() & 15) * 256 : 0;
  return __builtin_amdgcn_raw_buffer_load_b32(rs, voff, soff, SC1);
}
DEV void poll_wait(rsrc_t rs, int soff, bool all, u32 f) {
  if (__builtin_expect(!__all(f >= 16u), 0)) {
    const int voff = all ? (fresh_lane() & 15) * 256 : 0;
    int spins = SPIN_CAP;
    u32 t;
    asm volatile("s_nop 4\n"
                 "1:\n\t"
                 "buffer_load_dword %0, %2, %3, %4 offen sc1\n\t"
                 "s_sub_u32 %1, %1, 1\n\t"
                 "s_cbranch_scc1 2f\n\t"
                 "s_waitcnt vmcnt(0)\n\t"
                 "v_cmp_gt_u32 vcc, 16, %0\n\t"
                 "s_nop 4\n\t"
                 "s_cbranch_vccnz 1b\n"
                 "2:\n\t"
                 "s_waitcnt vmcnt(0)"
                 : "=&v"(t), "+s"(spins) : "v"(voff), "s"(rs), "s"(soff) : "memory", "vcc", "scc");
  }
  asm volatile("" ::: "memory");
}
template <int YOUNGER, bool LDSW>
DEV void drain_bar() {
  if constexpr (LDSW) asm volatile("s_waitcnt vmcnt(%c0) lgkmcnt(0)\n\ts_barrier" :: "i"(YOUNGER) : "memory", ACLOB);
  else asm volatile("s_waitcnt vmcnt(%c0)\n\ts_barrier" :: "i"(YOUNGER) : "memory", ACLOB);
}
constexpr int OOB = 0x7ffffff0;
DEV void signal_all(rsrc_t rs, int edge, int b, int w) {
  if (w == 0) {
    const int lane = fresh_lane();
    __builtin_amdgcn_raw_ptr_buffer_atomic_add_i32(1, rs, lane < 8 ? lane * 4096 : OOB, OFF_CA + edge * 32768 + (b & 15) * 256, SC1);
  }
}
DEV void signal_grp(rsrc_t rs, int idx, int w) {
  if (w == 0) {
    const int lane = fresh_lane();
    __builtin_amdgcn_raw_ptr_buffer_atomic_add_i32(1, rs, lane == 0 ? 0 : OOB, OFF_CH + idx * 256, SC1);
  }
}

template <int P> struct PrepRegs { u32x4 pa[P], pb[P]; u32x2 xv[2]; };
template <int P>
DEV void prep_ld1(rsrc_t rs, int i, int tid, int xso, int sso, PrepRegs<P>& pr) {
  if (i < 2 * P) {
    if (i & 1) pr.pb[i >> 1] = ld16(rs, tid * 32 + 16, sso + (i >> 1) * (L * 8));
    else pr.pa[i >> 1] = ld16(rs, tid * 32, sso + (i >> 1) * (L * 8));
  } else pr.xv[i - 2 * P] = ld8(rs, tid * 8, xso + (i - 2 * P) * (L * 2));
}
template <int P>
DEV void prep_load(rsrc_t rs, int tid, int xso, int sso, PrepRegs<P>& pr) {
#pragma unroll
  for (int i = 0; i < 2 * P + 2; ++i) prep_ld1<P>(rs, i, tid, xso, sso, pr);
  SB();
}
DEV void kill4(u32x4& x) { asm volatile("" : "=v"(x)); }
DEV void kill2(u32x2& x) { asm volatile("" : "=v"(x)); }
template <int P> DEV void prep_kill(PrepRegs<P>& pr) {
#pragma unroll
  for (int p = 0; p < P; ++p) { kill4(pr.pa[p]); kill4(pr.pb[p]); }
  kill2(pr.xv[0]); kill2(pr.xv[1]);
}
template <int P>
DEV void prep_ln(char* smem, int h, int tid, u32 dsk, const PrepRegs<P>& pr, f32x4 (&z)[2]) {
  f32x4 s0 = __builtin_bit_cast(f32x4, pr.pa[0]), s1 = __builtin_bit_cast(f32x4, pr.pb[0]);
#pragma unroll
  for (int p = 1; p < P; ++p) { s0 += __builtin_bit_cast(f32x4, pr.pa[p]); s1 += __builtin_bit_cast(f32x4, pr.pb[p]); }
  const f32x2 t0 = ln_mv(s0.xy), t1 = ln_mv(s0.zw), t2 = ln_mv(s1.xy), t3 = ln_mv(s1.zw);
  const f32x2 rr[2] = {{__builtin_amdgcn_rsqf(t0[1]), __builtin_amdgcn_rsqf(t1[1])}, {__builtin_amdgcn_rsqf(t2[1]), __builtin_amdgcn_rsqf(t3[1])}};
  const f32x2 mr[2] = {{-t0[0] * rr[0][0], -t1[0] * rr[0][1]}, {-t2[0] * rr[1][0], -t3[0] * rr[1][1]}};
#pragma unroll
  for (int c = 0; c < 2; ++c) {
    const float dk = c ? hi_f(dsk) : lo_f(dsk);
    u32x2 u;
    f32x2 zz[2];
#pragma unroll
    for (int i = 0; i < 2; ++i) {
      const f32x2 x = unpk(pr.xv[c][i]);
      const f32x2 uf = __builtin_elementwise_fma(x, rr[i], mr[i]);
      u[i] = pk_bf16(uf);
      zz[i] = __builtin_elementwise_fma(uf, (f32x2){dk, dk}, x);
    }
    z[c] = (f32x4){zz[0][0], zz[0][1], zz[1][0], zz[1][1]};
    *(u32x2*)(smem + LD_U + c * UCH + (256 + h * 1024) * 2 + tid * 8) = u;
  }
}
template <int M0, int I0, int I1> struct ConvPlan {
  static constexpr int Q0 = (M0 - I1) > 0 ? (M0 - I1) : 0, Q1 = M0 + 3 - I0;
  static constexpr int NQ = Q1 - Q0 + 1, NU = I1 - I0 + 1, NF = NQ + NU;
  static constexpr int count() { int n = 0; for (int i = I0; i <= I1; ++i) for (int m = 0; m < 4; ++m) n += (M0 + m - i >= 0); return n; }
  static constexpr int NM = count();
  static constexpr int im(int t) { int n = 0; for (int i = I0; i <= I1; ++i) for (int m = 0; m < 4; ++m) if (M0 + m - i >= 0) { if (n == t) return i * 4 + m; ++n; } return 0; }
  static constexpr int frag(int f) {
    if (f == 0) return 0;
    if (f <= 4) return -(Q1 - 4 + f + 1);
    int g = f - 5, nlow = NQ - 4;
    if (g < 2 * nlow) return (g & 1) ? -(Q1 - 4 - (g >> 1) + 1) : 1 + (g >> 1);
    return 1 + nlow + (g - 2 * nlow);
  }
};
template <int M0, int I0, int I1, bool FIRST, typename FN>
DEV void conv_mfma(char* smem, int w, int lane, f32x4 (&acc)[4], FN&& fill) {
  using P = ConvPlan<M0, I0, I1>;
  const int c16 = lane & 15, kq = lane >> 4;
  const int cch = w >> 1, wr = w & 1;
  const char* tb = smem + cch * KCH + (c16 >> 2) * KCOPY + (16 + 16 * (1 + 8 * wr) - 8 * kq + 4 * (c16 & 3) - 4) * 2;
  const char* ub = smem + LD_U + cch * UCH + (256 - 16 * (1 + 8 * wr + 6) + 16 * c16 + 8 * kq) * 2;
  u32x4 T[2][P::NQ], U[2][P::NU];
  auto ldf = [&](auto rc, auto fc) {
    constexpr int rr = IC(rc), f = P::frag(IC(fc));
    if constexpr (f >= 0) U[rr & 1][f] = *(const u32x4*)(ub + (3 - rr) * 64 + (I0 + f) * 512);
    else T[rr & 1][-f - 1 - P::Q0] = tr_frag(tb, rr * 64 + (-f - 1) * 512 + 8, rr * 64 + (-f - 1) * 512);
  };
  static_for<P::NF>([&](auto fc) { ldf(std::integral_constant<int, 0>{}, fc); });
  SB();
  static_for<4 * P::NM>([&](auto nc) {
    constexpr int n = IC(nc), rr = n / P::NM, t = n % P::NM, i = P::im(t) >> 2, m = P::im(t) & 3, q = M0 + m - i;
    mfma_v<FIRST && rr == 0 && i == I0>(acc[m], T[rr & 1][q - P::Q0], U[rr & 1][i - I0]);
    if constexpr (rr < 3 && t < P::NF) ldf(std::integral_constant<int, rr + 1>{}, std::integral_constant<int, t>{});
    SB();
    fill(nc);
    SB();
  });
}
DEV void conv_out(rsrc_t rs, char* smem, int xso, int w, int lane, f32x4 (&acc)[4], const f32x4 (&z)[2]) {
  const int tid = w * 64 + lane;
  char* yw = smem + LD_Y + w * YW + (lane & 15) * 80 + (lane >> 4) * 16;
  const char* yb = smem + LD_Y + tid * 16 + (tid >> 2) * 16;
  const int vo = ((tid & 1) * L + (tid >> 1) * 8) * 2;
  mfma_done4(acc[0], acc[1], acc[2], acc[3]);
#pragma unroll
  for (int m = 0; m < 4; ++m) *(f32x4*)(yw + m * 1280) = acc[m];
  bar();
  u32x2 o[2];
#pragma unroll
  for (int c = 0; c < 2; ++c) {
    const f32x4 y = (*(const f32x4*)(yb + (2 * c) * YW) + z[c]) + *(const f32x4*)(yb + (2 * c + 1) * YW);
    o[c] = (u32x2){pk_bf16(y[0], y[1]), pk_bf16(y[2], y[3])};
  }
  const bool odd = tid & 1;
  u32 s0 = odd ? o[0][0] : o[1][0], s1 = odd ? o[0][1] : o[1][1];
  u32 g0 = __builtin_amdgcn_update_dpp(0u, s0, 0xB1, 0xf, 0xf, true);
  u32 g1 = __builtin_amdgcn_update_dpp(0u, s1, 0xB1, 0xf, 0xf, true);
  u32x4 v = odd ? (u32x4){g0, g1, o[1][0], o[1][1]} : (u32x4){o[0][0], o[0][1], g0, g1};
  st16(rs, vo, xso, v);
  SB();
}

template <int R, int IMM>
DEV void ld_agpr(rsrc_t rs, int voff, int sbase) {
  int tmp;
  asm volatile("s_add_u32 %0, %3, %c6\n\tbuffer_load_dwordx4 a[%c4:%c5], %1, %2, %0 offen sc1"
               : "=&s"(tmp) : "v"(voff), "s"(rs), "s"(sbase), "i"(R), "i"(R + 3), "i"(IMM) : "memory", "scc");
}
template <int I> DEV void ld_w1(rsrc_t rs, int voff, int so01, int so2) {
  constexpr int ks = I / 3, i = I % 3;
  if constexpr (i < 2) ld_agpr<4 * I, (i * KS1 + ks) * 1024>(rs, voff, so01);
  else ld_agpr<4 * I, ks * 1024>(rs, voff, so2);
}
template <int R, int IMM>
DEV void ld_agpr_imm(rsrc_t rs, int voff, int soff) {
  asm volatile("buffer_load_dwordx4 a[%c3:%c4], %0, %1, %2 offen offset:%c5 sc1"
               :: "v"(voff), "s"(rs), "s"(soff), "i"(R), "i"(R + 3), "i"(IMM) : "memory");
}

DEV int xs_voff(int tid) { return ((tid >> 3) * L + (tid & 7) * 8) * 2; }
DEV char* xs_lds(char* smem, int tid) {
  const int fsw = ((tid >> 4) & 1) | (((tid >> 6) & 1) << 1);
  return smem + LD_XT + (tid >> 3) * 128 + ((((tid & 7) >> 1) ^ fsw) * 32) + (tid & 1) * 16;
}
constexpr int XS_STEP = 32 * L * 2;

__global__ __launch_bounds__(256, 1) void main_kernel(
    const u16* __restrict__ x0, const u16* __restrict__ Are, const u16* __restrict__ Aim,
    const u16* __restrict__ Bre, const u16* __restrict__ Bim, const u16* __restrict__ Cre,
    const u16* __restrict__ Cim, const u16* __restrict__ logdt, const u16* __restrict__ dskip,
    const u16* __restrict__ wg, const u16* __restrict__ wu, const u16* __restrict__ wd,
    char* __restrict__ ws, u16* __restrict__ out) {
  extern __shared__ __attribute__((aligned(16))) char smem[];
  const int w_ = __builtin_amdgcn_readfirstlane(threadIdx.x >> 6);
  const int b_ = blockIdx.x;
  const rsrc_t rs = __builtin_amdgcn_make_buffer_rsrc(ws, 0, WS_END, 0x00020000);
  STAMP_DECL
  STAMP();
  asm volatile("" ::: ACLOB);
  u32 cpf = 0; (void)cpf;
  const int rt_ = ((b_ & 7) >> 1) * 4 + ((b_ >> 3) & 3), cb_ = (b_ & 1) * 8 + (b_ >> 5);
  const int rt2_ = (b_ & 7) * 4 + ((b_ >> 3) & 3), ct_ = b_ >> 5;
  const int start_ = cb_ * 10 + (cb_ < 12 ? cb_ : 12), cnt_ = cb_ < 12 ? 11 : 10;
  const int nbase_ = (w_ < 3) ? w_ * 3 : cnt_ - 2;
  const int nt0_ = start_ + nbase_;
  const int ok2_ = __builtin_amdgcn_readfirstlane((w_ < 3) && (nbase_ + 2 < cnt_ - 2));
#define ROLES \
  const int w = w_, b = b_, rt = rt_, rt2 = rt2_, ct = ct_, nt0 = nt0_, ok2 = ok2_; \
  const int lane = fresh_lane(), tid = w * 64 + lane, c16 = lane & 15, kq = lane >> 4, wvo = lane * 16; \
  const int x1_off = OFF_X1 + layer * ACT, h_off = OFF_H + layer * HID; \
  const int hb_e = h_off + (rt2 * 2 * NT1 * 16 + w * 11 * 64) * 16, hb_l = hb_e + 64 * NT1 * 256; \
  const int s10 = (w == 3) ? 9 * 1024 : 10 * 1024; \
  (void)b; (void)rt; (void)ct; (void)nt0; (void)ok2; (void)tid; (void)c16; (void)kq; (void)wvo; (void)x1_off; (void)hb_e; (void)hb_l; (void)s10;

  u32 dsk;
  u32x4 xs[8];
  {
    constexpr int layer = 0;
    ROLES
    const int d0 = 2 * b;
    u32x4 xf[4], xt[2], wr[4];
    {
      const rsrc_t rx = __builtin_amdgcn_make_buffer_rsrc((void*)x0, 0, L * D * 2, 0x00020000);
      const rsrc_t rg = __builtin_amdgcn_make_buffer_rsrc((void*)wg, 0, D * F * 2, 0x00020000);
      const rsrc_t ru = __builtin_amdgcn_make_buffer_rsrc((void*)wu, 0, D * F * 2, 0x00020000);
      const int tvo = (lane >> 1) * (D * 2) + (lane & 1) * 16, tso = (rt2 * 64 * D + ct * 64 + w * 16) * 2;
      xt[0] = __builtin_amdgcn_raw_buffer_load_b128(rx, tvo, tso, 0);
      xt[1] = __builtin_amdgcn_raw_buffer_load_b128(rx, tvo, tso + 32 * D * 2, 0);
      const int gvo = kq * (F * 2) + (c16 < cnt_ ? c16 : 0) * 16, gso = ((rt * 32 + w * 8) * F + start_ * 8) * 2;
      wr[0] = __builtin_amdgcn_raw_buffer_load_b128(rg, gvo, gso, 0);
      wr[1] = __builtin_amdgcn_raw_buffer_load_b128(ru, gvo, gso, 0);
      wr[2] = __builtin_amdgcn_raw_buffer_load_b128(rg, gvo, gso + 4 * F * 2, 0);
      wr[3] = __builtin_amdgcn_raw_buffer_load_b128(ru, gvo, gso + 4 * F * 2, 0);
      const int fvo = (c16 & 7) * (D * 2) + kq * 16, fso = (b * 8 * D + w * 128) * 2;
#pragma unroll
      for (int i = 0; i < 4; ++i) xf[i] = __builtin_amdgcn_raw_buffer_load_b128(rx, fvo + i * 64, fso, 0);
    }
    const u32 pn = b * (2 * NS) + tid;
    const u32 p_ar = Are[pn], p_ai = Aim[pn], p_br = Bre[pn], p_bi = Bim[pn], p_cr = Cre[pn], p_ci = Cim[pn];
    const u32 p_ld = logdt[(u32)(d0 + (tid >> 7))];
    dsk = ((const u32*)dskip)[(u32)(b + (lane >> 6))];
    {
      unsigned long long pc; int len;
      asm volatile("s_getpc_b64 %0\n.Lmk_code_begin:\n\ts_mov_b32 %1, .Lmk_code_end-.Lmk_code_begin" : "=s"(pc), "=s"(len));
      const int off = min(((((b >> 3) & 3) * 4 + w) * 64 + lane) * 128, len - 4) & ~3;
      cpf = *(const GAS u32*)(pc + (unsigned long long)off);
    }
    SB();
    STAMP();
    {
      u32x4 z = splat4(0u);
      if (tid < 16) *(u32x4*)(smem + (tid >> 1) * KCOPY + (tid & 1) * 16) = z;
      if (tid >= 64 && tid < 128) *(u32x4*)(smem + LD_U + (lane >> 5) * UCH + (lane & 31) * 16) = z;
      if (tid == 128) *(u32x4*)(smem + LD_KT + 760 * 16) = z;
    }
    int ix[12];
#pragma unroll
    for (int i = 0; i < 12; i += 2) {
      const int l0 = tid * 8 - 4 + i;
      const u32 bits = pk_bf16((float)l0, (float)(l0 + 1));
      const int i0 = l0 < 256 ? (l0 < 0 ? 676 : l0) : (int)(bits & 0xffffu) - 17024;
      const int i1 = l0 < 256 ? (l0 < 0 ? 676 : l0 + 1) : (int)(bits >> 16) - 17024;
      ix[i] = LD_KT + (i0 + (i0 >> 3)) * 16; ix[i + 1] = LD_KT + (i1 + (i1 >> 3)) * 16;
      asm volatile("" : "+v"(ix[i]), "+v"(ix[i + 1]));
    }
    int kx[2][2][4];
#pragma unroll
    for (int c = 0; c < 2; ++c) {
      const int B = 16 * c + c16;
#pragma unroll
      for (int i = 0; i < 4; ++i) {
        const int i0 = kq < 2 ? 8 * B + 4 * kq + i : kq == 2 ? 256 + 4 * B + i : 384 + (i >> 1) * 64 + 2 * B + (i & 1);
        const int i1 = kq == 0 ? 512 + 32 * i + B : (kq == 1 && i == 0) ? 640 + B : 680;
        kx[0][c][i] = LD_KT + (i0 + (i0 >> 3)) * 16 + w * 4; kx[1][c][i] = LD_KT + (i1 + (i1 >> 3)) * 16 + w * 4;
        asm volatile("" : "+v"(kx[0][c][i]), "+v"(kx[1][c][i]));
      }
    }
    SB();
    {
      char* img = smem + LD_Y + w * YW;
      *(u32x4*)(img + wvo) = xt[0];
      *(u32x4*)(img + 1024 + wvo) = xt[1];
      const char* tp = img + (kq * 8 + (c16 >> 2)) * 32 + (c16 & 3) * 8;
      u32x4 t0 = tr_frag(tp, 0, 128), t1 = tr_frag(tp, 1024, 1024 + 128);
      const int so = OFF_XT + ((ct * 64 + w * 16) * L + rt2 * 64) * 2;
      st16(rs, c16 * (L * 2) + kq * 16, so, t0);
      st16(rs, c16 * (L * 2) + kq * 16 + 64, so, t1);
    }
    constexpr int WROW = 368;
    if (c16 < 11) {
      char* wdst = smem + LD_XT + (w * 8 + kq) * WROW + c16 * 32;
#pragma unroll
      for (int i = 0; i < 2; ++i) {
        const u32x4 g = wr[2 * i], u = wr[2 * i + 1];
        *(u32x4*)(wdst + i * 4 * WROW) = (u32x4){g[0], u[0], g[1], u[1]};
        *(u32x4*)(wdst + i * 4 * WROW + 16) = (u32x4){g[2], u[2], g[3], u[3]};
      }
    }
    {
      const u32x4 ones = splat4(0x3f803f80u);
      f32x4 sa, qa;
      static_for<4>([&](auto ic) { mfma_v<IC(ic) == 0>(sa, ones, xf[IC(ic)]); mfma_v<IC(ic) == 0>(qa, xf[IC(ic)], xf[IC(ic)]); });
      asm volatile("s_nop 7" : "+v"(sa), "+v"(qa));
      if ((c16 >> 2) == kq && c16 < 8) {
        const int r = c16 & 3;
        float q = r == 0 ? qa[0] : r == 1 ? qa[1] : r == 2 ? qa[2] : qa[3];
        *(f32x2*)(smem + LD_SS + (w * 8 + c16) * 8) = (f32x2){sa[0], q};
      }
    }
    bar();
    asm volatile("" :: "v"(cpf));
    const int so01 = OFF_WGU + nt0 * KS1 * 1024, so2 = so01 + (ok2 ? 2 * KS1 * 1024 : 0);
    {
      const char* wsrc = smem + LD_XT + (8 * kq + (c16 >> 2)) * WROW + nbase_ * 32 + (c16 & 3) * 8;
      u32x4 f0 = tr_frag(wsrc, 0, 4 * WROW), f1 = tr_frag(wsrc, 32, 32 + 4 * WROW), f2 = tr_frag(wsrc, 64, 64 + 4 * WROW);
      if (tid < 8) {
        const f32x2* sp = (const f32x2*)(smem + LD_SS) + tid;
        f32x2 v = (sp[0] + sp[8]) + (sp[16] + sp[24]);
        __builtin_amdgcn_raw_buffer_store_b64(__builtin_bit_cast(u32x2, v), rs, tid * 8, OFF_ST0 + b * 64, SC1);
      }
      st16(rs, wvo, so01 + rt * 1024, f0);
      st16(rs, wvo, so01 + (KS1 + rt) * 1024, f1);
      if (ok2) st16(rs, wvo, so2 + rt * 1024, f2);
    }
    SB();
    STAMP();
    PrepRegs<1> pe, pl;
    u32 pv;
    {
      constexpr int TROW = 272;
      char* tab = smem + LD_XT + 32768 - 4096 + w * (32 * TROW);
      char* tar = smem + LD_Y + w * YW;
      char* tar2 = smem + LD_W2 + w * 4096;
      const float dt = rbf(expf(lo_f(p_ld)));
      const float ar = dt * lo_f(p_ar), ai = dt * lo_f(p_ai), br = dt * lo_f(p_br), bi = dt * lo_f(p_bi);
      const float cr = lo_f(p_cr), ci = lo_f(p_ci);
      const float I2PI_HI = 0.15915494309189535f;
      const float I2PI_LO = (float)(0.15915494309189535 - (double)0.15915494309189535f);
      const float a2 = ar * 1.4426950408889634f;
      const float rh = ai * I2PI_HI;
      const float rl = __builtin_fmaf(ai, I2PI_LO, __builtin_fmaf(ai, I2PI_HI, -rh));
      auto cexp = [&](float l) {
        const float e = __builtin_amdgcn_exp2f(a2 * l);
        const float fr = __builtin_fmaf(l, rl, __builtin_amdgcn_fractf(rh * l));
        return (f32x2){__builtin_amdgcn_cosf(fr), __builtin_amdgcn_sinf(fr)} * e;
      };
      const float kr = cr * br - ci * bi, ki = cr * bi + ci * br;
      u32 mx = max(__float_as_uint(kr) & 0x7f800000u, __float_as_uint(ki) & 0x7f800000u);
      mx = max(mx, (u32)__builtin_amdgcn_update_dpp(0, (int)mx, 0xB1, 0xf, 0xf, true));
      mx = max(mx, (u32)__builtin_amdgcn_update_dpp(0, (int)mx, 0x4E, 0xf, 0xf, true));
      mx = max(mx, (u32)__builtin_amdgcn_update_dpp(0, (int)mx, 0x124, 0xf, 0xf, true));
      mx = max(mx, (u32)__builtin_amdgcn_update_dpp(0, (int)mx, 0x128, 0xf, 0xf, true));
      mx = max(max((u32)__builtin_amdgcn_readlane((int)mx, 0), (u32)__builtin_amdgcn_readlane((int)mx, 16)),
               max((u32)__builtin_amdgcn_readlane((int)mx, 32), (u32)__builtin_amdgcn_readlane((int)mx, 48)));
      mx = min(max(mx, 60u << 23), 200u << 23);
      const float up = __uint_as_float((267u << 23) - mx);
      const float kscale = __uint_as_float(mx - (27u << 23));
      const f32x2 e1 = cexp(1.f), e8 = cexp(8.f), e128 = cexp(128.f), e256 = cexp(256.f), e1024 = cexp(1024.f);
      const CM c8 = mkcm((f32x2){e8[0], -e8[1]}), c128 = mkcm((f32x2){e128[0], -e128[1]}), m1 = mkcm(e1), m256 = mkcm(e256);
      u32 hv;
      auto putr = [&](int r, u32 v) { *(u32*)((r < 16 ? tar + r * TROW : tar2 + (r - 16) * TROW) + lane * 4) = v; };
      auto putc = [&](int c, u32 v) { *(u32*)(tab + c * TROW + lane * 4) = v; };
      f32x2 p[8];
      p[0] = (f32x2){16384.f, 0.f}; putr(0, pk_f16(p[0]));
      p[1] = e1 * 16384.f; putr(1, pk_f16(p[1]));
#pragma unroll
      for (int i = 2; i < 8; ++i) { p[i] = cmul_h(p[i - 1], m1, hv); putr(i, hv); }
      f32x2 q0 = e256 * 16384.f; putr(8, pk_f16(q0));
      (void)cmul_h(p[2], m256, hv); putr(9, hv);
      f32x2 q4 = cmul_h(p[4], m256, hv); putr(10, hv);
      (void)cmul_h(p[6], m256, hv); putr(11, hv);
      q0 = cmul_h(q0, m256, hv); putr(12, hv);
      q4 = cmul_h(q4, m256, hv); putr(13, hv);
      (void)cmul_h(q0, m256, hv); putr(14, hv);
      (void)cmul_h(q4, m256, hv); putr(15, hv);
      q0 = e1024 * 16384.f; putr(16, pk_f16(q0));
#pragma unroll
      for (int i = 17; i < 21; ++i) { q0 = cmul_h(q0, m256, hv); putr(i, hv); }
      SB();
      drain_bar<0, false>();
      signal_all(rs, 6, b, w);
      SB();
      f32x2 v0 = (f32x2){kr, -ki} * up; putc(0, pk_f16(v0));
      f32x2 v1 = cmul_h(v0, c128, hv); putc(16, hv);
#pragma unroll
      for (int i = 1; i < 16; ++i) {
        v0 = cmul_h(v0, c8, hv); putc(i, hv);
        v1 = cmul_h(v1, c8, hv); putc(16 + i, hv);
      }
      f32x4 ka[2][2];
      const char* ra0 = tar + c16 * TROW + kq * 16;
      const char* ra1 = tar2 + (c16 < 5 ? c16 : 0) * TROW + kq * 16;
      const char* rb0 = tab + c16 * TROW + kq * 16;
      static_for<4>([&](auto sc) {
        constexpr int s = IC(sc);
        const u32x4 a0 = *(const u32x4*)(ra0 + s * 64), a1 = *(const u32x4*)(ra1 + s * 64);
        const u32x4 b0 = *(const u32x4*)(rb0 + s * 64), b1 = *(const u32x4*)(rb0 + 16 * TROW + s * 64);
        mfma_h<s == 0>(ka[0][0], a0, b0); mfma_h<s == 0>(ka[0][1], a0, b1);
        mfma_h<s == 0>(ka[1][0], a1, b0); mfma_h<s == 0>(ka[1][1], a1, b1);
      });
      pv = poll_ld(rs, all_off(6, b), true);
      mfma_done4(ka[0][0], ka[0][1], ka[1][0], ka[1][1]);
#pragma unroll
      for (int r = 0; r < 2; ++r)
#pragma unroll
        for (int c = 0; c < 2; ++c) {
          const f32x4 v = ka[r][c] * kscale;
#pragma unroll
          for (int i = 0; i < 4; ++i) *(float*)(smem + kx[r][c][i]) = v[i];
        }
    }
    bar();
    STAMP();
    {
      u32 e[2][6];
#pragma unroll
      for (int i = 0; i < 6; ++i) {
        const f32x4 a = *(const f32x4*)(smem + ix[2 * i]), c = *(const f32x4*)(smem + ix[2 * i + 1]);
        e[0][i] = pk_bf16(a[0] + a[1], c[0] + c[1]);
        e[1][i] = pk_bf16(a[2] + a[3], c[2] + c[3]);
      }
      SB();
      poll_wait(rs, all_off(6, b), true, pv);
      prep_load<1>(rs, tid, OFF_XT + d0 * L * 2, OFF_ST0, pe);
      prep_load<1>(rs, tid, OFF_XT + d0 * L * 2 + 2048, OFF_ST0 + 8192, pl);
      STAMP();
#pragma unroll
      for (int c = 0; c < 2; ++c) {
        char* kb = smem + c * KCH + tid * 16 + 32;
        u32 o[5];
#pragma unroll
        for (int i = 0; i < 5; ++i) o[i] = __builtin_amdgcn_alignbit(e[c][i + 1], e[c][i], 16);
        *(u32x4*)(kb) = (u32x4){e[c][2], e[c][3], e[c][4], e[c][5]};
        *(u32x4*)(kb + KCOPY) = (u32x4){o[1], o[2], o[3], o[4]};
        *(u32x4*)(kb + 2 * KCOPY) = (u32x4){e[c][1], e[c][2], e[c][3], e[c][4]};
        *(u32x4*)(kb + 3 * KCOPY) = (u32x4){o[0], o[1], o[2], o[3]};
      }
    }
    SB();
    f32x4 ze[2], zl[2];
    prep_ln<1>(smem, 0, tid, dsk, pe, ze);
    prep_ln<1>(smem, 1, tid, dsk, pl, zl);
    SB();
    static_for<12>([&](auto ic) { ld_w1<IC(ic)>(rs, wvo, so01, so2); });
    bar();
    STAMP();
    f32x4 acc[4];
    conv_mfma<0, 0, 3, true>(smem, w, lane, acc, [&](auto nc) {
      constexpr int n = IC(nc);
      if constexpr (n >= 8 && (n & 3) == 0) ld_w1<12 + (n - 8) / 4>(rs, wvo, so01, so2);
    });
    conv_out(rs, smem, OFF_X1 + d0 * L * 2, w, lane, acc, ze);
    STAMP();
    const int chunk = b + 256 * w;
    const rsrc_t rwd = __builtin_amdgcn_make_buffer_rsrc((void*)wd, 0, F * D * 2, 0x00020000);
    u32x4 w2r[4];
    conv_mfma<4, 0, 7, true>(smem, w, lane, acc, [&](auto nc) {
      constexpr int n = IC(nc);
      if constexpr (n < 42 && n % 3 == 0) ld_w1<20 + n / 3>(rs, wvo, so01, so2);
      if constexpr (n == 44) { drain_bar<14, false>(); signal_all(rs, 0, b, w); }
      if constexpr (n >= 48 && n < 90 && n % 3 == 0) ld_w1<34 + (n - 48) / 3>(rs, wvo, so01, so2);
      if constexpr (n >= 92 && n < 100 && !(n & 1))
        w2r[(n - 92) >> 1] = __builtin_amdgcn_raw_buffer_load_b128(rwd, (lane >> 3) * (D * 2) + (lane & 7) * 16,
                                                                  ((chunk >> 3) * 32 * D + (chunk & 7) * 64 + ((n - 92) >> 1) * 8 * D) * 2, 0);
    });
    STAMP();
    pv = poll_ld(rs, all_off(0, b), true);
    conv_out(rs, smem, OFF_X1 + d0 * L * 2 + 2048, w, lane, acc, zl);
    STAMP();
    poll_wait(rs, all_off(0, b), true, pv);
    STAMP();
    const int gx = xs_voff(tid), gs = OFF_X1 + rt * 128;
#pragma unroll
    for (int i = 0; i < 8; ++i) xs[i] = ld16(rs, gx, gs + i * XS_STEP);
    SB();
    {
      const u32x4 ones = splat4(0x3f803f80u);
      f32x4 c[3];
      static_for<48>([&](auto ic) { mfma_aA<4 * IC(ic), (IC(ic) < 3)>(c[IC(ic) % 3], ones); SB(); });
      asm volatile("s_nop 7" : "+v"(c[0]), "+v"(c[1]), "+v"(c[2]));
      if (c16 == 0) {
#pragma unroll
        for (int i = 0; i < 3; ++i) *(f32x4*)(smem + LD_CS + w * 192 + i * 64 + kq * 16) = -c[i];
      }
    }
    SB();
    if (chunk < KS2P * 8) {
      char* img = smem + LD_W2 + w * 4096;
#pragma unroll
      for (int i = 0; i < 4; ++i)
        *(u32x4*)(img + i * 1024 + (lane >> 3) * 128 + ((((lane & 7) >> 1) ^ (((lane >> 4) & 1) | ((i & 1) << 1))) * 32) + (lane & 1) * 16) = w2r[i];
      const int fr = ((c16 >> 3) & 1) | ((kq & 1) << 1);
#pragma unroll
      for (int i = 0; i < 4; ++i)
        st16(rs, wvo, OFF_WD + (((chunk & 7) * 4 + i) * KS2P + (chunk >> 3)) * 1024,
             tr_frag(img + (8 * kq + (c16 >> 2)) * 128 + ((i ^ fr) * 32) + (c16 & 3) * 8, 0, 512));
    }
    SB();
    char* xw = xs_lds(smem, tid);
#pragma unroll
    for (int i = 0; i < 8; ++i) { *(u32x4*)(xw + i * 4096) = xs[i]; xs[i] = ld16(rs, gx, gs + (8 + i) * XS_STEP); }
    SB();
    bar();
    STAMP();
  }
  static_for<2>([&](auto lc) {
    constexpr int layer = IC(lc);
    PrepRegs<8> pr;
    u32x4 W2[5][4];
    u32x4 hf[11][2];
    {
      ROLES
      int sw2[4];
#pragma unroll
      for (int i = 0; i < 4; ++i) sw2[i] = OFF_WD + ((ct * 4 + ((w + i) & 3)) * KS2P + w * 11) * 1024;
      const int fr = ((c16 >> 3) & 1) | ((kq & 1) << 1);
      const char* xr[4];
#pragma unroll
      for (int j = 0; j < 4; ++j) xr[j] = smem + LD_XT + (8 * kq + (c16 >> 2)) * 128 + ((((w + j) & 3) ^ fr) * 32) + (c16 & 3) * 8;
      char* xw = xs_lds(smem, tid);
      const int gx = xs_voff(tid), gs = x1_off + 2048 + rt * 128;
      char* ssw = smem + ((c16 >> 2) == kq ? LD_SS + w * 128 : LD_Y) + c16 * 8;
      const u32x4 ones = splat4(0x3f803f80u);
      u32x2 xb[2][4][2];
#pragma unroll
      for (int j = 0; j < 4; ++j) { xb[0][j][0] = tr_half(xr[j]); xb[0][j][1] = tr_half(xr[j] + 512); }
      SB();
      static_for<2>([&](auto hc) {
        constexpr int h = IC(hc);
        f32x4 ga[3][4], sa, qa;
        const int nxt_poll = h == 0 ? all_off(layer * 4 + 1, b) : grp_off(layer * 32 + (rt2 >> 1));
        {
          u32 pv;
          auto fill = [&](auto gc) {
            constexpr int g = IC(gc);
            if constexpr (g >= 10 && g < 18) *(u32x4*)(xw + (g - 2) * 4096) = xs[g - 10];
            if constexpr (g == 9) {
              drain_bar<(h == 0 ? PUB_CL : 0), false>();
              if constexpr (h == 1) signal_grp(rs, layer * 32 + rt, w); else signal_all(rs, layer * 4 + 1, b, w);
            }
            if constexpr (g == 27) bar();
            if constexpr (h == 0) {
              if constexpr (g == 36) pv = poll_ld(rs, nxt_poll, true);
              if constexpr (g == 50) poll_wait(rs, nxt_poll, true, pv);
              if constexpr (g >= 50 && g < 58) xs[g - 50] = ld16(rs, gx, gs + (g - 50) * XS_STEP);
            } else {
              if constexpr (layer == 0) {
                if constexpr (g >= 10 && g < 18) {
                  ld_agpr_imm<192 + 32 * (g & 1) + 4 * ((g - 10) >> 1), (g & 1) * 2048>(rs, wvo, sw2[(g - 10) >> 1]);
                  ld_agpr_imm<208 + 32 * (g & 1) + 4 * ((g - 10) >> 1), (g & 1) * 2048 + 1024>(rs, wvo, sw2[(g - 10) >> 1]);
                }
                if constexpr (g >= 18 && g < 26) xs[g - 18] = ld16(rs, wvo + ((g - 18) >> 2) * 1024, sw2[(g - 18) & 3] + 4096);
                if constexpr (g >= 40 && g < 48) *(u32x4*)(smem + LD_W2 + w * 8192 + (g - 40) * 1024 + wvo) = xs[g - 40];
              }
              if constexpr (g >= 28 && g < 48) {
                constexpr int i = (g - 28) / 5, s = (g - 28) % 5;
                W2[s][i] = ld16(rs, wvo + (s & 3) * 1024, sw2[i] + (6 + (s >> 2) * 4) * 1024);
              }
              if constexpr (g == 38) pv = poll_ld(rs, nxt_poll, false);
              if constexpr (g == 50) poll_wait(rs, nxt_poll, false, pv);
              if constexpr (g >= 50 && g < 58) hf[g - 50][0] = ld16(rs, wvo + ((g - 50) & 3) * 1024, hb_e + ((g - 50) >> 2) * 4096);
            }
          };
          static_for<60>([&](auto gc) {
            constexpr int g = IC(gc), ks = g >> 2, j = g & 3;
            const u32x4 x = {xb[ks & 1][j][0][0], xb[ks & 1][j][0][1], xb[ks & 1][j][1][0], xb[ks & 1][j][1][1]};
            mfma_aA<12 * ks, ks == 0>(ga[0][j], x);
            xb[(ks + 1) & 1][j][0] = tr_half(xr[j] + (ks + 1) * 4096);
            SB();
            mfma_aA<12 * ks + 4, ks == 0>(ga[1][j], x);
            xb[(ks + 1) & 1][j][1] = tr_half(xr[j] + (ks + 1) * 4096 + 512);
            SB();
            mfma_aA<12 * ks + 8, ks == 0>(ga[2][j], x);
            SB();
            fill(gc);
            SB();
            if constexpr (j == 0) { mfma_v<ks == 0>(sa, ones, x); SB(); mfma_v<ks == 0>(qa, x, x); SB(); }
          });
          u32x4 x[4];
#pragma unroll
          for (int j = 0; j < 4; ++j) x[j] = (u32x4){xb[1][j][0][0], xb[1][j][0][1], xb[1][j][1][0], xb[1][j][1][1]};
          mfma_vv(sa, ones, x[0]); SB();
          mfma_vv(qa, x[0], x[0]); SB();
#pragma unroll
          for (int j = 0; j < 4; ++j) { mfma_aA<180, false>(ga[0][j], x[j]); SB(); }
#pragma unroll
          for (int j = 0; j < 4; ++j) { mfma_aA<184, false>(ga[1][j], x[j]); SB(); }
          asm volatile("" : "+v"(sa), "+v"(qa));
          {
            const int r = c16 & 3;
            const float q01 = (r & 1) ? qa[1] : qa[0];
            SB();
            mfma_aA<188, false>(ga[2][0], x[0]); SB();
            const float q23 = (r & 1) ? qa[3] : qa[2];
            const float q = (r & 2) ? q23 : q01;
            SB();
            mfma_aA<188, false>(ga[2][1], x[1]); SB();
            *(f32x2*)ssw = (f32x2){sa[0], q};
            SB();
          }
          mfma_aA<188, false>(ga[2][2], x[2]); SB();
          mfma_aA<188, false>(ga[2][3], x[3]); SB();
        }
        STAMP();
        f32x4 cs[3];
#pragma unroll
        for (int i = 0; i < 3; ++i) cs[i] = *(const f32x4*)(smem + LD_CS + w * 192 + i * 64 + kq * 16);
        if constexpr (h == 0) {
#pragma unroll
          for (int i = 0; i < 8; ++i) *(u32x4*)(xw + i * 4096) = xs[i];
        }
        SB();
        bar();
        f32x2 st[4];
#pragma unroll
        for (int j = 0; j < 4; ++j) st[j] = *(const f32x2*)(smem + LD_SS + (((w + j) & 3) * 16 + c16) * 8);
        if constexpr (h == 0) {
#pragma unroll
          for (int i = 0; i < 8; ++i) xs[i] = ld16(rs, gx, gs + (8 + i) * XS_STEP);
#pragma unroll
          for (int j = 0; j < 4; ++j) { xb[0][j][0] = tr_half(xr[j]); xb[0][j][1] = tr_half(xr[j] + 512); }
        } else {
#pragma unroll
          for (int s = 8; s < 11; ++s) hf[s][0] = ld16(rs, wvo, hb_e + (s == 10 ? s10 : s * 1024));
#pragma unroll
          for (int s = 0; s < 5; ++s) hf[s][1] = ld16(rs, wvo + (s & 3) * 1024, hb_e + NT1 * 256 + (s >> 2) * 4096);
        }
        tie4(ga[0][0], ga[0][1], ga[0][2], ga[0][3]);
        tie4(ga[1][0], ga[1][1], ga[1][2], ga[1][3]);
        tie4(ga[2][0], ga[2][1], ga[2][2], ga[2][3]);
        SB();
        u32 hd[3][4];
#pragma unroll
        for (int j = 0; j < 4; ++j) {
          const f32x2 mv = ln_mv(st[j]);
          const float nl = __builtin_amdgcn_rsqf(mv[1]) * -1.4426950408889634f;
#pragma unroll
          for (int i = 0; i < 3; ++i) {
            const f32x4 v = __builtin_elementwise_fma(cs[i], (f32x4){mv[0], mv[0], mv[0], mv[0]}, ga[i][j]);
            const f32x2 g = {v[0], v[1]}, u = {v[2], v[3]};
            const f32x2 a = g * nl;
            const f32x2 e = {__builtin_amdgcn_exp2f(a[0]), __builtin_amdgcn_exp2f(a[1])};
            const f32x2 d = __builtin_elementwise_fma(e, (f32x2){mv[1], mv[1]}, (f32x2){mv[1], mv[1]});
            const f32x2 r = {__builtin_amdgcn_rcpf(d[0]), __builtin_amdgcn_rcpf(d[1])};
            hd[i][j] = pk_bf16((g * u) * r);
          }
        }
        const int hb = (((w + kq) & 3) * NT1 * 16 + c16) * 16, hs = h_off + ((h * 64 + rt * 4) * NT1 + nt0) * 256;
#pragma unroll
        for (int i = 0; i < 3; ++i) {
          auto a02 = __builtin_amdgcn_permlane32_swap(hd[i][0], hd[i][2], false, false);
          auto a13 = __builtin_amdgcn_permlane32_swap(hd[i][1], hd[i][3], false, false);
          auto o01 = __builtin_amdgcn_permlane16_swap(a02[0], a13[0], false, false);
          auto o23 = __builtin_amdgcn_permlane16_swap(a02[1], a13[1], false, false);
          u32x4 v = {o01[0], o01[1], o23[0], o23[1]};
          if (i < 2 || ok2) st16(rs, hb + i * 256, hs, v);
        }
        SB();
        STAMP();
      });
    }
    {
      ROLES
      char* rw[3];
#pragma unroll
      for (int i = 0; i < 3; ++i) rw[i] = smem + LD_XT + (((w + 1 + i) & 3) * 3 + i) * 2048 + wvo;
      const char* rb = smem + LD_XT + w * 6144 + wvo;
      const int rvo = c16 * (L * 2) + kq * 8;
      const int ovo = c16 * (L * 2) + (kq & 1) * 32 + (kq >> 1) * 16;
      char* ow = smem + LD_OT + (w * 16 + c16) * 64 + (kq & 1) * 32 + (kq >> 1) * 16;
      const char* ob = smem + LD_OT + (8 * kq + (c16 >> 2)) * 64 + (w & 1) * 32 + (c16 & 3) * 8;
      const char* wlp = smem + LD_W2 + w * 8192 + wvo;
      static_for<2>([&](auto hc) {
        constexpr int h = IC(hc);
        const int tok0 = h * 1024 + rt2 * 32;
        const int eso = ((ct * 64 + w * 16) * L + tok0) * 2;
        const int hb = h ? hb_l : hb_e;
        f32x4 da[4][2];
        u32x4 wl[2][4];
        u32x2 res[2];
        u32 pv;
        static_for<88>([&](auto nc) {
          constexpr int n = IC(nc), j = n / 44, s = (n % 44) >> 2, i = (n + 1) & 3;
          if constexpr (s < 4) mfma_aB<192 + 16 * s + 4 * i, s == 0>(da[i][j], hf[s][j]);
          else if constexpr (s < 6) mfma_vv(da[i][j], hf[s][j], wl[s - 4][i]);
          else mfma_vv(da[i][j], hf[s][j], W2[s - 6][i]);
          SB();
          if constexpr (n < 24 && (n & 3) == 3)
            hf[5 + (n >> 2)][1] = ld16(rs, wvo, hb + NT1 * 256 + ((n >> 2) == 5 ? s10 : (5 + (n >> 2)) * 1024));
          if constexpr (n % 44 >= 6 && n % 44 < 14) wl[(n % 44 - 6) >> 2][(n % 44 - 5) & 3] = *(const u32x4*)(wlp + (((n % 44 - 6) & 4) + ((n % 44 - 5) & 3)) * 1024);
          if constexpr (n == 25) res[0] = ld8(rs, rvo, x1_off + eso);
          if constexpr (n == 26) res[1] = ld8(rs, rvo + 32, x1_off + eso);
          if constexpr (h == 0 && n == 40) { drain_bar<8, false>(); signal_grp(rs, layer * 32 + 16 + rt, w); }
          if constexpr (n == 52 || n == 56 || n == 60) { asm volatile("" : "+v"(da[(n - 48) >> 2][0])); *(f32x4*)(rw[(n - 52) >> 2]) = da[(n - 48) >> 2][0]; }
          if constexpr (h == 1 && n == 64) pv = poll_ld(rs, all_off(2, b), true);
          SB();
        });
        mfma_done4(da[0][1], da[1][1], da[2][1], da[3][1]);
        SB();
        STAMP();
#pragma unroll
        for (int i = 0; i < 3; ++i) *(f32x4*)(rw[i] + 1024) = da[i + 1][1];
        if constexpr (h == 0) pv = poll_ld(rs, grp_off(layer * 32 + 16 + (rt2 >> 1)), false);
        bar();
        u32x2 xo[2];
#pragma unroll
        for (int j = 0; j < 2; ++j) {
          const f32x4 rf = {lo_f(res[j][0]), hi_f(res[j][0]), lo_f(res[j][1]), hi_f(res[j][1])};
          const f32x4 v = ((da[0][j] + rf) + *(const f32x4*)(rb + j * 1024)) + (*(const f32x4*)(rb + 2048 + j * 1024) + *(const f32x4*)(rb + 4096 + j * 1024));
          xo[j] = (u32x2){pk_bf16(v[0], v[1]), pk_bf16(v[2], v[3])};
        }
        auto s0 = __builtin_amdgcn_permlane16_swap(xo[0][0], xo[1][0], false, false);
        auto s1 = __builtin_amdgcn_permlane16_swap(xo[0][1], xo[1][1], false, false);
        const u32x4 xv = {s0[0], s1[0], s0[1], s1[1]};
        *(u32x4*)ow = xv;
        if constexpr (layer == 0) st16(rs, ovo, OFF_XT + ACT + eso, xv);
        bar();
        if constexpr (layer == 0) {
          if (w < 2) {
            u32x4 f0 = tr_frag(ob, 0, 256), f1 = tr_frag(ob, 2048, 2048 + 256);
            const u32x4 ones = splat4(0x3f803f80u);
            f32x4 sa, qa;
            mfma_v0(sa, ones, f0); mfma_v0(qa, f0, f0); mfma_vv(sa, ones, f1); mfma_vv(qa, f1, f1);
            asm volatile("s_nop 7" : "+v"(sa), "+v"(qa));
            const int r = c16 & 3;
            float q = r == 0 ? qa[0] : r == 1 ? qa[1] : r == 2 ? qa[2] : qa[3];
            u32x2 v = {__float_as_uint(sa[0]), __float_as_uint(q)};
            __builtin_amdgcn_raw_buffer_store_b64(v, rs, (c16 >> 2) == kq ? c16 * 8 : OOB, OFF_ST1 + (ct * L + tok0 + w * 16) * 8, SC1);
          }
        } else {
          u32x4 f = tr_frag(ob, (w >> 1) * 2048, (w >> 1) * 2048 + 256);
          const rsrc_t ro = __builtin_amdgcn_make_buffer_rsrc(out, 0, L * D * 2, 0x00020000);
          __builtin_amdgcn_raw_buffer_store_b128(f, ro, (c16 * D + kq * 8) * 2, ((tok0 + (w & 1) * 16) * D + ct * 64 + (w >> 1) * 32) * 2, 0);
        }
        SB();
        STAMP();
        if constexpr (h == 0) {
          poll_wait(rs, grp_off(layer * 32 + 16 + (rt2 >> 1)), false, pv);
          STAMP();
#pragma unroll
          for (int s = 0; s < 11; ++s) hf[s][0] = ld16(rs, wvo + (s < 8 ? (s & 3) * 1024 : 0), hb_l + (s < 8 ? (s >> 2) * 4096 : s == 10 ? s10 : s * 1024));
          SB();
          if constexpr (layer == 0) { drain_bar<11, false>(); signal_all(rs, 2, b, w); }
          SB();
#pragma unroll
          for (int s = 0; s < 5; ++s) hf[s][1] = ld16(rs, wvo + (s & 3) * 1024, hb_l + NT1 * 256 + (s >> 2) * 4096);
          SB();
        } else {
          if constexpr (layer == 0) {
            poll_wait(rs, all_off(2, b), true, pv);
            STAMP();
            prep_load<8>(rs, tid, OFF_XT + ACT + b * 2 * L * 2, OFF_ST1, pr);
            drain_bar<18, false>();
            signal_all(rs, 3, b, w);
          }
        }
      });
    }
    if constexpr (layer == 0) {
      ROLES
      const int xo = b * 2 * L * 2;
      f32x4 z[2], acc[4];
      u32 pv;
      prep_ln<8>(smem, 0, tid, dsk, pr, z);
      bar();
      STAMP();
      conv_mfma<0, 0, 3, true>(smem, w, lane, acc, [&](auto) {});
      conv_out(rs, smem, OFF_X1 + ACT + xo, w, lane, acc, z);
      STAMP();
      conv_mfma<4, 0, 3, true>(smem, w, lane, acc, [&](auto nc) {
        constexpr int n = IC(nc);
        if constexpr (n == 2) pv = poll_ld(rs, all_off(3, b), true);
        if constexpr (n == 26) { drain_bar<0, false>(); signal_all(rs, 4, b, w); poll_wait(rs, all_off(3, b), true, pv); }
        if constexpr (n >= 27 && n < 63 && (n & 1)) prep_ld1<8>(rs, (n - 27) >> 1, tid, OFF_XT + ACT + xo + 2048, OFF_ST1 + 8192, pr);
      });
      STAMP();
      prep_ln<8>(smem, 1, tid, dsk, pr, z);
      bar();
      STAMP();
      conv_mfma<4, 4, 7, false>(smem, w, lane, acc, [&](auto nc) { if constexpr (IC(nc) == 12) pv = poll_ld(rs, all_off(4, b), true); });
      STAMP();
      poll_wait(rs, all_off(4, b), true, pv);
      STAMP();
      const int gx = xs_voff(tid), gs = OFF_X1 + ACT + rt * 128;
#pragma unroll
      for (int i = 0; i < 8; ++i) xs[i] = ld16(rs, gx, gs + i * XS_STEP);
      SB();
      conv_out(rs, smem, OFF_X1 + ACT + xo + 2048, w, lane, acc, z);
      char* xw = xs_lds(smem, tid);
#pragma unroll
      for (int i = 0; i < 8; ++i) { *(u32x4*)(xw + i * 4096) = xs[i]; xs[i] = ld16(rs, gx, gs + (8 + i) * XS_STEP); }
      bar();
      STAMP();
    }
  });
  STAMP_OUT();
  asm volatile(".Lmk_code_end:" ::: "memory");
}

extern "C" void kernel_launch(void* const* d_in, const int* in_sizes, int n_in, void* d_out, int out_size,
                              void* d_ws, size_t ws_size, hipStream_t stream) {
  char* ws = (char*)d_ws;
  (void)hipFuncSetAttribute((const void*)main_kernel, hipFuncAttributeMaxDynamicSharedMemorySize, LDS_BYTES + LDS_EXTRA);
  (void)hipMemsetAsync(ws, 0, CTRL_BYTES, stream);
  main_kernel<<<256, 256, LDS_BYTES + LDS_EXTRA, stream>>>(
      (const u16*)d_in[0], (const u16*)d_in[1], (const u16*)d_in[2], (const u16*)d_in[3], (const u16*)d_in[4],
      (const u16*)d_in[5], (const u16*)d_in[6], (const u16*)d_in[7], (const u16*)d_in[8], (const u16*)d_in[10],
      (const u16*)d_in[11], (const u16*)d_in[12], ws, (u16*)d_out);
}
```

```cpp
#include <hip/hip_runtime.h>
#include <stdint.h>
#include <type_traits>

typedef unsigned short u16;
typedef unsigned int u32;
typedef u32 u32x4 __attribute__((ext_vector_type(4)));
typedef u32 u32x2 __attribute__((ext_vector_type(2)));
typedef float f32x4 __attribute__((ext_vector_type(4)));
typedef float f32x2 __attribute__((ext_vector_type(2)));
typedef short s16x4 __attribute__((ext_vector_type(4)));
typedef __bf16 bf16x2 __attribute__((ext_vector_type(2)));
typedef _Float16 f16x2 __attribute__((ext_vector_type(2)));
#define LAS __attribute__((address_space(3)))
#define GAS __attribute__((address_space(1)))
#define DEV __device__ __forceinline__
#define SB() __builtin_amdgcn_sched_barrier(0)
#define IC(x) decltype(x)::value
typedef __amdgpu_buffer_rsrc_t rsrc_t;

constexpr int L = 2048, D = 512, NS = 128, F = 1376;
constexpr int NT1 = 172;
constexpr int KS1 = 16;
constexpr int KS2P = 44;
constexpr float EPS = 1e-5f;
constexpr int SC1 = 16;
constexpr int P_SIG_CL = 9;
constexpr int P_SIG_HE = 12;
constexpr int P_SIG_HL = 44;
constexpr int P_SIG_XE = 30;
constexpr int P_POLL_G = 52;
constexpr int CE_P = 14;
constexpr int X0_P = 13;
constexpr int NSH = 16;
constexpr int ACC_E = 74;
constexpr int ACC_L = 20;
constexpr int SIG_CL = P_SIG_CL, SIG_HE = P_SIG_HE, SIG_HL = P_SIG_HL, SIG_XE = P_SIG_XE, POLL_G = P_POLL_G;

constexpr int XPAD = 64;
constexpr int LP = L + XPAD;
constexpr int ACT = D * LP * 2;
constexpr int HID = F * L * 2;
constexpr int N_ALL = 7;
constexpr int OFF_CA  = 0;
constexpr int OFF_CH  = OFF_CA + N_ALL * 128 * 256;
constexpr int OFF_INIT = OFF_CH + 64 * 256;
constexpr int CTRL_BYTES = OFF_INIT + 8 * 256;
constexpr int OFF_XT  = CTRL_BYTES;
constexpr int ACT2 = ACT, HID2 = HID;
constexpr int OFF_X1  = OFF_XT + ACT + ACT2;
constexpr int OFF_H   = OFF_X1 + ACT + ACT2;
constexpr int OFF_WGU = OFF_H + HID + HID2;
constexpr int OFF_WD  = OFF_WGU + (NT1 + 1) * KS1 * 1024;
constexpr int OFF_ST0 = OFF_WD + 32 * KS2P * 1024;
constexpr int OFF_ST1 = OFF_ST0 + L * 8;
constexpr int OFF_AC  = OFF_ST1 + 8 * L * 8;
constexpr int WS_END  = OFF_AC + L * 16;

DEV float lo_f(u32 v) { return __uint_as_float(v << 16); }
DEV float hi_f(u32 v) { return __uint_as_float(v & 0xffff0000u); }
DEV u32 pk_bf16(float a, float b) {
  f32x2 v = {a, b};
  return __builtin_bit_cast(u32, __builtin_convertvector(v, bf16x2));
}
DEV u32 pk_bf16(f32x2 v) { return __builtin_bit_cast(u32, __builtin_convertvector(v, bf16x2)); }
DEV u32 pk_f16(f32x2 v) { return __builtin_bit_cast(u32, __builtin_convertvector(v, f16x2)); }
DEV f32x2 unpk(u32 v) { return (f32x2){lo_f(v), hi_f(v)}; }
DEV float rbf(float a) { return lo_f(pk_bf16(a, a)); }
DEV f32x2 ln_mv(f32x2 sq) {
  f32x2 t = __builtin_elementwise_fma(sq, (f32x2){1.0f / D, 1.0f / D}, (f32x2){0.f, EPS});
  t[1] = fmaxf(__builtin_fmaf(-t[0], t[0], t[1]), EPS);
  return t;
}
struct CM { f32x2 re, im; };
DEV CM mkcm(f32x2 c) { return CM{(f32x2){c[0], c[0]}, (f32x2){-c[1], c[1]}}; }
DEV f32x2 cmul_h(f32x2 x, const CM& c, u32& h) {
  const f32x2 r = __builtin_elementwise_fma(x.yx, c.im, x * c.re);
  h = pk_f16(r);
  return r;
}

constexpr int KCOPY = 4160, KCH = 4 * KCOPY;
constexpr int LD_U = 2 * KCH;
constexpr int UCH = (256 + L) * 2;
constexpr int LD_XT = LD_U + 2 * UCH;
constexpr int LD_Y = LD_XT + 65536;
constexpr int YW = 5120;
constexpr int LD_SS = LD_Y + 4 * YW;
constexpr int LD_OT = LD_Y;
constexpr int LD_CS = LD_SS + 512;
constexpr int LD_W2 = LD_CS + 768;
constexpr int LDS_BYTES = LD_W2 + 32768;
static_assert(LDS_BYTES <= 163840, "LDS");
constexpr int LD_KT = LD_XT + 16384;
constexpr int RROW = 144, RW = 64 * RROW;

#define ACLOB "a0","a1","a2","a3","a4","a5","a6","a7","a8","a9","a10","a11","a12","a13","a14","a15","a16","a17","a18","a19","a20","a21","a22","a23","a24","a25","a26","a27","a28","a29","a30","a31","a32","a33","a34","a35","a36","a37","a38","a39","a40","a41","a42","a43","a44","a45","a46","a47","a48","a49","a50","a51","a52","a53","a54","a55","a56","a57","a58","a59","a60","a61","a62","a63","a64","a65","a66","a67","a68","a69","a70","a71","a72","a73","a74","a75","a76","a77","a78","a79","a80","a81","a82","a83","a84","a85","a86","a87","a88","a89","a90","a91","a92","a93","a94","a95","a96","a97","a98","a99","a100","a101","a102","a103","a104","a105","a106","a107","a108","a109","a110","a111","a112","a113","a114","a115","a116","a117","a118","a119","a120","a121","a122","a123","a124","a125","a126","a127","a128","a129","a130","a131","a132","a133","a134","a135","a136","a137","a138","a139","a140","a141","a142","a143","a144","a145","a146","a147","a148","a149","a150","a151","a152","a153","a154","a155","a156","a157","a158","a159","a160","a161","a162","a163","a164","a165","a166","a167","a168","a169","a170","a171","a172","a173","a174","a175","a176","a177","a178","a179","a180","a181","a182","a183","a184","a185","a186","a187","a188","a189","a190","a191","a192","a193","a194","a195","a196","a197","a198","a199","a200","a201","a202","a203","a204","a205","a206","a207","a208","a209","a210","a211","a212","a213","a214","a215","a216","a217","a218","a219","a220","a221","a222","a223","a224","a225","a226","a227","a228","a229","a230","a231","a232","a233","a234","a235","a236","a237","a238","a239","a240","a241","a242","a243","a244","a245","a246","a247","a248","a249","a250","a251","a252","a253","a254","a255"
#define MF "v_mfma_f32_16x16x32_bf16 "
DEV void mfma_vv(f32x4& c, u32x4 a, u32x4 b) { asm(MF "%0, %1, %2, %0" : "+v"(c) : "v"(a), "v"(b)); }
DEV void mfma_v0(f32x4& c, u32x4 a, u32x4 b) { asm(MF "%0, %1, %2, 0" : "=&v"(c) : "v"(a), "v"(b)); }
template <bool FIRST> DEV void mfma_v(f32x4& c, u32x4 a, u32x4 b) { if constexpr (FIRST) mfma_v0(c, a, b); else mfma_vv(c, a, b); }
template <int R, bool FIRST> DEV void mfma_aA(f32x4& c, u32x4 b) {
  if constexpr (FIRST) asm(MF "%0, a[%c2:%c3], %1, 0" : "=&v"(c) : "v"(b), "i"(R), "i"(R + 3));
  else asm(MF "%0, a[%c2:%c3], %1, %0" : "+v"(c) : "v"(b), "i"(R), "i"(R + 3));
}
template <int R, bool FIRST> DEV void mfma_aB(f32x4& c, u32x4 a) {
  if constexpr (FIRST) asm(MF "%0, %1, a[%c2:%c3], 0" : "=&v"(c) : "v"(a), "i"(R), "i"(R + 3));
  else asm(MF "%0, %1, a[%c2:%c3], %0" : "+v"(c) : "v"(a), "i"(R), "i"(R + 3));
}
template <bool FIRST> DEV void mfma_h(f32x4& c, u32x4 a, u32x4 b) {
  if constexpr (FIRST) asm("v_mfma_f32_16x16x32_f16 %0, %1, %2, 0" : "=&v"(c) : "v"(a), "v"(b));
  else asm("v_mfma_f32_16x16x32_f16 %0, %1, %2, %0" : "+v"(c) : "v"(a), "v"(b));
}
DEV void mfma_done4(f32x4& a, f32x4& b, f32x4& c, f32x4& d) { asm volatile("s_nop 7" : "+v"(a), "+v"(b), "+v"(c), "+v"(d)); }
DEV void tie4(f32x4& a, f32x4& b, f32x4& c, f32x4& d) { asm volatile("" : "+v"(a), "+v"(b), "+v"(c), "+v"(d)); }
DEV void bar() { asm volatile("s_waitcnt lgkmcnt(0)\n\ts_barrier" ::: "memory", ACLOB); }

DEV u32x4 ld16(rsrc_t rs, int voff, int soff) { return __builtin_amdgcn_raw_buffer_load_b128(rs, voff, soff, SC1); }
DEV u32x2 ld8(rsrc_t rs, int voff, int soff) { return __builtin_amdgcn_raw_buffer_load_b64(rs, voff, soff, SC1); }
DEV void st16(rsrc_t rs, int voff, int soff, u32x4 v) { __builtin_amdgcn_raw_buffer_store_b128(v, rs, voff, soff, SC1); }
DEV int fresh_lane() {
  int l;
  asm volatile("v_mbcnt_lo_u32_b32 %0, -1, 0\n\tv_mbcnt_hi_u32_b32 %0, -1, %0" : "=v"(l));
  return l;
}
DEV u32x4 splat4(u32 c) {
  u32x4 r;
  asm volatile("v_mov_b32 %0, %4\n\tv_mov_b32 %1, %4\n\tv_mov_b32 %2, %4\n\tv_mov_b32 %3, %4"
               : "=v"(r[0]), "=v"(r[1]), "=v"(r[2]), "=v"(r[3]) : "i"(c));
  asm volatile("s_nop 1" : "+v"(r));
  return r;
}
DEV int opq(int x) { asm volatile("" : "+s"(x)); return x; }
DEV u32x2 tr_half(const char* p) { return __builtin_bit_cast(u32x2, __builtin_amdgcn_ds_read_tr16_b64_v4i16((LAS s16x4*)p)); }
DEV u32x4 tr_frag(const char* p, int o0, int o1) {
  u32x2 a = tr_half(p + o0), c = tr_half(p + o1);
  return (u32x4){a[0], a[1], c[0], c[1]};
}
template <int N, typename FN, int I = 0>
DEV void static_for(FN&& fn) {
  if constexpr (I < N) { fn(std::integral_constant<int, I>{}); static_for<N, FN, I + 1>(static_cast<FN&&>(fn)); }
}

constexpr int OOB = 0x7ffffff0;
constexpr int SPIN_CAP = 400000;
constexpr int ACC_SLEEP = 2;
DEV int all_off(int edge, int b) { return OFF_CA + (edge * 8 + (b & 7)) * 4096; }
DEV int grp_off(int idx) { return OFF_CH + idx * 256; }
DEV u32 poll_ld(rsrc_t rs, int soff, bool all, int w) {
  const int voff = (all ? (fresh_lane() & (NSH - 1)) * 256 : 0) | (w == 3 ? 0 : 0x7ff00000);
  return __builtin_amdgcn_raw_buffer_load_b32(rs, voff, soff, SC1);
}
DEV void poll_spin(rsrc_t rs, int soff, bool all, u32 f) {
  if (__builtin_expect(!__all(f >= 64u), 0)) {
    const int voff = all ? (fresh_lane() & (NSH - 1)) * 256 : 0;
    int spins = SPIN_CAP;
    u32 t;
    asm volatile("s_nop 4\n"
                 "1:\n\t"
                 "buffer_load_dword %0, %2, %3, %4 offen sc1\n\t"
                 "s_sub_u32 %1, %1, 1\n\t"
                 "s_cbranch_scc1 2f\n\t"
                 "s_waitcnt vmcnt(0)\n\t"
                 "v_cmp_gt_u32 vcc, 64, %0\n\t"
                 "s_nop 4\n\t"
                 "s_cbranch_vccnz 1b\n"
                 "2:\n\t"
                 "s_waitcnt vmcnt(0)"
                 : "=&v"(t), "+s"(spins) : "v"(voff), "s"(rs), "s"(soff) : "memory", "vcc", "scc");
  }
  asm volatile("" ::: "memory");
}
DEV void poll_wait(rsrc_t rs, int soff, bool all, u32 f, int w) {
  if (w == 3) poll_spin(rs, soff, all, f);
  asm volatile("s_barrier" ::: "memory", ACLOB);
}
DEV void poll_wait2(rsrc_t rs, int soff, bool all, u32 f0, u32 f1, int w) {
  if (w == 3) { if (!__all(f0 >= 64u)) poll_spin(rs, soff, all, f1); }
  asm volatile("s_barrier" ::: "memory", ACLOB);
}
template <int YOUNGER, bool LDSW>
DEV void drain_bar() {
  if constexpr (LDSW) asm volatile("s_waitcnt vmcnt(%c0) lgkmcnt(0)\n\ts_barrier" :: "i"(YOUNGER) : "memory", ACLOB);
  else asm volatile("s_waitcnt vmcnt(%c0)\n\ts_barrier" :: "i"(YOUNGER) : "memory", ACLOB);
}
DEV void signal_all(rsrc_t rs, int edge, int b, int w) {
  if (w == 0) {
    const int lane = fresh_lane();
    __builtin_amdgcn_raw_ptr_buffer_atomic_add_i32(NSH / 4, rs, lane < 8 ? lane * 4096 : OOB, OFF_CA + edge * 32768 + (b & (NSH - 1)) * 256, SC1);
  }
}
DEV void signal_grp(rsrc_t rs, int idx, int w) {
  if (w == 0) {
    const int lane = fresh_lane();
    __builtin_amdgcn_raw_ptr_buffer_atomic_add_i32(4, rs, lane == 0 ? 0 : OOB, OFF_CH + idx * 256, SC1);
  }
}

DEV void init_bump(rsrc_t rs) {
  const int lane = fresh_lane();
  __builtin_amdgcn_raw_ptr_buffer_atomic_add_i32(1, rs, lane < 8 ? lane * 256 : OOB, OFF_INIT, SC1);
}
DEV void init_wait(rsrc_t rs, int soff, u32 f0, u32 f) {
  if (__builtin_expect(__any(f == f0), 0)) {
    int spins = SPIN_CAP;
    u32 t;
    asm volatile("s_nop 4\n"
                 "1:\n\t"
                 "buffer_load_dword %0, off, %3, %4 sc1\n\t"
                 "s_sub_u32 %1, %1, 1\n\t"
                 "s_cbranch_scc1 2f\n\t"
                 "s_waitcnt vmcnt(0)\n\t"
                 "v_cmp_eq_u32 vcc, %2, %0\n\t"
                 "s_nop 4\n\t"
                 "s_cbranch_vccnz 1b\n"
                 "2:\n\t"
                 "s_waitcnt vmcnt(0)"
                 : "=&v"(t), "+s"(spins) : "v"(f0), "s"(rs), "s"(soff) : "memory", "vcc", "scc");
  }
  asm volatile("" ::: "memory");
}

template <int P> struct PrepRegs { u32x4 pa[P], pb[P]; u32x2 xv[2]; };
template <int P>
DEV void prep_ld1(rsrc_t rs, int i, int tid, int xso, int sso, PrepRegs<P>& pr) {
  if (i < 2 * P) {
    if (i & 1) pr.pb[i >> 1] = ld16(rs, tid * 32 + 16, sso + (i >> 1) * (L * 8));
    else pr.pa[i >> 1] = ld16(rs, tid * 32, sso + (i >> 1) * (L * 8));
  } else pr.xv[i - 2 * P] = ld8(rs, tid * 8, xso + (i - 2 * P) * (LP * 2));
}
template <int P>
DEV void prep_load(rsrc_t rs, int tid, int xso, int sso, PrepRegs<P>& pr) {
#pragma unroll
  for (int i = 0; i < 2 * P + 2; ++i) prep_ld1<P>(rs, i, tid, xso, sso, pr);
  SB();
}
DEV void kill4(u32x4& x) { asm volatile("" : "=v"(x)); }
DEV void kill2(u32x2& x) { asm volatile("" : "=v"(x)); }
template <int P> DEV void prep_kill(PrepRegs<P>& pr) {
#pragma unroll
  for (int p = 0; p < P; ++p) { kill4(pr.pa[p]); kill4(pr.pb[p]); }
  kill2(pr.xv[0]); kill2(pr.xv[1]);
}
DEV void ln_apply(char* smem, int h, int tid, u32 dsk, f32x4 s0, f32x4 s1, const u32x2 (&xv)[2], f32x4 (&z)[2]) {
  const f32x2 t0 = ln_mv(s0.xy), t1 = ln_mv(s0.zw), t2 = ln_mv(s1.xy), t3 = ln_mv(s1.zw);
  const f32x2 rr[2] = {{__builtin_amdgcn_rsqf(t0[1]), __builtin_amdgcn_rsqf(t1[1])}, {__builtin_amdgcn_rsqf(t2[1]), __builtin_amdgcn_rsqf(t3[1])}};
  const f32x2 mr[2] = {{-t0[0] * rr[0][0], -t1[0] * rr[0][1]}, {-t2[0] * rr[1][0], -t3[0] * rr[1][1]}};
#pragma unroll
  for (int c = 0; c < 2; ++c) {
    const float dk = c ? hi_f(dsk) : lo_f(dsk);
    u32x2 u;
    f32x2 zz[2];
#pragma unroll
    for (int i = 0; i < 2; ++i) {
      const f32x2 x = unpk(xv[c][i]);
      const f32x2 uf = __builtin_elementwise_fma(x, rr[i], mr[i]);
      u[i] = pk_bf16(uf);
      zz[i] = __builtin_elementwise_fma(uf, (f32x2){dk, dk}, x);
    }
    z[c] = (f32x4){zz[0][0], zz[0][1], zz[1][0], zz[1][1]};
    *(u32x2*)(smem + LD_U + c * UCH + (256 + h * 1024) * 2 + tid * 8) = u;
  }
}
DEV void prep_ln(char* smem, int h, int tid, u32 dsk, const PrepRegs<1>& pr, f32x4 (&z)[2]) {
  ln_apply(smem, h, tid, dsk, __builtin_bit_cast(f32x4, pr.pa[0]), __builtin_bit_cast(f32x4, pr.pb[0]), pr.xv, z);
}
struct AccRegs { u32x4 a[4]; u32x2 xv[2]; };
DEV void acc_ld1(rsrc_t rs, int i, int tid, int xso, int h, AccRegs& r) {
  if (i < 4) r.a[i] = ld16(rs, tid * 16, OFF_AC + h * 16384 + i * 4096);
  else r.xv[i - 4] = ld8(rs, tid * 8, xso + (i - 4) * (LP * 2));
}
DEV u32x2 to_fix(float t) {
  const float hi = __builtin_floorf(t * 0x1p-32f);
  const u32 lo = (u32)fminf(__builtin_fmaf(hi, -0x1p32f, t), 4294967040.f);
  return (u32x2){(lo & ~15u) | 1u, (u32)(int)hi};
}
DEV bool acc_ready(const AccRegs& r) {
  u32 o = 0u, n = ~0u;
#pragma unroll
  for (int i = 0; i < 4; ++i) { o |= r.a[i][0] | r.a[i][2]; n &= r.a[i][0] & r.a[i][2]; }
  const u32 mx = max(max(r.xv[0][0], r.xv[0][1]), max(r.xv[1][0], r.xv[1][1]));
  return __all(((o & 7u) | (~n & 8u)) == 0u && mx != ~0u);
}
DEV void acc_wait(rsrc_t rs, int tid, int xso, int h, int poll_off, AccRegs& r, bool ready) {
  if (__builtin_expect(!ready, 0)) {
#pragma unroll 1
    for (int spins = 0; spins < SPIN_CAP; ++spins) {
      bool done = false;
      if ((spins & 7) == 7) done = __all(__builtin_amdgcn_raw_buffer_load_b32(rs, (tid & (NSH - 1)) * 256, poll_off, SC1) >= 64u);
      asm volatile("" ::: "memory");
#pragma unroll
      for (int i = 0; i < 6; ++i) acc_ld1(rs, i, tid, xso, h, r);
      if (done || acc_ready(r)) break;
      __builtin_amdgcn_s_sleep(ACC_SLEEP);
    }
    __builtin_amdgcn_s_waitcnt(0x0F70);
  }
}
DEV void acc_ln(rsrc_t rs, char* smem, int h, int tid, u32 dsk, const AccRegs& r, f32x4 (&z)[2]) {
  f32x2 st[4];
#pragma unroll
  for (int i = 0; i < 4; ++i) {
    const f32x2 hi = {(float)(int)r.a[i][1], (float)(int)r.a[i][3]}, lo = {(float)r.a[i][0], (float)r.a[i][2]};
    st[i] = __builtin_elementwise_fma(hi, (f32x2){0x1p2f, 0x1p6f}, lo * (f32x2){0x1p-30f, 0x1p-26f});
  }
  f32x4 s0 = {st[0][0], st[0][1], st[1][0], st[1][1]}, s1 = {st[2][0], st[2][1], st[3][0], st[3][1]};
  const u32 mk = (r.a[0][3] | r.a[1][3]) | (r.a[2][3] | r.a[3][3]);
  if (__builtin_expect(__any(mk >= (1u << 27)), 0)) {
    poll_spin(rs, all_off(2 + h, blockIdx.x), true, 0u);
    s0 = s1 = (f32x4){0.f, 0.f, 0.f, 0.f};
#pragma unroll 1
    for (int p = 0; p < 8; ++p) {
      s0 += __builtin_bit_cast(f32x4, ld16(rs, tid * 32, OFF_ST1 + h * 8192 + p * (L * 8)));
      s1 += __builtin_bit_cast(f32x4, ld16(rs, tid * 32 + 16, OFF_ST1 + h * 8192 + p * (L * 8)));
    }
  }
  ln_apply(smem, h, tid, dsk, s0, s1, r.xv, z);
}
template <int M0, int I0, int I1> struct ConvPlan {
  static constexpr int Q0 = (M0 - I1) > 0 ? (M0 - I1) : 0, Q1 = M0 + 3 - I0;
  static constexpr int NQ = Q1 - Q0 + 1, NU = I1 - I0 + 1, NF = NQ + NU;
  static constexpr int count() { int n = 0; for (int i = I0; i <= I1; ++i) for (int m = 0; m < 4; ++m) n += (M0 + m - i >= 0); return n; }
  static constexpr int NM = count();
  static constexpr int im(int t) { int n = 0; for (int i = I0; i <= I1; ++i) for (int m = 0; m < 4; ++m) if (M0 + m - i >= 0) { if (n == t) return i * 4 + m; ++n; } return 0; }
  static constexpr int frag(int f) {
    if (f == 0) return 0;
    if (f <= 4) return -(Q1 - 4 + f + 1);
    int g = f - 5, nlow = NQ - 4;
    if (g < 2 * nlow) return (g & 1) ? -(Q1 - 4 - (g >> 1) + 1) : 1 + (g >> 1);
    return 1 + nlow + (g - 2 * nlow);
  }
};
template <int M0, int I0, int I1, bool FIRST, typename FN>
DEV void conv_mfma(char* smem, int w, int lane, f32x4 (&acc)[4], FN&& fill) {
  using P = ConvPlan<M0, I0, I1>;
  const int c16 = lane & 15, kq = lane >> 4;
  const int cch = w >> 1, wr = w & 1;
  const char* tb = smem + cch * KCH + (c16 >> 2) * KCOPY + (16 + 16 * (1 + 8 * wr) - 8 * kq + 4 * (c16 & 3) - 4) * 2;
  const char* ub = smem + LD_U + cch * UCH + (256 - 16 * (1 + 8 * wr + 6) + 16 * c16 + 8 * kq) * 2;
  u32x4 T[2][P::NQ], U[2][P::NU];
  auto ldf = [&](auto rc, auto fc) {
    constexpr int rr = IC(rc), f = P::frag(IC(fc));
    if constexpr (f >= 0) U[rr & 1][f] = *(const u32x4*)(ub + (3 - rr) * 64 + (I0 + f) * 512);
    else T[rr & 1][-f - 1 - P::Q0] = tr_frag(tb, rr * 64 + (-f - 1) * 512 + 8, rr * 64 + (-f - 1) * 512);
  };
  static_for<P::NF>([&](auto fc) { ldf(std::integral_constant<int, 0>{}, fc); });
  SB();
  static_for<4 * P::NM>([&](auto nc) {
    constexpr int n = IC(nc), rr = n / P::NM, t = n % P::NM, i = P::im(t) >> 2, m = P::im(t) & 3, q = M0 + m - i;
    mfma_v<FIRST && rr == 0 && i == I0>(acc[m], T[rr & 1][q - P::Q0], U[rr & 1][i - I0]);
    if constexpr (rr < 3 && t < P::NF) ldf(std::integral_constant<int, rr + 1>{}, std::integral_constant<int, t>{});
    SB();
    fill(nc);
    SB();
  });
}
DEV void conv_out(rsrc_t rs, char* smem, int xso, int w, int lane, f32x4 (&acc)[4], const f32x4 (&z)[2]) {
  const int tid = w * 64 + lane;
  char* yw = smem + LD_Y + w * YW + (lane & 15) * 80 + (lane >> 4) * 16;
  const char* yb = smem + LD_Y + tid * 16 + (tid >> 2) * 16;
  const int vo = ((tid & 1) * LP + (tid >> 1) * 8) * 2;
  mfma_done4(acc[0], acc[1], acc[2], acc[3]);
#pragma unroll
  for (int m = 0; m < 4; ++m) *(f32x4*)(yw + m * 1280) = acc[m];
  bar();
  u32x2 o[2];
#pragma unroll
  for (int c = 0; c < 2; ++c) {
    const f32x4 y = (*(const f32x4*)(yb + (2 * c) * YW) + z[c]) + *(const f32x4*)(yb + (2 * c + 1) * YW);
    o[c] = (u32x2){pk_bf16(y[0], y[1]), pk_bf16(y[2], y[3])};
  }
  const bool odd = tid & 1;
  u32 s0 = odd ? o[0][0] : o[1][0], s1 = odd ? o[0][1] : o[1][1];
  u32 g0 = __builtin_amdgcn_update_dpp(0u, s0, 0xB1, 0xf, 0xf, true);
  u32 g1 = __builtin_amdgcn_update_dpp(0u, s1, 0xB1, 0xf, 0xf, true);
  u32x4 v = odd ? (u32x4){g0, g1, o[1][0], o[1][1]} : (u32x4){o[0][0], o[0][1], g0, g1};
  st16(rs, vo, xso, v);
  SB();
}

template <int R, int IMM>
DEV void ld_agpr(rsrc_t rs, int voff, int sbase) {
  int tmp;
  asm volatile("s_add_u32 %0, %3, %c6\n\tbuffer_load_dwordx4 a[%c4:%c5], %1, %2, %0 offen sc1"
               : "=&s"(tmp) : "v"(voff), "s"(rs), "s"(sbase), "i"(R), "i"(R + 3), "i"(IMM) : "memory", "scc");
}
template <int I> DEV void ld_w1(rsrc_t rs, int voff, int so01, int so2) {
  constexpr int ks = I / 3, i = I % 3;
  if constexpr (i < 2) ld_agpr<4 * I, (i * KS1 + ks) * 1024>(rs, voff, so01);
  else ld_agpr<4 * I, ks * 1024>(rs, voff, so2);
}
template <int R, int IMM>
DEV void ld_agpr_imm(rsrc_t rs, int voff, int soff) {
  asm volatile("buffer_load_dwordx4 a[%c3:%c4], %0, %1, %2 offen offset:%c5 sc1"
               :: "v"(voff), "s"(rs), "s"(soff), "i"(R), "i"(R + 3), "i"(IMM) : "memory");
}

constexpr int XS_STEP = 32 * LP * 2;
DEV int xs_voff(int w, int lane) {
  const int fsw = ((lane >> 4) & 1) | ((w & 1) << 1);
  return (w * 8 + (lane >> 3)) * (LP * 2) + ((((lane & 7) >> 1) ^ fsw) * 32) + (lane & 1) * 16;
}
template <int KS>
DEV void xs_dma(rsrc_t rs, int voff, int sbase, int ldsw) {
  int tmp;
  asm volatile("s_add_u32 m0, %4, %c5\n\ts_add_u32 %0, %3, %c6\n\tbuffer_load_dwordx4 %1, %2, %0 offen sc1 lds"
               : "=&s"(tmp) : "v"(voff), "s"(rs), "s"(sbase), "s"(ldsw), "i"(LD_XT + KS * 4096), "i"(KS * XS_STEP) : "memory", "scc");
}

__global__ __launch_bounds__(256, 1) void main_kernel(
    const u16* __restrict__ x0, const u16* __restrict__ Are, const u16* __restrict__ Aim,
    const u16* __restrict__ Bre, const u16* __restrict__ Bim, const u16* __restrict__ Cre,
    const u16* __restrict__ Cim, const u16* __restrict__ logdt, const u16* __restrict__ dskip,
    const u16* __restrict__ wg, const u16* __restrict__ wu, const u16* __restrict__ wd,
    char* __restrict__ ws, u16* __restrict__ out) {
  extern __shared__ __attribute__((aligned(16))) char smem[];
  const int w_ = __builtin_amdgcn_readfirstlane(threadIdx.x >> 6);
  const int b_ = blockIdx.x;

  const rsrc_t rs = __builtin_amdgcn_make_buffer_rsrc(ws, 0, WS_END, 0x00020000);
  asm volatile("" ::: ACLOB);
  u32 cpf;
  {
    unsigned long long pc; int len;
    asm volatile("s_getpc_b64 %0\n.Lmk_code_begin:\n\ts_mov_b32 %1, .Lmk_code_end-.Lmk_code_begin" : "=s"(pc), "=s"(len));
    const int off = min((int)((min(threadIdx.x & 63, 4u) * 4 + (threadIdx.x >> 6)) * 32 + (blockIdx.x >> 3)) * 128, len - 4) & ~3;
    cpf = *(const GAS u32*)(pc + (unsigned long long)(u32)off);
    SB();
    asm volatile("" :: "s"(x0), "s"(Are), "s"(Aim), "s"(Bre), "s"(Bim), "s"(Cre), "s"(Cim), "s"(logdt), "s"(dskip), "s"(wg), "s"(wu), "s"(wd), "s"(ws));
  }
  const int rt_ = ((b_ & 7) >> 2) * 8 + ((b_ >> 3) & 7), cb_ = (b_ & 3) * 4 + (b_ >> 6);
  const int rt2_ = (b_ & 7) * 4 + ((b_ >> 3) & 3), ct_ = b_ >> 5;
  const int start_ = cb_ * 11 - (cb_ >> 2), cnt_ = (cb_ & 3) == 3 ? 10 : 11;
  const int nbase_ = (w_ < 3) ? w_ * 3 : cnt_ - 2;
  const int nt0_ = start_ + nbase_;
  const int ok2_ = __builtin_amdgcn_readfirstlane((w_ < 3) && (nbase_ + 2 < cnt_ - 2));
#define ROLES(LANE) \
  const int w = w_, b = b_, rt = rt_, rt2 = rt2_, ct = ct_, nt0 = nt0_, ok2 = ok2_; \
  const int lane = LANE, tid = w * 64 + lane, c16 = lane & 15, kq = lane >> 4, wvo = lane * 16; \
  const int x1_off = OFF_X1 + layer * ACT2, h_off = OFF_H + layer * HID2; \
  const int hb_e = h_off + (rt2 * 2 * NT1 * 16 + w * 11 * 64) * 16, hb_l = hb_e + 64 * NT1 * 256; \
  const int s10 = (w == 3) ? 9 * 1024 : 10 * 1024; \
  (void)b; (void)rt; (void)ct; (void)nt0; (void)ok2; (void)tid; (void)c16; (void)kq; (void)wvo; (void)x1_off; (void)hb_e; (void)hb_l; (void)s10;

  u32 dsk;
  const int ldsb = (int)(u32)(size_t)(LAS char*)smem, ldsw = ldsb + w_ * 1024;
  {
    constexpr int layer = 0;
    ROLES(fresh_lane())
    const int d0 = 2 * b;
    u32x4 xf[4], xt[2], wr[4];
    const bool initw = (b == 0) && (w == 3);
    const int init_off = OFF_INIT + (b & 7) * 256;
    const u32 f0 = __builtin_amdgcn_raw_buffer_load_b32(rs, 0, init_off, SC1);
    __builtin_amdgcn_raw_buffer_store_b32(0u, rs, initw ? lane * 256 : OOB, OFF_CA + 6 * 32768, SC1);
    {
      const int ln = b * 4 + lane;
      const int vo = initw ? OFF_CA + 6 * 32768 + 16384 + lane * 256 : (w == 1 && lane < 4 && ln < 832) ? (ln < 768 ? ln : ln + 128) * 256 : OOB;
      __builtin_amdgcn_raw_buffer_store_b32(0u, rs, vo, 0, SC1);
    }
    SB();
    {
      const rsrc_t rx = __builtin_amdgcn_make_buffer_rsrc((void*)x0, 0, L * D * 2, 0x00020000);
      const rsrc_t rg = __builtin_amdgcn_make_buffer_rsrc((void*)wg, 0, D * F * 2, 0x00020000);
      const rsrc_t ru = __builtin_amdgcn_make_buffer_rsrc((void*)wu, 0, D * F * 2, 0x00020000);
      const int tvo = (lane >> 1) * (D * 2) + (lane & 1) * 16, tso = (rt2 * 64 * D + ct * 64 + w * 16) * 2;
      xt[0] = __builtin_amdgcn_raw_buffer_load_b128(rx, tvo, tso, 0);
      xt[1] = __builtin_amdgcn_raw_buffer_load_b128(rx, tvo, tso + 32 * D * 2, 0);
      const int gvo = kq * (F * 2) + (c16 < cnt_ ? c16 : 0) * 16, gso = ((rt * 32 + w * 8) * F + start_ * 8) * 2;
      wr[0] = __builtin_amdgcn_raw_buffer_load_b128(rg, gvo, gso, 0);
      wr[1] = __builtin_amdgcn_raw_buffer_load_b128(ru, gvo, gso, 0);
      wr[2] = __builtin_amdgcn_raw_buffer_load_b128(rg, gvo, gso + 4 * F * 2, 0);
      wr[3] = __builtin_amdgcn_raw_buffer_load_b128(ru, gvo, gso + 4 * F * 2, 0);
      const int fvo = (c16 & 7) * (D * 2) + kq * 16, fso = (b * 8 * D + w * 128) * 2;
#pragma unroll
      for (int i = 0; i < 4; ++i) xf[i] = __builtin_amdgcn_raw_buffer_load_b128(rx, fvo + i * 64, fso, 0);
    }
    SB();
    {
      u32x4 z = splat4(0u);
      if (tid < 16) *(u32x4*)(smem + (tid >> 1) * KCOPY + (tid & 1) * 16) = z;
      if (tid >= 64 && tid < 128) *(u32x4*)(smem + LD_U + (lane >> 5) * UCH + (lane & 31) * 16) = z;
      if (tid == 128) *(u32x4*)(smem + LD_KT + 760 * 16) = z;
    }
    int ix[12];
#pragma unroll
    for (int i = 0; i < 12; i += 2) {
      const int l0 = tid * 8 - 4 + i;
      const u32 bits = pk_bf16((float)l0, (float)(l0 + 1));
      const int i0 = l0 < 256 ? (l0 < 0 ? 676 : l0) : (int)(bits & 0xffffu) - 17024;
      const int i1 = l0 < 256 ? (l0 < 0 ? 676 : l0 + 1) : (int)(bits >> 16) - 17024;
      ix[i] = ldsb + LD_KT + (i0 + (i0 >> 3)) * 16; ix[i + 1] = ldsb + LD_KT + (i1 + (i1 >> 3)) * 16;
      asm volatile("" : "+v"(ix[i]), "+v"(ix[i + 1]));
    }
    SB();
    if (initw) { asm volatile("s_waitcnt vmcnt(10)" ::: "memory"); init_bump(rs); }
    const u32 pn = b * (2 * NS) + tid;
    const u32 p_ar = Are[pn], p_ai = Aim[pn], p_br = Bre[pn], p_bi = Bim[pn];
    SB();
    const int ktw = ldsb + LD_KT + (w & 1) * 8 + (w >> 1) * 4;
    int kx[2][2][4];
#pragma unroll
    for (int c = 0; c < 2; ++c) {
      const int B = 16 * c + c16;
#pragma unroll
      for (int i = 0; i < 4; ++i) {
        const int i0 = kq < 2 ? 8 * B + 4 * kq + i : kq == 2 ? 256 + 4 * B + i : 384 + (i >> 1) * 64 + 2 * B + (i & 1);
        const int i1 = kq == 0 ? 512 + 32 * i + B : (kq == 1 && i == 0) ? 640 + B : 680;
        kx[0][c][i] = ktw + (i0 + (i0 >> 3)) * 16; kx[1][c][i] = ktw + (i1 + (i1 >> 3)) * 16;
        asm volatile("" : "+v"(kx[0][c][i]), "+v"(kx[1][c][i]));
      }
    }
    SB();
    const u32 p_cr = Cre[pn], p_ci = Cim[pn];
    const u32 p_ld = logdt[(u32)(d0 + (tid >> 7))];
    dsk = ((const u32*)dskip)[(u32)(b + (lane >> 6))];
    const int chunk = b + 256 * (w ^ 2);
    u32x4 w2r[4];
    {
      const rsrc_t rwd = __builtin_amdgcn_make_buffer_rsrc((void*)wd, 0, F * D * 2, 0x00020000);
#pragma unroll
      for (int i = 0; i < 4; ++i)
        w2r[i] = __builtin_amdgcn_raw_buffer_load_b128(rwd, (lane >> 3) * (D * 2) + (lane & 7) * 16, ((chunk >> 3) * 32 * D + (chunk & 7) * 64 + i * 8 * D) * 2, 0);
    }
    SB();
    {
      char* img = smem + LD_Y + w * YW;
      *(u32x4*)(img + wvo) = xt[0];
      *(u32x4*)(img + 1024 + wvo) = xt[1];
      const char* tp = img + (kq * 8 + (c16 >> 2)) * 32 + (c16 & 3) * 8;
      u32x4 t0 = tr_frag(tp, 0, 128), t1 = tr_frag(tp, 1024, 1024 + 128);
      const int so = OFF_XT + ((ct * 64 + w * 16) * LP + rt2 * 64) * 2;
      st16(rs, c16 * (LP * 2) + kq * 16, so, t0);
      st16(rs, c16 * (LP * 2) + kq * 16 + 64, so, t1);
    }
    constexpr int WROW = 368;
    if (c16 < 11) {
      char* wdst = smem + LD_XT + (w * 8 + kq) * WROW + c16 * 32;
#pragma unroll
      for (int i = 0; i < 2; ++i) {
        const u32x4 g = wr[2 * i], u = wr[2 * i + 1];
        *(u32x4*)(wdst + i * 4 * WROW) = (u32x4){g[0], u[0], g[1], u[1]};
        *(u32x4*)(wdst + i * 4 * WROW + 16) = (u32x4){g[2], u[2], g[3], u[3]};
      }
    }
    {
      const u32x4 ones = splat4(0x3f803f80u);
      f32x4 sa, qa;
      static_for<4>([&](auto ic) { mfma_v<IC(ic) == 0>(sa, ones, xf[IC(ic)]); mfma_v<IC(ic) == 0>(qa, xf[IC(ic)], xf[IC(ic)]); });
      asm volatile("s_nop 7" : "+v"(sa), "+v"(qa));
      if ((c16 >> 2) == kq && c16 < 8) {
        const int r = c16 & 3;
        float q = r == 0 ? qa[0] : r == 1 ? qa[1] : r == 2 ? qa[2] : qa[3];
        *(f32x2*)(smem + LD_SS + (w * 8 + c16) * 8) = (f32x2){sa[0], q};
      }
    }
    bar();
    asm volatile("" :: "v"(cpf));
    if (initw) init_bump(rs);
    const int so01 = OFF_WGU + nt0 * KS1 * 1024, so2 = ok2 ? so01 + 2 * KS1 * 1024 : w == 3 ? OFF_WGU + NT1 * KS1 * 1024 : WS_END;
    {
      const char* wsrc = smem + LD_XT + (8 * kq + (c16 >> 2)) * WROW + nbase_ * 32 + (c16 & 3) * 8;
      u32x4 f0 = tr_frag(wsrc, 0, 4 * WROW), f1 = tr_frag(wsrc, 32, 32 + 4 * WROW), f2 = tr_frag(wsrc, 64, 64 + 4 * WROW);
      if (tid < 8) {
        const f32x2* sp = (const f32x2*)(smem + LD_SS) + tid;
        f32x2 v = (sp[0] + sp[8]) + (sp[16] + sp[24]);
        __builtin_amdgcn_raw_buffer_store_b64(__builtin_bit_cast(u32x2, v), rs, tid * 8, OFF_ST0 + b * 64, SC1);
      }
      st16(rs, wvo, so01 + rt * 1024, f0);
      st16(rs, wvo, so01 + (KS1 + rt) * 1024, f1);
      st16(rs, (w == 2 && lane < 8) ? wvo : OOB, OFF_AC + b * 128, splat4(0u));
      const u32x4 fillp = splat4(~0u);
      st16(rs, tid * 16, OFF_XT + ACT2 + b * 4 * LP, fillp);
      st16(rs, tid * 16, OFF_XT + ACT2 + b * 4 * LP + LP * 2, fillp);
      if (w == 3) f2 = splat4(0x3f803f80u);
      if (w != 3 || cb_ == 15) st16(rs, wvo, so2 + rt * 1024, f2);
    }
    SB();
    PrepRegs<1> pe, pl;
    u32 pv, pv2;
    const u32 f1 = __builtin_amdgcn_raw_buffer_load_b32(rs, 0, init_off, SC1);
    SB();
    {
      constexpr int TROW = 272;
      char* tab = smem + LD_XT + 32768 - 4096 + w * (32 * TROW);
      char* tar = smem + LD_Y + w * YW;
      char* tar2 = smem + LD_W2 + w * 4096;
      const float dt = rbf(expf(lo_f(p_ld)));
      const float ar = dt * lo_f(p_ar), ai = dt * lo_f(p_ai), br = dt * lo_f(p_br), bi = dt * lo_f(p_bi);
      const float cr = lo_f(p_cr), ci = lo_f(p_ci);
      const float I2PI_HI = 0.15915494309189535f;
      const float I2PI_LO = (float)(0.15915494309189535 - (double)0.15915494309189535f);
      const float a2 = ar * 1.4426950408889634f;
      const float rh = ai * I2PI_HI;
      const float rl = __builtin_fmaf(ai, I2PI_LO, __builtin_fmaf(ai, I2PI_HI, -rh));
      auto cexp = [&](float l) {
        const float e = __builtin_amdgcn_exp2f(a2 * l);
        const float fr = __builtin_fmaf(l, rl, __builtin_amdgcn_fractf(rh * l));
        return (f32x2){__builtin_amdgcn_cosf(fr), __builtin_amdgcn_sinf(fr)} * e;
      };
      const float kr = cr * br - ci * bi, ki = cr * bi + ci * br;
      u32 mx = max(__float_as_uint(kr) & 0x7f800000u, __float_as_uint(ki) & 0x7f800000u);
      mx = max(mx, (u32)__builtin_amdgcn_update_dpp(0, (int)mx, 0xB1, 0xf, 0xf, true));
      mx = max(mx, (u32)__builtin_amdgcn_update_dpp(0, (int)mx, 0x4E, 0xf, 0xf, true));
      mx = max(mx, (u32)__builtin_amdgcn_update_dpp(0, (int)mx, 0x124, 0xf, 0xf, true));
      mx = max(mx, (u32)__builtin_amdgcn_update_dpp(0, (int)mx, 0x128, 0xf, 0xf, true));
      mx = max(max((u32)__builtin_amdgcn_readlane((int)mx, 0), (u32)__builtin_amdgcn_readlane((int)mx, 16)),
               max((u32)__builtin_amdgcn_readlane((int)mx, 32), (u32)__builtin_amdgcn_readlane((int)mx, 48)));
      mx = min(max(mx, 60u << 23), 200u << 23);
      const float up = __uint_as_float((267u << 23) - mx);
      const float kscale = __uint_as_float(mx - (27u << 23));
      const f32x2 e1 = cexp(1.f), e8 = cexp(8.f), e128 = cexp(128.f), e256 = cexp(256.f), e1024 = cexp(1024.f);
      const CM c8 = mkcm((f32x2){e8[0], -e8[1]}), c128 = mkcm((f32x2){e128[0], -e128[1]}), m1 = mkcm(e1), m256 = mkcm(e256);
      u32 hv;
      auto putr = [&](int r, u32 v) { *(u32*)((r < 16 ? tar + r * TROW : tar2 + (r - 16) * TROW) + lane * 4) = v; };
      auto putc = [&](int c, u32 v) { *(u32*)(tab + c * TROW + lane * 4) = v; };
      f32x2 p[8];
      p[0] = (f32x2){16384.f, 0.f}; putr(0, pk_f16(p[0]));
      p[1] = e1 * 16384.f; putr(1, pk_f16(p[1]));
#pragma unroll
      for (int i = 2; i < 8; ++i) { p[i] = cmul_h(p[i - 1], m1, hv); putr(i, hv); }
      f32x2 q0 = e256 * 16384.f; putr(8, pk_f16(q0));
      (void)cmul_h(p[2], m256, hv); putr(9, hv);
      f32x2 q4 = cmul_h(p[4], m256, hv); putr(10, hv);
      (void)cmul_h(p[6], m256, hv); putr(11, hv);
      q0 = cmul_h(q0, m256, hv); putr(12, hv);
      q4 = cmul_h(q4, m256, hv); putr(13, hv);
      (void)cmul_h(q0, m256, hv); putr(14, hv);
      (void)cmul_h(q4, m256, hv); putr(15, hv);
      q0 = e1024 * 16384.f; putr(16, pk_f16(q0));
#pragma unroll
      for (int i = 17; i < 21; ++i) { q0 = cmul_h(q0, m256, hv); putr(i, hv); }
      SB();
      if (initw) init_bump(rs);
      init_wait(rs, init_off, f0, f1);
      drain_bar<0, false>();
      signal_all(rs, 6, b, w);
      SB();
      u32x4 fa[4][2];
      {
        const char* ra0 = tar + c16 * TROW + kq * 16;
        const char* ra1 = tar2 + (c16 < 5 ? c16 : 0) * TROW + kq * 16;
#pragma unroll
        for (int s = 0; s < 4; ++s) { fa[s][0] = *(const u32x4*)(ra0 + s * 64); fa[s][1] = *(const u32x4*)(ra1 + s * 64); }
      }
      SB();
      f32x2 v0 = (f32x2){kr, -ki} * up; putc(0, pk_f16(v0));
      f32x2 v1 = cmul_h(v0, c128, hv); putc(16, hv);
#pragma unroll
      for (int i = 1; i < 16; ++i) {
        v0 = cmul_h(v0, c8, hv); putc(i, hv);
        v1 = cmul_h(v1, c8, hv); putc(16 + i, hv);
      }
      f32x4 ka[2][2];
      const char* rb0 = tab + c16 * TROW + kq * 16;
      u32x4 fb[4][2];
#pragma unroll
      for (int s = 0; s < 4; ++s) { fb[s][0] = *(const u32x4*)(rb0 + s * 64); fb[s][1] = *(const u32x4*)(rb0 + 16 * TROW + s * 64); }
      SB();
      static_for<4>([&](auto sc) {
        constexpr int s = IC(sc);
        mfma_h<s == 0>(ka[0][0], fa[s][0], fb[s][0]); mfma_h<s == 0>(ka[0][1], fa[s][0], fb[s][1]);
        mfma_h<s == 0>(ka[1][0], fa[s][1], fb[s][0]); mfma_h<s == 0>(ka[1][1], fa[s][1], fb[s][1]);
      });
      pv = poll_ld(rs, all_off(6, b), true, w);
      mfma_done4(ka[0][0], ka[0][1], ka[1][0], ka[1][1]);
#pragma unroll
      for (int r = 0; r < 2; ++r)
#pragma unroll
        for (int c = 0; c < 2; ++c) {
          const f32x4 v = ka[r][c] * kscale;
#pragma unroll
          for (int i = 0; i < 4; ++i) *(LAS float*)(size_t)(u32)kx[r][c][i] = v[i];
        }
    }
    bar();
    {
      f32x4 tv[12];
#pragma unroll
      for (int i = 0; i < 12; ++i) tv[i] = *(const LAS f32x4*)(size_t)(u32)ix[i];
      SB();
      u32 e[2][6];
#pragma unroll
      for (int i = 0; i < 6; ++i) {
        const f32x2 a = tv[2 * i].xy + tv[2 * i].zw, c = tv[2 * i + 1].xy + tv[2 * i + 1].zw;
        e[0][i] = pk_bf16(a[0], c[0]);
        e[1][i] = pk_bf16(a[1], c[1]);
      }
#pragma unroll
      for (int c = 0; c < 2; ++c) {
        char* kb = smem + c * KCH + tid * 16 + 32;
        u32 o[5];
#pragma unroll
        for (int i = 0; i < 5; ++i) o[i] = __builtin_amdgcn_alignbit(e[c][i + 1], e[c][i], 16);
        *(u32x4*)(kb) = (u32x4){e[c][2], e[c][3], e[c][4], e[c][5]};
        *(u32x4*)(kb + KCOPY) = (u32x4){o[1], o[2], o[3], o[4]};
        *(u32x4*)(kb + 2 * KCOPY) = (u32x4){e[c][1], e[c][2], e[c][3], e[c][4]};
        *(u32x4*)(kb + 3 * KCOPY) = (u32x4){o[0], o[1], o[2], o[3]};
      }
    }
    SB();
    if (chunk < KS2P * 8) {
      char* img = smem + LD_W2 + 16384 + w * 4096;
#pragma unroll
      for (int i = 0; i < 4; ++i)
        *(u32x4*)(img + i * 1024 + (lane >> 3) * 128 + ((((lane & 7) >> 1) ^ (((lane >> 4) & 1) | ((i & 1) << 1))) * 32) + (lane & 1) * 16) = w2r[i];
      const int fr = ((c16 >> 3) & 1) | ((kq & 1) << 1);
#pragma unroll
      for (int i = 0; i < 4; ++i)
        st16(rs, wvo, OFF_WD + (((chunk & 7) * 4 + i) * KS2P + (chunk >> 3)) * 1024,
             tr_frag(img + (8 * kq + (c16 >> 2)) * 128 + ((i ^ fr) * 32) + (c16 & 3) * 8, 0, 512));
    }
    SB();
    if (initw) {
#pragma unroll 1
      for (int spins = 0; spins < SPIN_CAP && !__all(pv >= 64u); ++spins) { init_bump(rs); pv = poll_ld(rs, all_off(6, b), true, w); }
    }
    poll_wait(rs, all_off(6, b), true, pv, w);
    prep_load<1>(rs, tid, OFF_XT + d0 * LP * 2, OFF_ST0, pe);
    prep_load<1>(rs, tid, OFF_XT + d0 * LP * 2 + 2048, OFF_ST0 + 8192, pl);
    f32x4 ze[2], zl[2];
    prep_ln(smem, 0, tid, dsk, pe, ze);
    asm volatile("" : "+v"(pl.pa[0]), "+v"(pl.pb[0]), "+v"(pl.xv[0]), "+v"(pl.xv[1]));
    SB();
    static_for<4>([&](auto ic) { ld_w1<IC(ic)>(rs, wvo, so01, so2); });
    bar();
    f32x4 acc[4];
    conv_mfma<0, 0, 3, true>(smem, w, lane, acc, [&](auto nc) {
      constexpr int n = IC(nc);
      if constexpr (n % 3 == 0 && n < 36) ld_w1<4 + n / 3>(rs, wvo, so01, so2);
    });
    conv_out(rs, smem, OFF_X1 + d0 * LP * 2, w, lane, acc, ze);
    static_for<4>([&](auto ic) { ld_w1<16 + IC(ic)>(rs, wvo, so01, so2); });
    prep_ln(smem, 1, tid, dsk, pl, zl);
    SB();
    static_for<4>([&](auto ic) { ld_w1<20 + IC(ic)>(rs, wvo, so01, so2); });
    drain_bar<8, true>();
    signal_all(rs, 0, b, w);
    conv_mfma<4, 0, 7, true>(smem, w, lane, acc, [&](auto nc) {
      constexpr int n = IC(nc);
      if constexpr (n % 4 == 0 && n < 96) ld_w1<24 + n / 4>(rs, wvo, so01, so2);
    });
    pv = poll_ld(rs, all_off(0, b), true, w);
    conv_out(rs, smem, OFF_X1 + d0 * LP * 2 + 2048, w, lane, acc, zl);
    pv2 = poll_ld(rs, all_off(0, b), true, w);
    SB();
    asm volatile("s_waitcnt vmcnt(3)" ::: "memory");
    poll_wait2(rs, all_off(0, b), true, pv, pv2, w);
    {
      const int gx = xs_voff(w, lane), gs = OFF_X1 + rt * 128;
      const u32x4 nones = splat4(0xbf80bf80u);
      f32x4 c[3];
      static_for<48>([&](auto ic) {
        mfma_aA<4 * IC(ic), (IC(ic) < 3)>(c[IC(ic) % 3], nones); SB();
        if constexpr (IC(ic) < 16 && !(IC(ic) & 1)) { xs_dma<IC(ic) / 2>(rs, gx, gs, ldsw); SB(); }
      });
      asm volatile("s_nop 7" : "+v"(c[0]), "+v"(c[1]), "+v"(c[2]));
#pragma unroll
      for (int i = 0; i < 3; ++i) *(f32x4*)(smem + LD_CS + w * 192 + i * 64 + kq * 16) = c[i];
    }
    drain_bar<5, true>();
    signal_all(rs, 1, b, w);
  }
  const int lane_g = fresh_lane();
  static_for<2>([&](auto lc) {
    constexpr int layer = IC(lc);
    AccRegs pr;
    bool rdy = false;
    u32x4 W2[5][4];
    u32x4 hf[11][2];
    {
      ROLES(lane_g)
      int sw2[4];
#pragma unroll
      for (int i = 0; i < 4; ++i) sw2[i] = OFF_WD + ((ct * 4 + ((w + i) & 3)) * KS2P + w * 11) * 1024;
      const int fr = ((c16 >> 3) & 1) | ((kq & 1) << 1);
      const char* xr[4];
#pragma unroll
      for (int j = 0; j < 4; ++j) xr[j] = smem + LD_XT + (8 * kq + (c16 >> 2)) * 128 + ((((w + j) & 3) ^ fr) * 32) + (c16 & 3) * 8;
      const int gx = xs_voff(w, lane), gs = x1_off + rt * 128;
      u32x4 xs[8];
      char* ssq = smem + ((c16 >> 2) == kq ? LD_SS + w * 128 : LD_Y) + c16 * 8 + 4;
      char* sss = smem + (w == 3 ? LD_SS : LD_Y) + c16 * 8;
      u32x2 xb[2][4][2];
#pragma unroll
      for (int j = 0; j < 4; ++j) { xb[0][j][0] = tr_half(xr[j]); xb[0][j][1] = tr_half(xr[j] + 512); }
      SB();
      static_for<2>([&](auto hc) {
        constexpr int h = IC(hc);
        f32x4 ga[3][4], qa;
        const int nxt_poll = h == 0 ? all_off(layer * 4 + 1, b) : grp_off(layer * 32 + (rt2 >> 1));
        u32 pv, pv2;
        {
          auto fill = [&](auto gc) {
            constexpr int g = IC(gc);
            if constexpr (g < 8) xs_dma<8 + g>(rs, gx, gs + h * 2048, ldsw);
            if constexpr (g == (h == 0 ? 7 : SIG_HE)) {
              drain_bar<8, false>();
              if constexpr (h == 1) signal_grp(rs, layer * 32 + rt, w); else if constexpr (layer == 1) signal_all(rs, layer * 4 + 1, b, w);
            }
            if constexpr (g == 27) drain_bar<(h == 0 && layer == 0) ? 16 : (h == 1 ? 7 : 0), true>();
            if constexpr (h == 0 && layer == 0) {
              if constexpr (g >= 10 && g < 26) ld_agpr_imm<192 + 16 * ((g - 10) & 3) + 4 * ((g - 10) >> 2), ((g - 10) & 3) * 1024>(rs, wvo, sw2[(g - 10) >> 2]);
              if constexpr (g >= 28 && g < 36) xs[g - 28] = ld16(rs, wvo + ((g - 28) >> 2) * 1024, sw2[(g - 28) & 3] + 4096);
              if constexpr (g >= 43 && g < 51) *(u32x4*)(smem + LD_W2 + w * 8192 + (g - 43) * 1024 + wvo) = xs[g - 43];
            }
            if constexpr (h == 1) {
              if constexpr (g >= 14 && g < 47 && (g > 27 || !(g & 1)) && (g < 34 || !(g & 1))) {
                constexpr int n = g < 28 ? (g - 14) / 2 : g < 34 ? g - 21 : 13 + (g - 34) / 2, i = n / 5, s = n % 5;
                W2[s][i] = ld16(rs, wvo + (s & 3) * 1024, sw2[i] + (6 + (s >> 2) * 4) * 1024);
              }
            }
            if constexpr (g == POLL_G - 12) pv = poll_ld(rs, nxt_poll, h == 0, w);
            if constexpr (g == POLL_G - 5) pv2 = poll_ld(rs, nxt_poll, h == 0, w);
            if constexpr (g == POLL_G) poll_wait2(rs, nxt_poll, h == 0, pv, pv2, w);
            if constexpr (g >= POLL_G && g < POLL_G + 8) {
              if constexpr (h == 0) xs_dma<g - POLL_G>(rs, gx, gs + 2048, ldsw);
              else hf[g - POLL_G][0] = ld16(rs, wvo + ((g - POLL_G) & 3) * 1024, hb_e + ((g - POLL_G) >> 2) * 4096);
            }
          };
          static_for<60>([&](auto gc) {
            constexpr int g = IC(gc), ks = g >> 2, j = g & 3;
            const u32x4 x = {xb[ks & 1][j][0][0], xb[ks & 1][j][0][1], xb[ks & 1][j][1][0], xb[ks & 1][j][1][1]};
            mfma_aA<12 * ks, ks == 0>(ga[0][j], x);
            xb[(ks + 1) & 1][j][0] = tr_half(xr[j] + (ks + 1) * 4096);
            SB();
            mfma_aA<12 * ks + 4, ks == 0>(ga[1][j], x);
            xb[(ks + 1) & 1][j][1] = tr_half(xr[j] + (ks + 1) * 4096 + 512);
            SB();
            mfma_aA<12 * ks + 8, ks == 0>(ga[2][j], x);
            SB();
            fill(gc);
            SB();
            if constexpr (j == 0) { mfma_v<ks == 0>(qa, x, x); SB(); }
          });
          u32x4 x[4];
#pragma unroll
          for (int j = 0; j < 4; ++j) x[j] = (u32x4){xb[1][j][0][0], xb[1][j][0][1], xb[1][j][1][0], xb[1][j][1][1]};
          mfma_vv(qa, x[0], x[0]); SB();
#pragma unroll
          for (int j = 0; j < 4; ++j) { mfma_aA<188, false>(ga[2][j], x[j]); SB(); }
#pragma unroll
          for (int j = 0; j < 4; ++j) { mfma_aA<180, false>(ga[0][j], x[j]); SB(); }
          asm volatile("" : "+v"(qa));
          {
            const int r = c16 & 3;
            const float q01 = (r & 1) ? qa[1] : qa[0], q23 = (r & 1) ? qa[3] : qa[2];
            SB();
            mfma_aA<184, false>(ga[1][0], x[0]); SB();
            const float q = (r & 2) ? q23 : q01;
            *(float*)ssq = q;
            SB();
            mfma_aA<184, false>(ga[1][1], x[1]); SB();
            tie4(ga[2][0], ga[2][1], ga[2][2], ga[2][3]);
            *(float*)(sss + 384) = ga[2][0][0];
            *(float*)(sss) = ga[2][1][0];
            SB();
            mfma_aA<184, false>(ga[1][2], x[2]); SB();
            *(float*)(sss + 128) = ga[2][2][0];
            *(float*)(sss + 256) = ga[2][3][0];
            SB();
            mfma_aA<184, false>(ga[1][3], x[3]); SB();
          }
        }
        f32x4 cs[3];
#pragma unroll
        for (int i = 0; i < 3; ++i) cs[i] = *(const f32x4*)(smem + LD_CS + w * 192 + i * 64 + kq * 16);
        bar();
        f32x2 st[4];
#pragma unroll
        for (int j = 0; j < 4; ++j) st[j] = *(const f32x2*)(smem + LD_SS + (((w + j) & 3) * 16 + c16) * 8);
        tie4(ga[0][0], ga[0][1], ga[0][2], ga[0][3]);
        tie4(ga[1][0], ga[1][1], ga[1][2], ga[1][3]);
        tie4(ga[2][0], ga[2][1], ga[2][2], ga[2][3]);
        SB();
        u32 hd[3][4];
#pragma unroll
        for (int j = 0; j < 4; ++j) {
          const f32x2 mv = ln_mv(st[j]);
          const float nl = __builtin_amdgcn_rsqf(mv[1]) * -1.4426950408889634f;
#pragma unroll
          for (int i = 0; i < 3; ++i) {
            const f32x4 v = __builtin_elementwise_fma(cs[i], (f32x4){mv[0], mv[0], mv[0], mv[0]}, ga[i][j]);
            const f32x2 g = {v[0], v[1]}, u = {v[2], v[3]};
            const f32x2 a = g * nl;
            const f32x2 e = {__builtin_amdgcn_exp2f(a[0]), __builtin_amdgcn_exp2f(a[1])};
            const f32x2 d = __builtin_elementwise_fma(e, (f32x2){mv[1], mv[1]}, (f32x2){mv[1], mv[1]});
            const f32x2 r = {__builtin_amdgcn_rcpf(d[0]), __builtin_amdgcn_rcpf(d[1])};
            hd[i][j] = pk_bf16((g * u) * r);
            if constexpr (h == 1) {
              const int t = j * 3 + i;
              if (t < 3) hf[8 + t][0] = ld16(rs, wvo, hb_e + (t == 2 ? s10 : (8 + t) * 1024));
              else hf[t - 3][1] = ld16(rs, wvo + ((t - 3) & 3) * 1024, hb_e + NT1 * 256 + ((t - 3) >> 2) * 4096);
            }
          }
          if constexpr (h == 1) SB();
        }
        const int hb = (((w + kq) & 3) * NT1 * 16 + c16) * 16, hs = h_off + ((h * 64 + rt * 4) * NT1 + nt0) * 256;
#pragma unroll
        for (int i = 0; i < 3; ++i) {
          auto a02 = __builtin_amdgcn_permlane32_swap(hd[i][0], hd[i][2], false, false);
          auto a13 = __builtin_amdgcn_permlane32_swap(hd[i][1], hd[i][3], false, false);
          auto o01 = __builtin_amdgcn_permlane16_swap(a02[0], a13[0], false, false);
          auto o23 = __builtin_amdgcn_permlane16_swap(a02[1], a13[1], false, false);
          u32x4 v = {o01[0], o01[1], o23[0], o23[1]};
          st16(rs, (i < 2 || ok2) ? hb + i * 256 : OOB, hs, v);
        }
        SB();
        if constexpr (h == 0) {
          drain_bar<3, false>();
#pragma unroll
          for (int j = 0; j < 4; ++j) { xb[0][j][0] = tr_half(xr[j]); xb[0][j][1] = tr_half(xr[j] + 512); }
        }
        SB();
      });
    }
    {
      ROLES(fresh_lane())
      char* rw[3];
#pragma unroll
      for (int i = 0; i < 3; ++i) rw[i] = smem + LD_XT + (((w + 1 + i) & 3) * 3 + i) * 2048 + wvo;
      const char* rb = smem + LD_XT + w * 6144 + wvo;
      const int rvo = c16 * (LP * 2) + kq * 8;
      const int ovo = c16 * (LP * 2) + (kq & 1) * 32 + (kq >> 1) * 16;
      char* ow = smem + LD_OT + (w * 16 + c16) * 64 + (kq & 1) * 32 + (kq >> 1) * 16;
      const char* ob = smem + LD_OT + (8 * kq + (c16 >> 2)) * 64 + (w & 1) * 32 + (c16 & 3) * 8;
      const char* wlp = smem + LD_W2 + w * 8192 + wvo;
      static_for<2>([&](auto hc) {
        constexpr int h = IC(hc);
        const int tok0 = h * 1024 + rt2 * 32;
        const int eso = ((ct * 64 + w * 16) * LP + tok0) * 2;
        const int hb = h ? hb_l : hb_e;
        f32x4 da[4][2];
        u32x4 wl[2][4];
        u32x2 res[2];
        u32 pv, pv2;
        static_for<88>([&](auto nc) {
          constexpr int n = IC(nc), j = n / 44, s = (n % 44) >> 2, i = (n + 1) & 3;
          if constexpr (s < 4) mfma_aB<192 + 16 * s + 4 * i, s == 0>(da[i][j], hf[s][j]);
          else if constexpr (s < 6) mfma_vv(da[i][j], hf[s][j], wl[s - 4][i]);
          else mfma_vv(da[i][j], hf[s][j], W2[s - 6][i]);
          SB();
          if constexpr (h == 0) {
            if constexpr (n == 3 || n == 7) hf[9 + (n >> 2)][1] = ld16(rs, wvo, hb + NT1 * 256 + (n == 7 ? s10 : 9 * 1024));
            if constexpr (n == 10) res[0] = ld8(rs, rvo, x1_off + eso);
            if constexpr (n == 13) res[1] = ld8(rs, rvo + 32, x1_off + eso);
            if constexpr (n == SIG_HL) { drain_bar<4, false>(); signal_grp(rs, layer * 32 + 16 + rt, w); }
            if constexpr (n == 86) pv = poll_ld(rs, grp_off(layer * 32 + 16 + (rt2 >> 1)), false, w);
          } else {
            if constexpr (n < 33 && n % 3 == 2) hf[n / 3][1] = ld16(rs, wvo + (n / 3 < 8 ? ((n / 3) & 3) * 1024 : 0), hb + NT1 * 256 + (n / 3 < 8 ? (n / 12) * 4096 : n / 3 == 10 ? s10 : (n / 3) * 1024));
            if constexpr (layer == 0 && n == SIG_XE) { drain_bar<(SIG_XE + 1) / 3, false>(); signal_all(rs, 2, b, w); }
            if constexpr (n == 35) res[0] = ld8(rs, rvo, x1_off + eso);
            if constexpr (n == 38) res[1] = ld8(rs, rvo + 32, x1_off + eso);
            if constexpr (layer == 0 && n >= ACC_E && n < ACC_E + 12 && !((n - ACC_E) & 1)) acc_ld1(rs, (n - ACC_E) >> 1, tid, OFF_XT + ACT2 + b * 2 * LP * 2, 0, pr);
          }
          if constexpr (n % 44 >= 6 && n % 44 < 14) wl[(n % 44 - 6) >> 2][(n % 44 - 5) & 3] = *(const u32x4*)(wlp + (((n % 44 - 6) & 4) + ((n % 44 - 5) & 3)) * 1024);
          if constexpr (n == 52 || n == 56 || n == 60) { asm volatile("" : "+v"(da[(n - 48) >> 2][0])); *(f32x4*)(rw[(n - 52) >> 2]) = da[(n - 48) >> 2][0]; }
          SB();
        });
        mfma_done4(da[0][1], da[1][1], da[2][1], da[3][1]);
        SB();
#pragma unroll
        for (int i = 0; i < 3; ++i) *(f32x4*)(rw[i] + 1024) = da[i + 1][1];
        if constexpr (h == 0) pv2 = poll_ld(rs, grp_off(layer * 32 + 16 + (rt2 >> 1)), false, w);
        bar();
        u32x2 xo[2];
#pragma unroll
        for (int j = 0; j < 2; ++j) {
          const f32x4 rf = {lo_f(res[j][0]), hi_f(res[j][0]), lo_f(res[j][1]), hi_f(res[j][1])};
          const f32x4 v = ((da[0][j] + rf) + *(const f32x4*)(rb + j * 1024)) + (*(const f32x4*)(rb + 2048 + j * 1024) + *(const f32x4*)(rb + 4096 + j * 1024));
          xo[j] = (u32x2){pk_bf16(v[0], v[1]), pk_bf16(v[2], v[3])};
        }
        auto s0 = __builtin_amdgcn_permlane16_swap(xo[0][0], xo[1][0], false, false);
        auto s1 = __builtin_amdgcn_permlane16_swap(xo[0][1], xo[1][1], false, false);
        const u32x4 xv = {s0[0], s1[0], s0[1], s1[1]};
        *(u32x4*)ow = xv;
        if constexpr (layer == 0 && h == 1) { SB(); rdy = acc_ready(pr); SB(); }
        if constexpr (layer == 0) st16(rs, ovo, OFF_XT + ACT2 + eso, xv);
        SB();
        if constexpr (h == 0) {
          poll_wait2(rs, grp_off(layer * 32 + 16 + (rt2 >> 1)), false, pv, pv2, w);
#pragma unroll
          for (int s = 0; s < 6; ++s) hf[s][0] = ld16(rs, wvo + (s & 3) * 1024, hb_l + (s >> 2) * 4096);
        }
        SB();
        bar();
        if constexpr (h == 0) {
#pragma unroll
          for (int s = 6; s < 11; ++s) hf[s][0] = ld16(rs, wvo + (s < 8 ? (s & 3) * 1024 : 0), hb_l + (s < 8 ? 4096 : s == 10 ? s10 : s * 1024));
          SB();
        }
        if constexpr (layer == 0) {
          if (w < 2) {
            u32x4 f0 = tr_frag(ob, 0, 256), f1 = tr_frag(ob, 2048, 2048 + 256);
            const u32x4 ones = splat4(0x3f803f80u);
            f32x4 sa, qa;
            mfma_v0(sa, ones, f0); mfma_v0(qa, f0, f0); mfma_vv(sa, ones, f1); mfma_vv(qa, f1, f1);
            asm volatile("s_nop 7" : "+v"(sa), "+v"(qa));
            const int r = c16 & 3;
            float q = r == 0 ? qa[0] : r == 1 ? qa[1] : r == 2 ? qa[2] : qa[3];
            u32x2 v = {__float_as_uint(sa[0]), __float_as_uint(q)};
            const bool diag = (c16 >> 2) == kq;
            const bool fits = q < 0x1p29f;
            const u32x2 fs = to_fix(fits ? sa[0] * 0x1p30f : 0.f), fq = to_fix(fits ? q * 0x1p26f : 0x1p59f);
            const int avo = diag ? (c16 & 3) * 4096 + kq * 16 : OOB, aso = OFF_AC + h * 16384 + (rt2 * 8 + w * 4) * 16;
            asm volatile("s_nop 4\n\tbuffer_atomic_add_x2 %0, %2, %3, %4 offen sc1\n\tbuffer_atomic_add_x2 %1, %2, %3, %4 offen offset:8 sc1"
                         :: "v"(fs), "v"(fq), "v"(avo), "s"(rs), "s"(aso) : "memory");
            __builtin_amdgcn_raw_buffer_store_b64(v, rs, diag ? c16 * 8 : OOB, OFF_ST1 + (ct * L + tok0 + w * 16) * 8, SC1);
          }
        } else {
          u32x4 f = tr_frag(ob, (w >> 1) * 2048, (w >> 1) * 2048 + 256);
          const rsrc_t ro = __builtin_amdgcn_make_buffer_rsrc(out, 0, L * D * 2, 0x00020000);
          __builtin_amdgcn_raw_buffer_store_b128(f, ro, (c16 * D + kq * 8) * 2, ((tok0 + (w & 1) * 16) * D + ct * 64 + (w >> 1) * 32) * 2, SC1);
        }
        SB();
      });
    }
    if constexpr (layer == 0) {
      ROLES(fresh_lane())
      const int xo = b * 2 * LP * 2;
      f32x4 z[2], acc[4];
      u32 pv, pv2;
      acc_wait(rs, tid, OFF_XT + ACT2 + xo, 0, all_off(2, b), pr, rdy);
      acc_ln(rs, smem, 0, tid, dsk, pr, z);
      bar();
      conv_mfma<0, 0, 3, true>(smem, w, lane, acc, [&](auto nc) { if constexpr (IC(nc) == 30) { drain_bar<0, false>(); signal_all(rs, 3, b, w); } });
      conv_out(rs, smem, OFF_X1 + ACT2 + xo, w, lane, acc, z);
      conv_mfma<4, 0, 3, true>(smem, w, lane, acc, [&](auto nc) {
        constexpr int n = IC(nc);
        if constexpr (n >= ACC_L && n < ACC_L + 12 && !((n - ACC_L) & 1)) acc_ld1(rs, (n - ACC_L) >> 1, tid, OFF_XT + ACT2 + xo + 2048, 1, pr);
        if constexpr (n == 36) { drain_bar<6, false>(); signal_all(rs, 4, b, w); }
      });
      acc_wait(rs, tid, OFF_XT + ACT2 + xo + 2048, 1, all_off(3, b), pr, acc_ready(pr));
      acc_ln(rs, smem, 1, tid, dsk, pr, z);
      bar();
      const int gx = xs_voff(w, lane), gs = OFF_X1 + ACT2 + rt * 128;
      conv_mfma<4, 4, 7, false>(smem, w, lane, acc, [&](auto nc) {
        constexpr int n = IC(nc);
        if constexpr (n == CE_P) pv = poll_ld(rs, all_off(4, b), true, w);
        if constexpr (n == CE_P + 14) pv2 = poll_ld(rs, all_off(4, b), true, w);
      });
      poll_wait2(rs, all_off(4, b), true, pv, pv2, w);
      static_for<8>([&](auto ic) { xs_dma<IC(ic)>(rs, gx, gs, ldsw); });
      conv_out(rs, smem, OFF_X1 + ACT2 + xo + 2048, w, lane, acc, z);
      drain_bar<6, false>();
    }
  });
  asm volatile(".Lmk_code_end:" ::: "memory");
}

extern "C" void kernel_launch(void* const* d_in, const int* in_sizes, int n_in, void* d_out, int out_size,
                              void* d_ws, size_t ws_size, hipStream_t stream) {
  char* ws = (char*)d_ws;
  (void)hipFuncSetAttribute((const void*)main_kernel, hipFuncAttributeMaxDynamicSharedMemorySize, LDS_BYTES);
  main_kernel<<<256, 256, LDS_BYTES, stream>>>(
      (const u16*)d_in[0], (const u16*)d_in[1], (const u16*)d_in[2], (const u16*)d_in[3], (const u16*)d_in[4],
      (const u16*)d_in[5], (const u16*)d_in[6], (const u16*)d_in[7], (const u16*)d_in[8], (const u16*)d_in[10],
      (const u16*)d_in[11], (const u16*)d_in[12], ws, (u16*)d_out);
}
```

```cpp
#include <hip/hip_runtime.h>
#include <stdint.h>
#include <type_traits>

typedef unsigned short u16;
typedef unsigned int u32;
typedef u32 u32x4 __attribute__((ext_vector_type(4)));
typedef u32 u32x2 __attribute__((ext_vector_type(2)));
typedef float f32x4 __attribute__((ext_vector_type(4)));
typedef float f32x2 __attribute__((ext_vector_type(2)));
typedef short s16x4 __attribute__((ext_vector_type(4)));
typedef __bf16 bf16x2 __attribute__((ext_vector_type(2)));
typedef _Float16 f16x2 __attribute__((ext_vector_type(2)));
#define LAS __attribute__((address_space(3)))
#define GAS __attribute__((address_space(1)))
#define DEV __device__ __forceinline__
#define SB() __builtin_amdgcn_sched_barrier(0)
#define IC(x) decltype(x)::value
typedef __amdgpu_buffer_rsrc_t rsrc_t;

constexpr int L = 2048, D = 512, NS = 128, F = 1376;
constexpr int NT1 = 172;
constexpr int KS1 = 16;
constexpr int KS2P = 44;
constexpr float EPS = 1e-5f;
constexpr int SC1 = 16;
constexpr int NSH = 16;
constexpr int SIG_HE = 12, SIG_HL = 44, SIG_XE = 30, POLL_G = 52, CE_P = 14, ACC_E = 74, ACC_L = 20;

constexpr int XPAD = 64;
constexpr int LP = L + XPAD;
constexpr int ACT = D * LP * 2;
constexpr int HID = F * L * 2;
constexpr int N_ALL = 7;
constexpr int OFF_CA  = 0;
constexpr int OFF_CH  = OFF_CA + N_ALL * 128 * 256;
constexpr int OFF_INIT = OFF_CH + 64 * 256;
constexpr int CTRL_BYTES = OFF_INIT + 8 * 256;
constexpr int OFF_XT  = CTRL_BYTES;
constexpr int ACT2 = ACT, HID2 = HID;
constexpr int OFF_X1  = OFF_XT + ACT + ACT2;
constexpr int OFF_H   = OFF_X1 + ACT + ACT2;
constexpr int OFF_WGU = OFF_H + HID + HID2;
constexpr int OFF_WD  = OFF_WGU + (NT1 + 1) * KS1 * 1024;
constexpr int OFF_ST0 = OFF_WD + 32 * KS2P * 1024;
constexpr int OFF_ST1 = OFF_ST0 + L * 8;
constexpr int OFF_AC  = OFF_ST1 + 8 * L * 8;
constexpr int WS_END  = OFF_AC + L * 16;

DEV float lo_f(u32 v) { return __uint_as_float(v << 16); }
DEV float hi_f(u32 v) { return __uint_as_float(v & 0xffff0000u); }
DEV u32 pk_bf16(float a, float b) {
  f32x2 v = {a, b};
  return __builtin_bit_cast(u32, __builtin_convertvector(v, bf16x2));
}
DEV u32 pk_bf16(f32x2 v) { return __builtin_bit_cast(u32, __builtin_convertvector(v, bf16x2)); }
DEV u32 pk_f16(f32x2 v) { return __builtin_bit_cast(u32, __builtin_convertvector(v, f16x2)); }
DEV f32x2 unpk(u32 v) { return (f32x2){lo_f(v), hi_f(v)}; }
DEV float rbf(float a) { return lo_f(pk_bf16(a, a)); }
DEV f32x2 ln_mv(f32x2 sq) {
  f32x2 t = __builtin_elementwise_fma(sq, (f32x2){1.0f / D, 1.0f / D}, (f32x2){0.f, EPS});
  t[1] = fmaxf(__builtin_fmaf(-t[0], t[0], t[1]), EPS);
  return t;
}
struct CM { f32x2 re, im; };
DEV CM mkcm(f32x2 c) { return CM{(f32x2){c[0], c[0]}, (f32x2){-c[1], c[1]}}; }
DEV f32x2 cmul_h(f32x2 x, const CM& c, u32& h) {
  const f32x2 r = __builtin_elementwise_fma(x.yx, c.im, x * c.re);
  h = pk_f16(r);
  return r;
}

constexpr int KCOPY = 4160, KCH = 4 * KCOPY;
constexpr int LD_U = 2 * KCH;
constexpr int UCH = (256 + L) * 2;
constexpr int LD_XT = LD_U + 2 * UCH;
constexpr int LD_Y = LD_XT + 65536;
constexpr int YW = 5120;
constexpr int LD_SS = LD_Y + 4 * YW;
constexpr int LD_OT = LD_Y;
constexpr int LD_CS = LD_SS + 512;
constexpr int LD_W2 = LD_CS + 768;
constexpr int LDS_BYTES = LD_W2 + 32768;
static_assert(LDS_BYTES <= 163840, "LDS");
constexpr int LD_KT = LD_XT + 16384;

#define ACLOB "a0","a1","a2","a3","a4","a5","a6","a7","a8","a9","a10","a11","a12","a13","a14","a15","a16","a17","a18","a19","a20","a21","a22","a23","a24","a25","a26","a27","a28","a29","a30","a31","a32","a33","a34","a35","a36","a37","a38","a39","a40","a41","a42","a43","a44","a45","a46","a47","a48","a49","a50","a51","a52","a53","a54","a55","a56","a57","a58","a59","a60","a61","a62","a63","a64","a65","a66","a67","a68","a69","a70","a71","a72","a73","a74","a75","a76","a77","a78","a79","a80","a81","a82","a83","a84","a85","a86","a87","a88","a89","a90","a91","a92","a93","a94","a95","a96","a97","a98","a99","a100","a101","a102","a103","a104","a105","a106","a107","a108","a109","a110","a111","a112","a113","a114","a115","a116","a117","a118","a119","a120","a121","a122","a123","a124","a125","a126","a127","a128","a129","a130","a131","a132","a133","a134","a135","a136","a137","a138","a139","a140","a141","a142","a143","a144","a145","a146","a147","a148","a149","a150","a151","a152","a153","a154","a155","a156","a157","a158","a159","a160","a161","a162","a163","a164","a165","a166","a167","a168","a169","a170","a171","a172","a173","a174","a175","a176","a177","a178","a179","a180","a181","a182","a183","a184","a185","a186","a187","a188","a189","a190","a191","a192","a193","a194","a195","a196","a197","a198","a199","a200","a201","a202","a203","a204","a205","a206","a207","a208","a209","a210","a211","a212","a213","a214","a215","a216","a217","a218","a219","a220","a221","a222","a223","a224","a225","a226","a227","a228","a229","a230","a231","a232","a233","a234","a235","a236","a237","a238","a239","a240","a241","a242","a243","a244","a245","a246","a247","a248","a249","a250","a251","a252","a253","a254","a255"
#define MF "v_mfma_f32_16x16x32_bf16 "
DEV void mfma_vv(f32x4& c, u32x4 a, u32x4 b) { asm(MF "%0, %1, %2, %0" : "+v"(c) : "v"(a), "v"(b)); }
DEV void mfma_v0(f32x4& c, u32x4 a, u32x4 b) { asm(MF "%0, %1, %2, 0" : "=&v"(c) : "v"(a), "v"(b)); }
template <bool FIRST> DEV void mfma_v(f32x4& c, u32x4 a, u32x4 b) { if constexpr (FIRST) mfma_v0(c, a, b); else mfma_vv(c, a, b); }
template <int R, bool FIRST> DEV void mfma_aA(f32x4& c, u32x4 b) {
  if constexpr (FIRST) asm(MF "%0, a[%c2:%c3], %1, 0" : "=&v"(c) : "v"(b), "i"(R), "i"(R + 3));
  else asm(MF "%0, a[%c2:%c3], %1, %0" : "+v"(c) : "v"(b), "i"(R), "i"(R + 3));
}
template <int R, bool FIRST> DEV void mfma_aB(f32x4& c, u32x4 a) {
  if constexpr (FIRST) asm(MF "%0, %1, a[%c2:%c3], 0" : "=&v"(c) : "v"(a), "i"(R), "i"(R + 3));
  else asm(MF "%0, %1, a[%c2:%c3], %0" : "+v"(c) : "v"(a), "i"(R), "i"(R + 3));
}
template <bool FIRST> DEV void mfma_h(f32x4& c, u32x4 a, u32x4 b) {
  if constexpr (FIRST) asm("v_mfma_f32_16x16x32_f16 %0, %1, %2, 0" : "=&v"(c) : "v"(a), "v"(b));
  else asm("v_mfma_f32_16x16x32_f16 %0, %1, %2, %0" : "+v"(c) : "v"(a), "v"(b));
}
DEV void mfma_done4(f32x4& a, f32x4& b, f32x4& c, f32x4& d) { asm volatile("s_nop 7" : "+v"(a), "+v"(b), "+v"(c), "+v"(d)); }
DEV void tie4(f32x4& a, f32x4& b, f32x4& c, f32x4& d) { asm volatile("" : "+v"(a), "+v"(b), "+v"(c), "+v"(d)); }
DEV void bar() { asm volatile("s_waitcnt lgkmcnt(0)\n\ts_barrier" ::: "memory", ACLOB); }

DEV u32x4 ld16(rsrc_t rs, int voff, int soff) { return __builtin_amdgcn_raw_buffer_load_b128(rs, voff, soff, SC1); }
DEV u32x2 ld8(rsrc_t rs, int voff, int soff) { return __builtin_amdgcn_raw_buffer_load_b64(rs, voff, soff, SC1); }
DEV void st16(rsrc_t rs, int voff, int soff, u32x4 v) { __builtin_amdgcn_raw_buffer_store_b128(v, rs, voff, soff, SC1); }
DEV int fresh_lane() {
  int l;
  asm volatile("v_mbcnt_lo_u32_b32 %0, -1, 0\n\tv_mbcnt_hi_u32_b32 %0, -1, %0" : "=v"(l));
  return l;
}
DEV u32x4 splat4(u32 c) {
  u32x4 r;
  asm volatile("v_mov_b32 %0, %4\n\tv_mov_b32 %1, %4\n\tv_mov_b32 %2, %4\n\tv_mov_b32 %3, %4"
               : "=v"(r[0]), "=v"(r[1]), "=v"(r[2]), "=v"(r[3]) : "i"(c));
  asm volatile("s_nop 1" : "+v"(r));
  return r;
}
DEV u32x2 tr_half(const char* p) { return __builtin_bit_cast(u32x2, __builtin_amdgcn_ds_read_tr16_b64_v4i16((LAS s16x4*)p)); }
DEV u32x4 tr_frag(const char* p, int o0, int o1) {
  u32x2 a = tr_half(p + o0), c = tr_half(p + o1);
  return (u32x4){a[0], a[1], c[0], c[1]};
}
template <int N, typename FN, int I = 0>
DEV void static_for(FN&& fn) {
  if constexpr (I < N) { fn(std::integral_constant<int, I>{}); static_for<N, FN, I + 1>(static_cast<FN&&>(fn)); }
}

constexpr int OOB = 0x7ffffff0;
constexpr int SPIN_CAP = 400000;
constexpr int ACC_SLEEP = 2;
DEV int all_off(int edge, int b) { return OFF_CA + (edge * 8 + (b & 7)) * 4096; }
DEV int grp_off(int idx) { return OFF_CH + idx * 256; }
DEV u32 poll_ld(rsrc_t rs, int soff, bool all, int w) {
  const int voff = (all ? (fresh_lane() & (NSH - 1)) * 256 : 0) | (w == 3 ? 0 : 0x7ff00000);
  return __builtin_amdgcn_raw_buffer_load_b32(rs, voff, soff, SC1);
}
DEV void poll_spin(rsrc_t rs, int soff, bool all, u32 f) {
  if (__builtin_expect(!__all(f >= 64u), 0)) {
    const int voff = all ? (fresh_lane() & (NSH - 1)) * 256 : 0;
    int spins = SPIN_CAP;
    u32 t;
    asm volatile("s_nop 4\n"
                 "1:\n\t"
                 "buffer_load_dword %0, %2, %3, %4 offen sc1\n\t"
                 "s_sub_u32 %1, %1, 1\n\t"
                 "s_cbranch_scc1 2f\n\t"
                 "s_waitcnt vmcnt(0)\n\t"
                 "v_cmp_gt_u32 vcc, 64, %0\n\t"
                 "s_nop 4\n\t"
                 "s_cbranch_vccnz 1b\n"
                 "2:\n\t"
                 "s_waitcnt vmcnt(0)"
                 : "=&v"(t), "+s"(spins) : "v"(voff), "s"(rs), "s"(soff) : "memory", "vcc", "scc");
  }
  asm volatile("" ::: "memory");
}
DEV void poll_wait(rsrc_t rs, int soff, bool all, u32 f, int w) {
  if (w == 3) poll_spin(rs, soff, all, f);
  asm volatile("s_barrier" ::: "memory", ACLOB);
}
DEV void poll_wait2(rsrc_t rs, int soff, bool all, u32 f0, u32 f1, int w) {
  if (w == 3) { if (!__all(f0 >= 64u)) poll_spin(rs, soff, all, f1); }
  asm volatile("s_barrier" ::: "memory", ACLOB);
}
template <int YOUNGER, bool LDSW>
DEV void drain_bar() {
  if constexpr (LDSW) asm volatile("s_waitcnt vmcnt(%c0) lgkmcnt(0)\n\ts_barrier" :: "i"(YOUNGER) : "memory", ACLOB);
  else asm volatile("s_waitcnt vmcnt(%c0)\n\ts_barrier" :: "i"(YOUNGER) : "memory", ACLOB);
}
DEV void signal_all(rsrc_t rs, int edge, int b, int w) {
  if (w == 0) {
    const int lane = fresh_lane();
    __builtin_amdgcn_raw_ptr_buffer_atomic_add_i32(NSH / 4, rs, lane < 8 ? lane * 4096 : OOB, OFF_CA + edge * 32768 + (b & (NSH - 1)) * 256, SC1);
  }
}
DEV void signal_grp(rsrc_t rs, int idx, int w) {
  if (w == 0) {
    const int lane = fresh_lane();
    __builtin_amdgcn_raw_ptr_buffer_atomic_add_i32(4, rs, lane == 0 ? 0 : OOB, OFF_CH + idx * 256, SC1);
  }
}

DEV void init_bump(rsrc_t rs) {
  const int lane = fresh_lane();
  __builtin_amdgcn_raw_ptr_buffer_atomic_add_i32(1, rs, lane < 8 ? lane * 256 : OOB, OFF_INIT, SC1);
}
DEV void init_wait(rsrc_t rs, int soff, u32 f0, u32 f) {
  if (__builtin_expect(__any(f == f0), 0)) {
    int spins = SPIN_CAP;
    u32 t;
    asm volatile("s_nop 4\n"
                 "1:\n\t"
                 "buffer_load_dword %0, off, %3, %4 sc1\n\t"
                 "s_sub_u32 %1, %1, 1\n\t"
                 "s_cbranch_scc1 2f\n\t"
                 "s_waitcnt vmcnt(0)\n\t"
                 "v_cmp_eq_u32 vcc, %2, %0\n\t"
                 "s_nop 4\n\t"
                 "s_cbranch_vccnz 1b\n"
                 "2:\n\t"
                 "s_waitcnt vmcnt(0)"
                 : "=&v"(t), "+s"(spins) : "v"(f0), "s"(rs), "s"(soff) : "memory", "vcc", "scc");
  }
  asm volatile("" ::: "memory");
}

template <int P> struct PrepRegs { u32x4 pa[P], pb[P]; u32x2 xv[2]; };
template <int P>
DEV void prep_ld1(rsrc_t rs, int i, int tid, int xso, int sso, PrepRegs<P>& pr) {
  if (i < 2 * P) {
    if (i & 1) pr.pb[i >> 1] = ld16(rs, tid * 32 + 16, sso + (i >> 1) * (L * 8));
    else pr.pa[i >> 1] = ld16(rs, tid * 32, sso + (i >> 1) * (L * 8));
  } else pr.xv[i - 2 * P] = ld8(rs, tid * 8, xso + (i - 2 * P) * (LP * 2));
}
template <int P>
DEV void prep_load(rsrc_t rs, int tid, int xso, int sso, PrepRegs<P>& pr) {
#pragma unroll
  for (int i = 0; i < 2 * P + 2; ++i) prep_ld1<P>(rs, i, tid, xso, sso, pr);
  SB();
}
DEV void ln_apply(char* smem, int h, int tid, u32 dsk, f32x4 s0, f32x4 s1, const u32x2 (&xv)[2], f32x4 (&z)[2]) {
  const f32x2 t0 = ln_mv(s0.xy), t1 = ln_mv(s0.zw), t2 = ln_mv(s1.xy), t3 = ln_mv(s1.zw);
  const f32x2 rr[2] = {{__builtin_amdgcn_rsqf(t0[1]), __builtin_amdgcn_rsqf(t1[1])}, {__builtin_amdgcn_rsqf(t2[1]), __builtin_amdgcn_rsqf(t3[1])}};
  const f32x2 mr[2] = {{-t0[0] * rr[0][0], -t1[0] * rr[0][1]}, {-t2[0] * rr[1][0], -t3[0] * rr[1][1]}};
#pragma unroll
  for (int c = 0; c < 2; ++c) {
    const float dk = c ? hi_f(dsk) : lo_f(dsk);
    u32x2 u;
    f32x2 zz[2];
#pragma unroll
    for (int i = 0; i < 2; ++i) {
      const f32x2 x = unpk(xv[c][i]);
      const f32x2 uf = __builtin_elementwise_fma(x, rr[i], mr[i]);
      u[i] = pk_bf16(uf);
      zz[i] = __builtin_elementwise_fma(uf, (f32x2){dk, dk}, x);
    }
    z[c] = (f32x4){zz[0][0], zz[0][1], zz[1][0], zz[1][1]};
    *(u32x2*)(smem + LD_U + c * UCH + (256 + h * 1024) * 2 + tid * 8) = u;
  }
}
DEV void prep_ln(char* smem, int h, int tid, u32 dsk, const PrepRegs<1>& pr, f32x4 (&z)[2]) {
  ln_apply(smem, h, tid, dsk, __builtin_bit_cast(f32x4, pr.pa[0]), __builtin_bit_cast(f32x4, pr.pb[0]), pr.xv, z);
}
struct AccRegs { u32x4 a[4]; u32x2 xv[2]; };
DEV void acc_ld1(rsrc_t rs, int i, int tid, int xso, int h, AccRegs& r) {
  if (i < 4) r.a[i] = ld16(rs, tid * 16, OFF_AC + h * 16384 + i * 4096);
  else r.xv[i - 4] = ld8(rs, tid * 8, xso + (i - 4) * (LP * 2));
}
DEV u32x2 to_fix(float t) {
  const float hi = __builtin_floorf(t * 0x1p-32f);
  const u32 lo = (u32)fminf(__builtin_fmaf(hi, -0x1p32f, t), 4294967040.f);
  return (u32x2){(lo & ~15u) | 1u, (u32)(int)hi};
}
DEV bool acc_ready(const AccRegs& r) {
  u32 o = 0u, n = ~0u;
#pragma unroll
  for (int i = 0; i < 4; ++i) { o |= r.a[i][0] | r.a[i][2]; n &= r.a[i][0] & r.a[i][2]; }
  const u32 mx = max(max(r.xv[0][0], r.xv[0][1]), max(r.xv[1][0], r.xv[1][1]));
  return __all(((o & 7u) | (~n & 8u)) == 0u && mx != ~0u);
}
DEV void acc_wait(rsrc_t rs, int tid, int xso, int h, int poll_off, AccRegs& r, bool ready) {
  if (__builtin_expect(!ready, 0)) {
#pragma unroll 1
    for (int spins = 0; spins < SPIN_CAP; ++spins) {
      bool done = false;
      if ((spins & 7) == 7) done = __all(__builtin_amdgcn_raw_buffer_load_b32(rs, (tid & (NSH - 1)) * 256, poll_off, SC1) >= 64u);
      asm volatile("" ::: "memory");
#pragma unroll
      for (int i = 0; i < 6; ++i) acc_ld1(rs, i, tid, xso, h, r);
      if (done || acc_ready(r)) break;
      __builtin_amdgcn_s_sleep(ACC_SLEEP);
    }
    __builtin_amdgcn_s_waitcnt(0x0F70);
  }
}
DEV void acc_ln(rsrc_t rs, char* smem, int h, int tid, u32 dsk, const AccRegs& r, f32x4 (&z)[2]) {
  f32x2 st[4];
#pragma unroll
  for (int i = 0; i < 4; ++i) {
    const f32x2 hi = {(float)(int)r.a[i][1], (float)(int)r.a[i][3]}, lo = {(float)r.a[i][0], (float)r.a[i][2]};
    st[i] = __builtin_elementwise_fma(hi, (f32x2){0x1p2f, 0x1p6f}, lo * (f32x2){0x1p-30f, 0x1p-26f});
  }
  f32x4 s0 = {st[0][0], st[0][1], st[1][0], st[1][1]}, s1 = {st[2][0], st[2][1], st[3][0], st[3][1]};
  const u32 mk = (r.a[0][3] | r.a[1][3]) | (r.a[2][3] | r.a[3][3]);
  if (__builtin_expect(__any(mk >= (1u << 27)), 0)) {
    poll_spin(rs, all_off(2 + h, blockIdx.x), true, 0u);
    s0 = s1 = (f32x4){0.f, 0.f, 0.f, 0.f};
#pragma unroll 1
    for (int p = 0; p < 8; ++p) {
      s0 += __builtin_bit_cast(f32x4, ld16(rs, tid * 32, OFF_ST1 + h * 8192 + p * (L * 8)));
      s1 += __builtin_bit_cast(f32x4, ld16(rs, tid * 32 + 16, OFF_ST1 + h * 8192 + p * (L * 8)));
    }
  }
  ln_apply(smem, h, tid, dsk, s0, s1, r.xv, z);
}
template <int M0, int I0, int I1> struct ConvPlan {
  static constexpr int Q0 = (M0 - I1) > 0 ? (M0 - I1) : 0, Q1 = M0 + 3 - I0;
  static constexpr int NQ = Q1 - Q0 + 1, NU = I1 - I0 + 1, NF = NQ + NU;
  static constexpr int count() { int n = 0; for (int i = I0; i <= I1; ++i) for (int m = 0; m < 4; ++m) n += (M0 + m - i >= 0); return n; }
  static constexpr int NM = count();
  static constexpr int im(int t) { int n = 0; for (int i = I0; i <= I1; ++i) for (int m = 0; m < 4; ++m) if (M0 + m - i >= 0) { if (n == t) return i * 4 + m; ++n; } return 0; }
  static constexpr int frag(int f) {
    if (f == 0) return 0;
    if (f <= 4) return -(Q1 - 4 + f + 1);
    int g = f - 5, nlow = NQ - 4;
    if (g < 2 * nlow) return (g & 1) ? -(Q1 - 4 - (g >> 1) + 1) : 1 + (g >> 1);
    return 1 + nlow + (g - 2 * nlow);
  }
};
template <int M0, int I0, int I1, bool FIRST, typename FN>
DEV void conv_mfma(char* smem, int w, int lane, f32x4 (&acc)[4], FN&& fill) {
  using P = ConvPlan<M0, I0, I1>;
  const int c16 = lane & 15, kq = lane >> 4;
  const int cch = w >> 1, wr = w & 1;
  const char* tb = smem + cch * KCH + (c16 >> 2) * KCOPY + (16 + 16 * (1 + 8 * wr) - 8 * kq + 4 * (c16 & 3) - 4) * 2;
  const char* ub = smem + LD_U + cch * UCH + (256 - 16 * (1 + 8 * wr + 6) + 16 * c16 + 8 * kq) * 2;
  u32x4 T[2][P::NQ], U[2][P::NU];
  auto ldf = [&](auto rc, auto fc) {
    constexpr int rr = IC(rc), f = P::frag(IC(fc));
    if constexpr (f >= 0) U[rr & 1][f] = *(const u32x4*)(ub + (3 - rr) * 64 + (I0 + f) * 512);
    else T[rr & 1][-f - 1 - P::Q0] = tr_frag(tb, rr * 64 + (-f - 1) * 512 + 8, rr * 64 + (-f - 1) * 512);
  };
  static_for<P::NF>([&](auto fc) { ldf(std::integral_constant<int, 0>{}, fc); });
  SB();
  static_for<4 * P::NM>([&](auto nc) {
    constexpr int n = IC(nc), rr = n / P::NM, t = n % P::NM, i = P::im(t) >> 2, m = P::im(t) & 3, q = M0 + m - i;
    mfma_v<FIRST && rr == 0 && i == I0>(acc[m], T[rr & 1][q - P::Q0], U[rr & 1][i - I0]);
    if constexpr (rr < 3 && t < P::NF) ldf(std::integral_constant<int, rr + 1>{}, std::integral_constant<int, t>{});
    SB();
    fill(nc);
    SB();
  });
}
DEV void conv_out(rsrc_t rs, char* smem, int xso, int w, int lane, f32x4 (&acc)[4], const f32x4 (&z)[2]) {
  const int tid = w * 64 + lane;
  char* yw = smem + LD_Y + w * YW + (lane & 15) * 80 + (lane >> 4) * 16;
  const char* yb = smem + LD_Y + tid * 16 + (tid >> 2) * 16;
  const int vo = ((tid & 1) * LP + (tid >> 1) * 8) * 2;
  mfma_done4(acc[0], acc[1], acc[2], acc[3]);
#pragma unroll
  for (int m = 0; m < 4; ++m) *(f32x4*)(yw + m * 1280) = acc[m];
  bar();
  u32x2 o[2];
#pragma unroll
  for (int c = 0; c < 2; ++c) {
    const f32x4 y = (*(const f32x4*)(yb + (2 * c) * YW) + z[c]) + *(const f32x4*)(yb + (2 * c + 1) * YW);
    o[c] = (u32x2){pk_bf16(y[0], y[1]), pk_bf16(y[2], y[3])};
  }
  const bool odd = tid & 1;
  u32 s0 = odd ? o[0][0] : o[1][0], s1 = odd ? o[0][1] : o[1][1];
  u32 g0 = __builtin_amdgcn_update_dpp(0u, s0, 0xB1, 0xf, 0xf, true);
  u32 g1 = __builtin_amdgcn_update_dpp(0u, s1, 0xB1, 0xf, 0xf, true);
  u32x4 v = odd ? (u32x4){g0, g1, o[1][0], o[1][1]} : (u32x4){o[0][0], o[0][1], g0, g1};
  st16(rs, vo, xso, v);
  SB();
}

template <int R, int IMM>
DEV void ld_agpr(rsrc_t rs, int voff, int sbase) {
  int tmp;
  asm volatile("s_add_u32 %0, %3, %c6\n\tbuffer_load_dwordx4 a[%c4:%c5], %1, %2, %0 offen sc1"
               : "=&s"(tmp) : "v"(voff), "s"(rs), "s"(sbase), "i"(R), "i"(R + 3), "i"(IMM) : "memory", "scc");
}
template <int I> DEV void ld_w1(rsrc_t rs, int voff, int so01, int so2) {
  constexpr int ks = I / 3, i = I % 3;
  if constexpr (i < 2) ld_agpr<4 * I, (i * KS1 + ks) * 1024>(rs, voff, so01);
  else ld_agpr<4 * I, ks * 1024>(rs, voff, so2);
}
template <int R, int IMM>
DEV void ld_agpr_imm(rsrc_t rs, int voff, int soff) {
  asm volatile("buffer_load_dwordx4 a[%c3:%c4], %0, %1, %2 offen offset:%c5 sc1"
               :: "v"(voff), "s"(rs), "s"(soff), "i"(R), "i"(R + 3), "i"(IMM) : "memory");
}

constexpr int XS_STEP = 32 * LP * 2;
DEV int xs_voff(int w, int lane) {
  const int fsw = ((lane >> 4) & 1) | ((w & 1) << 1);
  return (w * 8 + (lane >> 3)) * (LP * 2) + ((((lane & 7) >> 1) ^ fsw) * 32) + (lane & 1) * 16;
}
template <int KS>
DEV void xs_dma(rsrc_t rs, int voff, int sbase, int ldsw) {
  int tmp;
  asm volatile("s_add_u32 m0, %4, %c5\n\ts_add_u32 %0, %3, %c6\n\tbuffer_load_dwordx4 %1, %2, %0 offen sc1 lds"
               : "=&s"(tmp) : "v"(voff), "s"(rs), "s"(sbase), "s"(ldsw), "i"(LD_XT + KS * 4096), "i"(KS * XS_STEP) : "memory", "scc");
}

__global__ __launch_bounds__(256, 1) void main_kernel(
    const u16* __restrict__ x0, const u16* __restrict__ Are, const u16* __restrict__ Aim,
    const u16* __restrict__ Bre, const u16* __restrict__ Bim, const u16* __restrict__ Cre,
    const u16* __restrict__ Cim, const u16* __restrict__ logdt, const u16* __restrict__ dskip,
    const u16* __restrict__ wg, const u16* __restrict__ wu, const u16* __restrict__ wd,
    char* __restrict__ ws, u16* __restrict__ out) {
  extern __shared__ __attribute__((aligned(16))) char smem[];
  const int w_ = __builtin_amdgcn_readfirstlane(threadIdx.x >> 6);
  const int b_ = blockIdx.x;

  const rsrc_t rs = __builtin_amdgcn_make_buffer_rsrc(ws, 0, WS_END, 0x00020000);
  asm volatile("" ::: ACLOB);
  u32 cpf;
  {
    unsigned long long pc; int len;
    asm volatile("s_getpc_b64 %0\n.Lmk_code_begin:\n\ts_mov_b32 %1, .Lmk_code_end-.Lmk_code_begin" : "=s"(pc), "=s"(len));
    const int off = min((int)((min(threadIdx.x & 63, 4u) * 4 + (threadIdx.x >> 6)) * 32 + (blockIdx.x >> 3)) * 128, len - 4) & ~3;
    cpf = *(const GAS u32*)(pc + (unsigned long long)(u32)off);
    SB();
    asm volatile("" :: "s"(x0), "s"(Are), "s"(Aim), "s"(Bre), "s"(Bim), "s"(Cre), "s"(Cim), "s"(logdt), "s"(dskip), "s"(wg), "s"(wu), "s"(wd), "s"(ws));
  }
  const int rt_ = ((b_ & 7) >> 2) * 8 + ((b_ >> 3) & 7), cb_ = (b_ & 3) * 4 + (b_ >> 6);
  const int rt2_ = (b_ & 7) * 4 + ((b_ >> 3) & 3), ct_ = b_ >> 5;
  const int start_ = cb_ * 11 - (cb_ >> 2), cnt_ = (cb_ & 3) == 3 ? 10 : 11;
  const int nbase_ = (w_ < 3) ? w_ * 3 : cnt_ - 2;
  const int nt0_ = start_ + nbase_;
  const int ok2_ = __builtin_amdgcn_readfirstlane((w_ < 3) && (nbase_ + 2 < cnt_ - 2));
#define ROLES(LANE) \
  const int w = w_, b = b_, rt = rt_, rt2 = rt2_, ct = ct_, nt0 = nt0_, ok2 = ok2_; \
  const int lane = LANE, tid = w * 64 + lane, c16 = lane & 15, kq = lane >> 4, wvo = lane * 16; \
  const int x1_off = OFF_X1 + layer * ACT2, h_off = OFF_H + layer * HID2; \
  const int hb_e = h_off + (rt2 * 2 * NT1 * 16 + w * 11 * 64) * 16, hb_l = hb_e + 64 * NT1 * 256; \
  const int s10 = (w == 3) ? 9 * 1024 : 10 * 1024; \
  (void)b; (void)rt; (void)ct; (void)nt0; (void)ok2; (void)tid; (void)c16; (void)kq; (void)wvo; (void)x1_off; (void)hb_e; (void)hb_l; (void)s10;

  u32 dsk;
  const int ldsb = (int)(u32)(size_t)(LAS char*)smem, ldsw = ldsb + w_ * 1024;
  {
    constexpr int layer = 0;
    ROLES(fresh_lane())
    const int d0 = 2 * b;
    u32x4 xf[4], xt[2], wr[4];
    const bool initw = (b == 0) && (w == 3);
    const int init_off = OFF_INIT + (b & 7) * 256;
    const u32 f0 = __builtin_amdgcn_raw_buffer_load_b32(rs, 0, init_off, SC1);
    if (initw) {
      __builtin_amdgcn_raw_buffer_store_b32(0u, rs, lane * 256, OFF_CA + 6 * 32768, SC1);
      __builtin_amdgcn_raw_buffer_store_b32(0u, rs, lane * 256, OFF_CA + 6 * 32768 + 16384, SC1);
    }
    SB();
    {
      const rsrc_t rx = __builtin_amdgcn_make_buffer_rsrc((void*)x0, 0, L * D * 2, 0x00020000);
      const rsrc_t rg = __builtin_amdgcn_make_buffer_rsrc((void*)wg, 0, D * F * 2, 0x00020000);
      const rsrc_t ru = __builtin_amdgcn_make_buffer_rsrc((void*)wu, 0, D * F * 2, 0x00020000);
      const int fvo = (c16 & 7) * (D * 2) + kq * 16, fso = (b * 8 * D + w * 128) * 2;
#pragma unroll
      for (int i = 0; i < 4; ++i) xf[i] = __builtin_amdgcn_raw_buffer_load_b128(rx, fvo + i * 64, fso, 0);
      SB();
      const int gvo = kq * (F * 2) + (c16 < cnt_ ? c16 : 0) * 16, gso = ((rt * 32 + w * 8) * F + start_ * 8) * 2;
      wr[0] = __builtin_amdgcn_raw_buffer_load_b128(rg, gvo, gso, 0);
      wr[1] = __builtin_amdgcn_raw_buffer_load_b128(ru, gvo, gso, 0);
      wr[2] = __builtin_amdgcn_raw_buffer_load_b128(rg, gvo, gso + 4 * F * 2, 0);
      wr[3] = __builtin_amdgcn_raw_buffer_load_b128(ru, gvo, gso + 4 * F * 2, 0);
      SB();
      const int tvo = (lane >> 1) * (D * 2) + (lane & 1) * 16, tso = (rt2 * 64 * D + ct * 64 + w * 16) * 2;
      xt[0] = __builtin_amdgcn_raw_buffer_load_b128(rx, tvo, tso, 0);
      xt[1] = __builtin_amdgcn_raw_buffer_load_b128(rx, tvo, tso + 32 * D * 2, 0);
    }
    SB();
    {
      u32x4 z = splat4(0u);
      if (tid < 16) *(u32x4*)(smem + (tid >> 1) * KCOPY + (tid & 1) * 16) = z;
      if (tid >= 64 && tid < 128) *(u32x4*)(smem + LD_U + (lane >> 5) * UCH + (lane & 31) * 16) = z;
      if (tid == 128) *(u32x4*)(smem + LD_KT + 760 * 16) = z;
    }
    int ix[12];
#pragma unroll
    for (int i = 0; i < 12; i += 2) {
      const int l0 = tid * 8 - 4 + i;
      const u32 bits = pk_bf16((float)l0, (float)(l0 + 1));
      const int i0 = l0 < 256 ? (l0 < 0 ? 676 : l0) : (int)(bits & 0xffffu) - 17024;
      const int i1 = l0 < 256 ? (l0 < 0 ? 676 : l0 + 1) : (int)(bits >> 16) - 17024;
      ix[i] = ldsb + LD_KT + (i0 + (i0 >> 3)) * 16; ix[i + 1] = ldsb + LD_KT + (i1 + (i1 >> 3)) * 16;
      asm volatile("" : "+v"(ix[i]), "+v"(ix[i + 1]));
    }
    SB();
    if (initw) { asm volatile("s_waitcnt vmcnt(10)" ::: "memory"); init_bump(rs); }
    const u32 pn = b * (2 * NS) + tid;
    const u32 p_ar = Are[pn], p_ai = Aim[pn], p_br = Bre[pn], p_bi = Bim[pn];
    SB();
    const int ktw = ldsb + LD_KT + (w & 1) * 8 + (w >> 1) * 4;
    int kx[2][2][4];
#pragma unroll
    for (int c = 0; c < 2; ++c) {
      const int B = 16 * c + c16;
#pragma unroll
      for (int i = 0; i < 4; ++i) {
        const int i0 = kq < 2 ? 8 * B + 4 * kq + i : kq == 2 ? 256 + 4 * B + i : 384 + (i >> 1) * 64 + 2 * B + (i & 1);
        const int i1 = kq == 0 ? 512 + 32 * i + B : (kq == 1 && i == 0) ? 640 + B : 680;
        kx[0][c][i] = ktw + (i0 + (i0 >> 3)) * 16; kx[1][c][i] = ktw + (i1 + (i1 >> 3)) * 16;
        asm volatile("" : "+v"(kx[0][c][i]), "+v"(kx[1][c][i]));
      }
    }
    SB();
    const u32 p_cr = Cre[pn], p_ci = Cim[pn];
    const u32 p_ld = logdt[(u32)(d0 + (tid >> 7))];
    dsk = ((const u32*)dskip)[(u32)(b + (lane >> 6))];
    const int chunk = b + 256 * (w ^ 2);
    u32x4 w2r[4];
    SB();
    {
      const u32x4 ones = splat4(0x3f803f80u);
      f32x4 sa, qa;
      static_for<4>([&](auto ic) { mfma_v<IC(ic) == 0>(sa, ones, xf[IC(ic)]); mfma_v<IC(ic) == 0>(qa, xf[IC(ic)], xf[IC(ic)]); });
      asm volatile("s_nop 7" : "+v"(sa), "+v"(qa));
      if ((c16 >> 2) == kq && c16 < 8) {
        const int r = c16 & 3;
        float q = r == 0 ? qa[0] : r == 1 ? qa[1] : r == 2 ? qa[2] : qa[3];
        *(f32x2*)(smem + LD_SS + (w * 8 + c16) * 8) = (f32x2){sa[0], q};
      }
    }
    SB();
    constexpr int WROW = 368;
    if (c16 < 11) {
      char* wdst = smem + LD_XT + (w * 8 + kq) * WROW + c16 * 32;
#pragma unroll
      for (int i = 0; i < 2; ++i) {
        const u32x4 g = wr[2 * i], u = wr[2 * i + 1];
        *(u32x4*)(wdst + i * 4 * WROW) = (u32x4){g[0], u[0], g[1], u[1]};
        *(u32x4*)(wdst + i * 4 * WROW + 16) = (u32x4){g[2], u[2], g[3], u[3]};
      }
    }
    bar();
    asm volatile("" :: "v"(cpf));
    if (initw) init_bump(rs);
    const int so01 = OFF_WGU + nt0 * KS1 * 1024, so2 = ok2 ? so01 + 2 * KS1 * 1024 : w == 3 ? OFF_WGU + NT1 * KS1 * 1024 : WS_END;
    {
      const char* wsrc = smem + LD_XT + (8 * kq + (c16 >> 2)) * WROW + nbase_ * 32 + (c16 & 3) * 8;
      u32x4 f0 = tr_frag(wsrc, 0, 4 * WROW), f1 = tr_frag(wsrc, 32, 32 + 4 * WROW), f2 = tr_frag(wsrc, 64, 64 + 4 * WROW);
      if (tid < 8) {
        const f32x2* sp = (const f32x2*)(smem + LD_SS) + tid;
        f32x2 v = (sp[0] + sp[8]) + (sp[16] + sp[24]);
        __builtin_amdgcn_raw_buffer_store_b64(__builtin_bit_cast(u32x2, v), rs, tid * 8, OFF_ST0 + b * 64, SC1);
      }
      st16(rs, wvo, so01 + rt * 1024, f0);
      st16(rs, wvo, so01 + (KS1 + rt) * 1024, f1);
      if (w == 2) {
        const int ln = b * 4 + lane - 8;
        st16(rs, lane < 8 ? OFF_AC + b * 128 + wvo : (lane < 12 && ln < 832) ? (ln < 768 ? ln : ln + 128) * 256 : OOB, 0, splat4(0u));
      }
      const u32x4 fillp = splat4(~0u);
      st16(rs, tid * 16, OFF_XT + ACT2 + b * 4 * LP, fillp);
      st16(rs, tid * 16, OFF_XT + ACT2 + b * 4 * LP + LP * 2, fillp);
      if (w == 3) f2 = splat4(0x3f803f80u);
      if (w != 3 || cb_ == 15) st16(rs, wvo, so2 + rt * 1024, f2);
    }
    SB();
    {
      char* img = smem + LD_Y + w * YW;
      *(u32x4*)(img + wvo) = xt[0];
      *(u32x4*)(img + 1024 + wvo) = xt[1];
      const char* tp = img + (kq * 8 + (c16 >> 2)) * 32 + (c16 & 3) * 8;
      u32x4 t0 = tr_frag(tp, 0, 128), t1 = tr_frag(tp, 1024, 1024 + 128);
      const int so = OFF_XT + ((ct * 64 + w * 16) * LP + rt2 * 64) * 2;
      st16(rs, c16 * (LP * 2) + kq * 16, so, t0);
      st16(rs, c16 * (LP * 2) + kq * 16 + 64, so, t1);
    }
    SB();
    PrepRegs<1> pe, pl;
    u32 pv, pv2;
    const u32 f1 = __builtin_amdgcn_raw_buffer_load_b32(rs, 0, init_off, SC1);
    SB();
    {
      constexpr int TROW = 272;
      char* tab = smem + LD_XT + 32768 - 4096 + w * (32 * TROW);
      char* tar = smem + LD_Y + w * YW;
      char* tar2 = smem + LD_W2 + w * 4096;
      const float dt = rbf(expf(lo_f(p_ld)));
      const float ar = dt * lo_f(p_ar), ai = dt * lo_f(p_ai), br = dt * lo_f(p_br), bi = dt * lo_f(p_bi);
      const float cr = lo_f(p_cr), ci = lo_f(p_ci);
      const float I2PI_HI = 0.15915494309189535f;
      const float I2PI_LO = (float)(0.15915494309189535 - (double)0.15915494309189535f);
      const float a2 = ar * 1.4426950408889634f;
      const float rh = ai * I2PI_HI;
      const float rl = __builtin_fmaf(ai, I2PI_LO, __builtin_fmaf(ai, I2PI_HI, -rh));
      auto cexp = [&](float l) {
        const float e = __builtin_amdgcn_exp2f(a2 * l);
        const float fr = __builtin_fmaf(l, rl, __builtin_amdgcn_fractf(rh * l));
        return (f32x2){__builtin_amdgcn_cosf(fr), __builtin_amdgcn_sinf(fr)} * e;
      };
      const float kr = cr * br - ci * bi, ki = cr * bi + ci * br;
      u32 mx = max(__float_as_uint(kr) & 0x7f800000u, __float_as_uint(ki) & 0x7f800000u);
      mx = max(mx, (u32)__builtin_amdgcn_update_dpp(0, (int)mx, 0xB1, 0xf, 0xf, true));
      mx = max(mx, (u32)__builtin_amdgcn_update_dpp(0, (int)mx, 0x4E, 0xf, 0xf, true));
      mx = max(mx, (u32)__builtin_amdgcn_update_dpp(0, (int)mx, 0x124, 0xf, 0xf, true));
      mx = max(mx, (u32)__builtin_amdgcn_update_dpp(0, (int)mx, 0x128, 0xf, 0xf, true));
      mx = max(max((u32)__builtin_amdgcn_readlane((int)mx, 0), (u32)__builtin_amdgcn_readlane((int)mx, 16)),
               max((u32)__builtin_amdgcn_readlane((int)mx, 32), (u32)__builtin_amdgcn_readlane((int)mx, 48)));
      mx = min(max(mx, 60u << 23), 200u << 23);
      const float up = __uint_as_float((267u << 23) - mx);
      const float kscale = __uint_as_float(mx - (27u << 23));
      const f32x2 e1 = cexp(1.f), e8 = cexp(8.f), e128 = cexp(128.f), e256 = cexp(256.f), e1024 = cexp(1024.f);
      const CM c8 = mkcm((f32x2){e8[0], -e8[1]}), c128 = mkcm((f32x2){e128[0], -e128[1]}), m1 = mkcm(e1), m256 = mkcm(e256);
      u32 hv;
      auto putr = [&](int r, u32 v) { *(u32*)((r < 16 ? tar + r * TROW : tar2 + (r - 16) * TROW) + lane * 4) = v; };
      auto putc = [&](int c, u32 v) { *(u32*)(tab + c * TROW + lane * 4) = v; };
      f32x2 p[8];
      p[0] = (f32x2){16384.f, 0.f}; putr(0, pk_f16(p[0]));
      p[1] = e1 * 16384.f; putr(1, pk_f16(p[1]));
#pragma unroll
      for (int i = 2; i < 8; ++i) { p[i] = cmul_h(p[i - 1], m1, hv); putr(i, hv); }
      f32x2 q0 = e256 * 16384.f; putr(8, pk_f16(q0));
      (void)cmul_h(p[2], m256, hv); putr(9, hv);
      f32x2 q4 = cmul_h(p[4], m256, hv); putr(10, hv);
      (void)cmul_h(p[6], m256, hv); putr(11, hv);
      q0 = cmul_h(q0, m256, hv); putr(12, hv);
      q4 = cmul_h(q4, m256, hv); putr(13, hv);
      (void)cmul_h(q0, m256, hv); putr(14, hv);
      (void)cmul_h(q4, m256, hv); putr(15, hv);
      q0 = e1024 * 16384.f; putr(16, pk_f16(q0));
#pragma unroll
      for (int i = 17; i < 21; ++i) { q0 = cmul_h(q0, m256, hv); putr(i, hv); }
      SB();
      if (initw) init_bump(rs);
      init_wait(rs, init_off, f0, f1);
      drain_bar<0, false>();
      signal_all(rs, 6, b, w);
    {
      const rsrc_t rwd = __builtin_amdgcn_make_buffer_rsrc((void*)wd, 0, F * D * 2, 0x00020000);
#pragma unroll
      for (int i = 0; i < 4; ++i)
        w2r[i] = __builtin_amdgcn_raw_buffer_load_b128(rwd, (lane >> 3) * (D * 2) + (lane & 7) * 16, ((chunk >> 3) * 32 * D + (chunk & 7) * 64 + i * 8 * D) * 2, 0);
    }
      SB();
      u32x4 fa[4][2];
      {
        const char* ra0 = tar + c16 * TROW + kq * 16;
        const char* ra1 = tar2 + (c16 < 5 ? c16 : 0) * TROW + kq * 16;
#pragma unroll
        for (int s = 0; s < 4; ++s) { fa[s][0] = *(const u32x4*)(ra0 + s * 64); fa[s][1] = *(const u32x4*)(ra1 + s * 64); }
      }
      SB();
      f32x2 v0 = (f32x2){kr, -ki} * up; putc(0, pk_f16(v0));
      f32x2 v1 = cmul_h(v0, c128, hv); putc(16, hv);
#pragma unroll
      for (int i = 1; i < 16; ++i) {
        v0 = cmul_h(v0, c8, hv); putc(i, hv);
        v1 = cmul_h(v1, c8, hv); putc(16 + i, hv);
      }
      f32x4 ka[2][2];
      const char* rb0 = tab + c16 * TROW + kq * 16;
      u32x4 fb[4][2];
#pragma unroll
      for (int s = 0; s < 4; ++s) { fb[s][0] = *(const u32x4*)(rb0 + s * 64); fb[s][1] = *(const u32x4*)(rb0 + 16 * TROW + s * 64); }
      SB();
      static_for<4>([&](auto sc) {
        constexpr int s = IC(sc);
        mfma_h<s == 0>(ka[0][0], fa[s][0], fb[s][0]); mfma_h<s == 0>(ka[0][1], fa[s][0], fb[s][1]);
        mfma_h<s == 0>(ka[1][0], fa[s][1], fb[s][0]); mfma_h<s == 0>(ka[1][1], fa[s][1], fb[s][1]);
      });
      pv = poll_ld(rs, all_off(6, b), true, w);
      mfma_done4(ka[0][0], ka[0][1], ka[1][0], ka[1][1]);
#pragma unroll
      for (int r = 0; r < 2; ++r)
#pragma unroll
        for (int c = 0; c < 2; ++c) {
          const f32x4 v = ka[r][c] * kscale;
#pragma unroll
          for (int i = 0; i < 4; ++i) *(LAS float*)(size_t)(u32)kx[r][c][i] = v[i];
        }
    }
    bar();
    {
      f32x4 tv[12];
#pragma unroll
      for (int i = 0; i < 12; ++i) tv[i] = *(const LAS f32x4*)(size_t)(u32)ix[i];
      SB();
      u32 e[2][6];
#pragma unroll
      for (int i = 0; i < 6; ++i) {
        const f32x2 a = tv[2 * i].xy + tv[2 * i].zw, c = tv[2 * i + 1].xy + tv[2 * i + 1].zw;
        e[0][i] = pk_bf16(a[0], c[0]);
        e[1][i] = pk_bf16(a[1], c[1]);
      }
#pragma unroll
      for (int c = 0; c < 2; ++c) {
        char* kb = smem + c * KCH + tid * 16 + 32;
        u32 o[5];
#pragma unroll
        for (int i = 0; i < 5; ++i) o[i] = __builtin_amdgcn_alignbit(e[c][i + 1], e[c][i], 16);
        *(u32x4*)(kb) = (u32x4){e[c][2], e[c][3], e[c][4], e[c][5]};
        *(u32x4*)(kb + KCOPY) = (u32x4){o[1], o[2], o[3], o[4]};
        *(u32x4*)(kb + 2 * KCOPY) = (u32x4){e[c][1], e[c][2], e[c][3], e[c][4]};
        *(u32x4*)(kb + 3 * KCOPY) = (u32x4){o[0], o[1], o[2], o[3]};
      }
    }
    SB();
    if (chunk < KS2P * 8) {
      char* img = smem + LD_W2 + 16384 + w * 4096;
#pragma unroll
      for (int i = 0; i < 4; ++i)
        *(u32x4*)(img + i * 1024 + (lane >> 3) * 128 + ((((lane & 7) >> 1) ^ (((lane >> 4) & 1) | ((i & 1) << 1))) * 32) + (lane & 1) * 16) = w2r[i];
      const int fr = ((c16 >> 3) & 1) | ((kq & 1) << 1);
#pragma unroll
      for (int i = 0; i < 4; ++i)
        st16(rs, wvo, OFF_WD + (((chunk & 7) * 4 + i) * KS2P + (chunk >> 3)) * 1024,
             tr_frag(img + (8 * kq + (c16 >> 2)) * 128 + ((i ^ fr) * 32) + (c16 & 3) * 8, 0, 512));
    }
    SB();
    if (initw) {
#pragma unroll 1
      for (int spins = 0; spins < SPIN_CAP && !__all(pv >= 64u); ++spins) { init_bump(rs); pv = poll_ld(rs, all_off(6, b), true, w); }
    }
    poll_wait(rs, all_off(6, b), true, pv, w);
    prep_load<1>(rs, tid, OFF_XT + d0 * LP * 2, OFF_ST0, pe);
    prep_load<1>(rs, tid, OFF_XT + d0 * LP * 2 + 2048, OFF_ST0 + 8192, pl);
    f32x4 ze[2], zl[2];
    prep_ln(smem, 0, tid, dsk, pe, ze);
    asm volatile("" : "+v"(pl.pa[0]), "+v"(pl.pb[0]), "+v"(pl.xv[0]), "+v"(pl.xv[1]));
    SB();
    static_for<4>([&](auto ic) { ld_w1<IC(ic)>(rs, wvo, so01, so2); });
    bar();
    f32x4 acc[4];
    conv_mfma<0, 0, 3, true>(smem, w, lane, acc, [&](auto nc) {
      constexpr int n = IC(nc);
      if constexpr (n % 3 == 0 && n < 36) ld_w1<4 + n / 3>(rs, wvo, so01, so2);
    });
    conv_out(rs, smem, OFF_X1 + d0 * LP * 2, w, lane, acc, ze);
    static_for<4>([&](auto ic) { ld_w1<16 + IC(ic)>(rs, wvo, so01, so2); });
    prep_ln(smem, 1, tid, dsk, pl, zl);
    SB();
    static_for<4>([&](auto ic) { ld_w1<20 + IC(ic)>(rs, wvo, so01, so2); });
    drain_bar<8, true>();
    signal_all(rs, 0, b, w);
    conv_mfma<4, 0, 7, true>(smem, w, lane, acc, [&](auto nc) {
      constexpr int n = IC(nc);
      if constexpr (n % 4 == 0 && n < 96) ld_w1<24 + n / 4>(rs, wvo, so01, so2);
    });
    pv = poll_ld(rs, all_off(0, b), true, w);
    conv_out(rs, smem, OFF_X1 + d0 * LP * 2 + 2048, w, lane, acc, zl);
    pv2 = poll_ld(rs, all_off(0, b), true, w);
    SB();
    asm volatile("s_waitcnt vmcnt(3)" ::: "memory");
    poll_wait2(rs, all_off(0, b), true, pv, pv2, w);
    {
      const int gx = xs_voff(w, lane), gs = OFF_X1 + rt * 128;
      const u32x4 nones = splat4(0xbf80bf80u);
      f32x4 c[3];
      static_for<48>([&](auto ic) {
        mfma_aA<4 * IC(ic), (IC(ic) < 3)>(c[IC(ic) % 3], nones); SB();
        if constexpr (IC(ic) < 16 && !(IC(ic) & 1)) { xs_dma<IC(ic) / 2>(rs, gx, gs, ldsw); SB(); }
      });
      asm volatile("s_nop 7" : "+v"(c[0]), "+v"(c[1]), "+v"(c[2]));
#pragma unroll
      for (int i = 0; i < 3; ++i) *(f32x4*)(smem + LD_CS + w * 192 + i * 64 + kq * 16) = c[i];
    }
    drain_bar<5, true>();
    signal_all(rs, 1, b, w);
  }
  const int lane_g = fresh_lane();
  static_for<2>([&](auto lc) {
    constexpr int layer = IC(lc);
    AccRegs pr;
    bool rdy = false;
    u32x4 W2[5][4];
    u32x4 hf[11][2];
    {
      ROLES(lane_g)
      int sw2[4];
#pragma unroll
      for (int i = 0; i < 4; ++i) sw2[i] = OFF_WD + ((ct * 4 + ((w + i) & 3)) * KS2P + w * 11) * 1024;
      const int fr = ((c16 >> 3) & 1) | ((kq & 1) << 1);
      const char* xr[4];
#pragma unroll
      for (int j = 0; j < 4; ++j) xr[j] = smem + LD_XT + (8 * kq + (c16 >> 2)) * 128 + ((((w + j) & 3) ^ fr) * 32) + (c16 & 3) * 8;
      const int gx = xs_voff(w, lane), gs = x1_off + rt * 128;
      u32x4 xs[8];
      char* ssq = smem + ((c16 >> 2) == kq ? LD_SS + w * 128 : LD_Y) + c16 * 8 + 4;
      char* sss = smem + (w == 3 ? LD_SS : LD_Y) + c16 * 8;
      u32x2 xb[2][4][2];
#pragma unroll
      for (int j = 0; j < 4; ++j) { xb[0][j][0] = tr_half(xr[j]); xb[0][j][1] = tr_half(xr[j] + 512); }
      SB();
      static_for<2>([&](auto hc) {
        constexpr int h = IC(hc);
        f32x4 ga[3][4], qa;
        const int nxt_poll = h == 0 ? all_off(layer * 4 + 1, b) : grp_off(layer * 32 + (rt2 >> 1));
        u32 pv, pv2;
        {
          auto fill = [&](auto gc) {
            constexpr int g = IC(gc);
            if constexpr (g < 8) xs_dma<8 + g>(rs, gx, gs + h * 2048, ldsw);
            if constexpr (g == (h == 0 ? 7 : SIG_HE)) {
              drain_bar<8, false>();
              if constexpr (h == 1) signal_grp(rs, layer * 32 + rt, w); else if constexpr (layer == 1) signal_all(rs, layer * 4 + 1, b, w);
            }
            if constexpr (g == 27) drain_bar<(h == 0 && layer == 0) ? 16 : (h == 1 ? 7 : 0), true>();
            if constexpr (h == 0 && layer == 0) {
              if constexpr (g >= 10 && g < 26) ld_agpr_imm<192 + 16 * ((g - 10) & 3) + 4 * ((g - 10) >> 2), ((g - 10) & 3) * 1024>(rs, wvo, sw2[(g - 10) >> 2]);
              if constexpr (g >= 28 && g < 36) xs[g - 28] = ld16(rs, wvo + ((g - 28) >> 2) * 1024, sw2[(g - 28) & 3] + 4096);
              if constexpr (g >= 43 && g < 51) *(u32x4*)(smem + LD_W2 + w * 8192 + (g - 43) * 1024 + wvo) = xs[g - 43];
            }
            if constexpr (h == 1) {
              if constexpr (g >= 14 && g < 47 && (g > 27 || !(g & 1)) && (g < 34 || !(g & 1))) {
                constexpr int n = g < 28 ? (g - 14) / 2 : g < 34 ? g - 21 : 13 + (g - 34) / 2, i = n / 5, s = n % 5;
                W2[s][i] = ld16(rs, wvo + (s & 3) * 1024, sw2[i] + (6 + (s >> 2) * 4) * 1024);
              }
            }
            if constexpr (g == POLL_G - 12) pv = poll_ld(rs, nxt_poll, h == 0, w);
            if constexpr (g == POLL_G - 5) pv2 = poll_ld(rs, nxt_poll, h == 0, w);
            if constexpr (g == POLL_G) poll_wait2(rs, nxt_poll, h == 0, pv, pv2, w);
            if constexpr (g >= POLL_G && g < POLL_G + 8) {
              if constexpr (h == 0) xs_dma<g - POLL_G>(rs, gx, gs + 2048, ldsw);
              else hf[g - POLL_G][0] = ld16(rs, wvo + ((g - POLL_G) & 3) * 1024, hb_e + ((g - POLL_G) >> 2) * 4096);
            }
          };
          static_for<60>([&](auto gc) {
            constexpr int g = IC(gc), ks = g >> 2, j = g & 3;
            const u32x4 x = {xb[ks & 1][j][0][0], xb[ks & 1][j][0][1], xb[ks & 1][j][1][0], xb[ks & 1][j][1][1]};
            mfma_aA<12 * ks, ks == 0>(ga[0][j], x);
            xb[(ks + 1) & 1][j][0] = tr_half(xr[j] + (ks + 1) * 4096);
            SB();
            mfma_aA<12 * ks + 4, ks == 0>(ga[1][j], x);
            xb[(ks + 1) & 1][j][1] = tr_half(xr[j] + (ks + 1) * 4096 + 512);
            SB();
            mfma_aA<12 * ks + 8, ks == 0>(ga[2][j], x);
            SB();
            fill(gc);
            SB();
            if constexpr (j == 0) { mfma_v<ks == 0>(qa, x, x); SB(); }
          });
          u32x4 x[4];
#pragma unroll
          for (int j = 0; j < 4; ++j) x[j] = (u32x4){xb[1][j][0][0], xb[1][j][0][1], xb[1][j][1][0], xb[1][j][1][1]};
          mfma_vv(qa, x[0], x[0]); SB();
#pragma unroll
          for (int j = 0; j < 4; ++j) { mfma_aA<188, false>(ga[2][j], x[j]); SB(); }
#pragma unroll
          for (int j = 0; j < 4; ++j) { mfma_aA<180, false>(ga[0][j], x[j]); SB(); }
          asm volatile("" : "+v"(qa));
          {
            const int r = c16 & 3;
            const float q01 = (r & 1) ? qa[1] : qa[0], q23 = (r & 1) ? qa[3] : qa[2];
            SB();
            mfma_aA<184, false>(ga[1][0], x[0]); SB();
            const float q = (r & 2) ? q23 : q01;
            *(float*)ssq = q;
            SB();
            mfma_aA<184, false>(ga[1][1], x[1]); SB();
            tie4(ga[2][0], ga[2][1], ga[2][2], ga[2][3]);
            *(float*)(sss + 384) = ga[2][0][0];
            *(float*)(sss) = ga[2][1][0];
            SB();
            mfma_aA<184, false>(ga[1][2], x[2]); SB();
            *(float*)(sss + 128) = ga[2][2][0];
            *(float*)(sss + 256) = ga[2][3][0];
            SB();
            mfma_aA<184, false>(ga[1][3], x[3]); SB();
          }
        }
        f32x4 cs[3];
#pragma unroll
        for (int i = 0; i < 3; ++i) cs[i] = *(const f32x4*)(smem + LD_CS + w * 192 + i * 64 + kq * 16);
        bar();
        f32x2 st[4];
#pragma unroll
        for (int j = 0; j < 4; ++j) st[j] = *(const f32x2*)(smem + LD_SS + (((w + j) & 3) * 16 + c16) * 8);
        tie4(ga[0][0], ga[0][1], ga[0][2], ga[0][3]);
        tie4(ga[1][0], ga[1][1], ga[1][2], ga[1][3]);
        tie4(ga[2][0], ga[2][1], ga[2][2], ga[2][3]);
        SB();
        u32 hd[3][4];
#pragma unroll
        for (int j = 0; j < 4; ++j) {
          const f32x2 mv = ln_mv(st[j]);
          const float nl = __builtin_amdgcn_rsqf(mv[1]) * -1.4426950408889634f;
#pragma unroll
          for (int i = 0; i < 3; ++i) {
            const f32x4 v = __builtin_elementwise_fma(cs[i], (f32x4){mv[0], mv[0], mv[0], mv[0]}, ga[i][j]);
            const f32x2 g = {v[0], v[1]}, u = {v[2], v[3]};
            const f32x2 a = g * nl;
            const f32x2 e = {__builtin_amdgcn_exp2f(a[0]), __builtin_amdgcn_exp2f(a[1])};
            const f32x2 d = __builtin_elementwise_fma(e, (f32x2){mv[1], mv[1]}, (f32x2){mv[1], mv[1]});
            const f32x2 r = {__builtin_amdgcn_rcpf(d[0]), __builtin_amdgcn_rcpf(d[1])};
            hd[i][j] = pk_bf16((g * u) * r);
            if constexpr (h == 1) {
              const int t = j * 3 + i;
              if (t < 3) hf[8 + t][0] = ld16(rs, wvo, hb_e + (t == 2 ? s10 : (8 + t) * 1024));
              else hf[t - 3][1] = ld16(rs, wvo + ((t - 3) & 3) * 1024, hb_e + NT1 * 256 + ((t - 3) >> 2) * 4096);
            }
          }
          if constexpr (h == 1) SB();
        }
        const int hb = (((w + kq) & 3) * NT1 * 16 + c16) * 16, hs = h_off + ((h * 64 + rt * 4) * NT1 + nt0) * 256;
#pragma unroll
        for (int i = 0; i < 3; ++i) {
          auto a02 = __builtin_amdgcn_permlane32_swap(hd[i][0], hd[i][2], false, false);
          auto a13 = __builtin_amdgcn_permlane32_swap(hd[i][1], hd[i][3], false, false);
          auto o01 = __builtin_amdgcn_permlane16_swap(a02[0], a13[0], false, false);
          auto o23 = __builtin_amdgcn_permlane16_swap(a02[1], a13[1], false, false);
          u32x4 v = {o01[0], o01[1], o23[0], o23[1]};
          st16(rs, (i < 2 || ok2) ? hb + i * 256 : OOB, hs, v);
        }
        SB();
        if constexpr (h == 0) {
          drain_bar<3, false>();
#pragma unroll
          for (int j = 0; j < 4; ++j) { xb[0][j][0] = tr_half(xr[j]); xb[0][j][1] = tr_half(xr[j] + 512); }
        }
        SB();
      });
    }
    {
      ROLES(fresh_lane())
      char* rw[3];
#pragma unroll
      for (int i = 0; i < 3; ++i) rw[i] = smem + LD_XT + (((w + 1 + i) & 3) * 3 + i) * 2048 + wvo;
      const char* rb = smem + LD_XT + w * 6144 + wvo;
      const int rvo = c16 * (LP * 2) + kq * 8;
      const int ovo = c16 * (LP * 2) + (kq & 1) * 32 + (kq >> 1) * 16;
      char* ow = smem + LD_OT + (w * 16 + c16) * 64 + (kq & 1) * 32 + (kq >> 1) * 16;
      const char* ob = smem + LD_OT + (8 * kq + (c16 >> 2)) * 64 + (w & 1) * 32 + (c16 & 3) * 8;
      const char* wlp = smem + LD_W2 + w * 8192 + wvo;
      static_for<2>([&](auto hc) {
        constexpr int h = IC(hc);
        const int tok0 = h * 1024 + rt2 * 32;
        const int eso = ((ct * 64 + w * 16) * LP + tok0) * 2;
        const int hb = h ? hb_l : hb_e;
        f32x4 da[4][2];
        u32x4 wl[2][4];
        u32x2 res[2];
        u32 pv, pv2;
        static_for<88>([&](auto nc) {
          constexpr int n = IC(nc), j = n / 44, s = (n % 44) >> 2, i = (n + 1) & 3;
          if constexpr (s < 4) mfma_aB<192 + 16 * s + 4 * i, s == 0>(da[i][j], hf[s][j]);
          else if constexpr (s < 6) mfma_vv(da[i][j], hf[s][j], wl[s - 4][i]);
          else mfma_vv(da[i][j], hf[s][j], W2[s - 6][i]);
          SB();
          if constexpr (h == 0) {
            if constexpr (n == 3 || n == 7) hf[9 + (n >> 2)][1] = ld16(rs, wvo, hb + NT1 * 256 + (n == 7 ? s10 : 9 * 1024));
            if constexpr (n == 10) res[0] = ld8(rs, rvo, x1_off + eso);
            if constexpr (n == 13) res[1] = ld8(rs, rvo + 32, x1_off + eso);
            if constexpr (n == SIG_HL) { drain_bar<4, false>(); signal_grp(rs, layer * 32 + 16 + rt, w); }
            if constexpr (n == 86) pv = poll_ld(rs, grp_off(layer * 32 + 16 + (rt2 >> 1)), false, w);
          } else {
            if constexpr (n < 33 && n % 3 == 2) hf[n / 3][1] = ld16(rs, wvo + (n / 3 < 8 ? ((n / 3) & 3) * 1024 : 0), hb + NT1 * 256 + (n / 3 < 8 ? (n / 12) * 4096 : n / 3 == 10 ? s10 : (n / 3) * 1024));
            if constexpr (layer == 0 && n == SIG_XE) { drain_bar<(SIG_XE + 1) / 3, false>(); signal_all(rs, 2, b, w); }
            if constexpr (n == 35) res[0] = ld8(rs, rvo, x1_off + eso);
            if constexpr (n == 38) res[1] = ld8(rs, rvo + 32, x1_off + eso);
            if constexpr (layer == 0 && n >= ACC_E && n < ACC_E + 12 && !((n - ACC_E) & 1)) acc_ld1(rs, (n - ACC_E) >> 1, tid, OFF_XT + ACT2 + b * 2 * LP * 2, 0, pr);
          }
          if constexpr (n % 44 >= 6 && n % 44 < 14) wl[(n % 44 - 6) >> 2][(n % 44 - 5) & 3] = *(const u32x4*)(wlp + (((n % 44 - 6) & 4) + ((n % 44 - 5) & 3)) * 1024);
          if constexpr (n == 52 || n == 56 || n == 60) { asm volatile("" : "+v"(da[(n - 48) >> 2][0])); *(f32x4*)(rw[(n - 52) >> 2]) = da[(n - 48) >> 2][0]; }
          SB();
        });
        mfma_done4(da[0][1], da[1][1], da[2][1], da[3][1]);
        SB();
#pragma unroll
        for (int i = 0; i < 3; ++i) *(f32x4*)(rw[i] + 1024) = da[i + 1][1];
        if constexpr (h == 0) pv2 = poll_ld(rs, grp_off(layer * 32 + 16 + (rt2 >> 1)), false, w);
        bar();
        u32x2 xo[2];
#pragma unroll
        for (int j = 0; j < 2; ++j) {
          const f32x4 rf = {lo_f(res[j][0]), hi_f(res[j][0]), lo_f(res[j][1]), hi_f(res[j][1])};
          const f32x4 v = ((da[0][j] + rf) + *(const f32x4*)(rb + j * 1024)) + (*(const f32x4*)(rb + 2048 + j * 1024) + *(const f32x4*)(rb + 4096 + j * 1024));
          xo[j] = (u32x2){pk_bf16(v[0], v[1]), pk_bf16(v[2], v[3])};
        }
        auto s0 = __builtin_amdgcn_permlane16_swap(xo[0][0], xo[1][0], false, false);
        auto s1 = __builtin_amdgcn_permlane16_swap(xo[0][1], xo[1][1], false, false);
        const u32x4 xv = {s0[0], s1[0], s0[1], s1[1]};
        *(u32x4*)ow = xv;
        if constexpr (layer == 0 && h == 1) { SB(); rdy = acc_ready(pr); SB(); }
        if constexpr (layer == 0) st16(rs, ovo, OFF_XT + ACT2 + eso, xv);
        SB();
        if constexpr (h == 0) {
          poll_wait2(rs, grp_off(layer * 32 + 16 + (rt2 >> 1)), false, pv, pv2, w);
#pragma unroll
          for (int s = 0; s < 6; ++s) hf[s][0] = ld16(rs, wvo + (s & 3) * 1024, hb_l + (s >> 2) * 4096);
        }
        SB();
        bar();
        if constexpr (h == 0) {
#pragma unroll
          for (int s = 6; s < 11; ++s) hf[s][0] = ld16(rs, wvo + (s < 8 ? (s & 3) * 1024 : 0), hb_l + (s < 8 ? 4096 : s == 10 ? s10 : s * 1024));
          SB();
        }
        if constexpr (layer == 0) {
          if (w < 2) {
            u32x4 f0 = tr_frag(ob, 0, 256), f1 = tr_frag(ob, 2048, 2048 + 256);
            const u32x4 ones = splat4(0x3f803f80u);
            f32x4 sa, qa;
            mfma_v0(sa, ones, f0); mfma_v0(qa, f0, f0); mfma_vv(sa, ones, f1); mfma_vv(qa, f1, f1);
            asm volatile("s_nop 7" : "+v"(sa), "+v"(qa));
            const int r = c16 & 3;
            float q = r == 0 ? qa[0] : r == 1 ? qa[1] : r == 2 ? qa[2] : qa[3];
            u32x2 v = {__float_as_uint(sa[0]), __float_as_uint(q)};
            const bool diag = (c16 >> 2) == kq;
            const bool fits = q < 0x1p29f;
            const u32x2 fs = to_fix(fits ? sa[0] * 0x1p30f : 0.f), fq = to_fix(fits ? q * 0x1p26f : 0x1p59f);
            const int avo = diag ? (c16 & 3) * 4096 + kq * 16 : OOB, aso = OFF_AC + h * 16384 + (rt2 * 8 + w * 4) * 16;
            asm volatile("s_nop 4\n\tbuffer_atomic_add_x2 %0, %2, %3, %4 offen sc1\n\tbuffer_atomic_add_x2 %1, %2, %3, %4 offen offset:8 sc1"
                         :: "v"(fs), "v"(fq), "v"(avo), "s"(rs), "s"(aso) : "memory");
            __builtin_amdgcn_raw_buffer_store_b64(v, rs, diag ? c16 * 8 : OOB, OFF_ST1 + (ct * L + tok0 + w * 16) * 8, SC1);
          }
        } else {
          u32x4 f = tr_frag(ob, (w >> 1) * 2048, (w >> 1) * 2048 + 256);
          const rsrc_t ro = __builtin_amdgcn_make_buffer_rsrc(out, 0, L * D * 2, 0x00020000);
          __builtin_amdgcn_raw_buffer_store_b128(f, ro, (c16 * D + kq * 8) * 2, ((tok0 + (w & 1) * 16) * D + ct * 64 + (w >> 1) * 32) * 2, SC1);
        }
        SB();
      });
    }
    if constexpr (layer == 0) {
      ROLES(fresh_lane())
      const int xo = b * 2 * LP * 2;
      f32x4 z[2], acc[4];
      u32 pv, pv2;
      acc_wait(rs, tid, OFF_XT + ACT2 + xo, 0, all_off(2, b), pr, rdy);
      acc_ln(rs, smem, 0, tid, dsk, pr, z);
      bar();
      conv_mfma<0, 0, 3, true>(smem, w, lane, acc, [&](auto nc) { if constexpr (IC(nc) == 30) { drain_bar<0, false>(); signal_all(rs, 3, b, w); } });
      conv_out(rs, smem, OFF_X1 + ACT2 + xo, w, lane, acc, z);
      conv_mfma<4, 0, 3, true>(smem, w, lane, acc, [&](auto nc) {
        constexpr int n = IC(nc);
        if constexpr (n >= ACC_L && n < ACC_L + 12 && !((n - ACC_L) & 1)) acc_ld1(rs, (n - ACC_L) >> 1, tid, OFF_XT + ACT2 + xo + 2048, 1, pr);
        if constexpr (n == 36) { drain_bar<6, false>(); signal_all(rs, 4, b, w); }
      });
      acc_wait(rs, tid, OFF_XT + ACT2 + xo + 2048, 1, all_off(3, b), pr, acc_ready(pr));
      acc_ln(rs, smem, 1, tid, dsk, pr, z);
      bar();
      const int gx = xs_voff(w, lane), gs = OFF_X1 + ACT2 + rt * 128;
      conv_mfma<4, 4, 7, false>(smem, w, lane, acc, [&](auto nc) {
        constexpr int n = IC(nc);
        if constexpr (n == CE_P) pv = poll_ld(rs, all_off(4, b), true, w);
        if constexpr (n == CE_P + 14) pv2 = poll_ld(rs, all_off(4, b), true, w);
      });
      poll_wait2(rs, all_off(4, b), true, pv, pv2, w);
      static_for<8>([&](auto ic) { xs_dma<IC(ic)>(rs, gx, gs, ldsw); });
      conv_out(rs, smem, OFF_X1 + ACT2 + xo + 2048, w, lane, acc, z);
      drain_bar<6, false>();
    }
  });
  asm volatile(".Lmk_code_end:" ::: "memory");
}

extern "C" void kernel_launch(void* const* d_in, const int* in_sizes, int n_in, void* d_out, int out_size,
                              void* d_ws, size_t ws_size, hipStream_t stream) {
  char* ws = (char*)d_ws;
  (void)hipFuncSetAttribute((const void*)main_kernel, hipFuncAttributeMaxDynamicSharedMemorySize, LDS_BYTES);
  main_kernel<<<256, 256, LDS_BYTES, stream>>>(
      (const u16*)d_in[0], (const u16*)d_in[1], (const u16*)d_in[2], (const u16*)d_in[3], (const u16*)d_in[4],
      (const u16*)d_in[5], (const u16*)d_in[6], (const u16*)d_in[7], (const u16*)d_in[8], (const u16*)d_in[10],
      (const u16*)d_in[11], (const u16*)d_in[12], ws, (u16*)d_out);
}
```

```cpp
#include <hip/hip_runtime.h>
#include <stdint.h>
#include <type_traits>

typedef unsigned short u16;
typedef unsigned int u32;
typedef u32 u32x4 __attribute__((ext_vector_type(4)));
typedef u32 u32x2 __attribute__((ext_vector_type(2)));
typedef float f32x4 __attribute__((ext_vector_type(4)));
typedef float f32x2 __attribute__((ext_vector_type(2)));
typedef short s16x4 __attribute__((ext_vector_type(4)));
typedef __bf16 bf16x2 __attribute__((ext_vector_type(2)));
typedef _Float16 f16x2 __attribute__((ext_vector_type(2)));
#define LAS __attribute__((address_space(3)))
#define GAS __attribute__((address_space(1)))
#define DEV __device__ __forceinline__
#define SB() __builtin_amdgcn_sched_barrier(0)
#define IC(x) decltype(x)::value
typedef __amdgpu_buffer_rsrc_t rsrc_t;

constexpr int L = 2048, D = 512, NS = 128, F = 1376;
constexpr int NT1 = 172;
constexpr int KS1 = 16;
constexpr int KS2P = 44;
constexpr float EPS = 1e-5f;
constexpr int SC1 = 16;
#define P_SIG_CL 9
#define P_SIG_HE 12
#define P_SIG_HL 44
#define P_SIG_XE 30
#define P_POLL_G 52
#define CE_P 14
#define X0_P 13
#define NSH 16
#define SHSEL (b & (NSH - 1))
#define ACC_E 74
#define ACC_L 20
constexpr int SIG_CL = P_SIG_CL, SIG_HE = P_SIG_HE, SIG_HL = P_SIG_HL, SIG_XE = P_SIG_XE, POLL_G = P_POLL_G;

#define XPAD 64
constexpr int LP = L + XPAD;
constexpr int ACT = D * LP * 2;
constexpr int HID = F * L * 2;
constexpr int N_ALL = 7;
constexpr int OFF_CA  = 0;
constexpr int OFF_CH  = OFF_CA + N_ALL * 128 * 256;
constexpr int OFF_INIT = OFF_CH + 64 * 256;
constexpr int CTRL_BYTES = OFF_INIT + 8 * 256;
constexpr int OFF_XT  = CTRL_BYTES;
constexpr int ACT2 = ACT, HID2 = HID;
constexpr int OFF_X1  = OFF_XT + ACT + ACT2;
constexpr int OFF_H   = OFF_X1 + ACT + ACT2;
constexpr int OFF_WGU = OFF_H + HID + HID2;
constexpr int OFF_WD  = OFF_WGU + (NT1 + 1) * KS1 * 1024;
constexpr int OFF_ST0 = OFF_WD + 32 * KS2P * 1024;
constexpr int OFF_ST1 = OFF_ST0 + L * 8;
constexpr int OFF_AC  = OFF_ST1 + 8 * L * 8;
constexpr int WS_END  = OFF_AC + L * 16;

#define STAMP_DECL
#define STAMP()
#define STAMPE()
#define STAMP_OUT()
#define LDS_EXTRA 0

DEV float lo_f(u32 v) { return __uint_as_float(v << 16); }
DEV float hi_f(u32 v) { return __uint_as_float(v & 0xffff0000u); }
DEV u32 pk_bf16(float a, float b) {
  f32x2 v = {a, b};
  return __builtin_bit_cast(u32, __builtin_convertvector(v, bf16x2));
}
DEV u32 pk_bf16(f32x2 v) { return __builtin_bit_cast(u32, __builtin_convertvector(v, bf16x2)); }
DEV u32 pk_f16(f32x2 v) { return __builtin_bit_cast(u32, __builtin_convertvector(v, f16x2)); }
DEV f32x2 unpk(u32 v) { return (f32x2){lo_f(v), hi_f(v)}; }
DEV float rbf(float a) { return lo_f(pk_bf16(a, a)); }
DEV f32x2 ln_mv(f32x2 sq) {
  f32x2 t = __builtin_elementwise_fma(sq, (f32x2){1.0f / D, 1.0f / D}, (f32x2){0.f, EPS});
  t[1] = fmaxf(__builtin_fmaf(-t[0], t[0], t[1]), EPS);
  return t;
}
struct CM { f32x2 re, im; };
DEV CM mkcm(f32x2 c) { return CM{(f32x2){c[0], c[0]}, (f32x2){-c[1], c[1]}}; }
DEV f32x2 cmul_h(f32x2 x, const CM& c, u32& h) {
  const f32x2 r = __builtin_elementwise_fma(x.yx, c.im, x * c.re);
  h = pk_f16(r);
  return r;
}

constexpr int KCOPY = 4160, KCH = 4 * KCOPY;
constexpr int LD_U = 2 * KCH;
constexpr int UCH = (256 + L) * 2;
constexpr int LD_XT = LD_U + 2 * UCH;
constexpr int LD_Y = LD_XT + 65536;
constexpr int YW = 5120;
constexpr int LD_SS = LD_Y + 4 * YW;
constexpr int LD_OT = LD_Y;
constexpr int LD_CS = LD_SS + 512;
constexpr int LD_W2 = LD_CS + 768;
constexpr int LDS_BYTES = LD_W2 + 32768;
static_assert(LDS_BYTES <= 163840, "LDS");
constexpr int LD_KT = LD_XT + 16384;
constexpr int RROW = 144, RW = 64 * RROW;

#define ACLOB "a0","a1","a2","a3","a4","a5","a6","a7","a8","a9","a10","a11","a12","a13","a14","a15","a16","a17","a18","a19","a20","a21","a22","a23","a24","a25","a26","a27","a28","a29","a30","a31","a32","a33","a34","a35","a36","a37","a38","a39","a40","a41","a42","a43","a44","a45","a46","a47","a48","a49","a50","a51","a52","a53","a54","a55","a56","a57","a58","a59","a60","a61","a62","a63","a64","a65","a66","a67","a68","a69","a70","a71","a72","a73","a74","a75","a76","a77","a78","a79","a80","a81","a82","a83","a84","a85","a86","a87","a88","a89","a90","a91","a92","a93","a94","a95","a96","a97","a98","a99","a100","a101","a102","a103","a104","a105","a106","a107","a108","a109","a110","a111","a112","a113","a114","a115","a116","a117","a118","a119","a120","a121","a122","a123","a124","a125","a126","a127","a128","a129","a130","a131","a132","a133","a134","a135","a136","a137","a138","a139","a140","a141","a142","a143","a144","a145","a146","a147","a148","a149","a150","a151","a152","a153","a154","a155","a156","a157","a158","a159","a160","a161","a162","a163","a164","a165","a166","a167","a168","a169","a170","a171","a172","a173","a174","a175","a176","a177","a178","a179","a180","a181","a182","a183","a184","a185","a186","a187","a188","a189","a190","a191","a192","a193","a194","a195","a196","a197","a198","a199","a200","a201","a202","a203","a204","a205","a206","a207","a208","a209","a210","a211","a212","a213","a214","a215","a216","a217","a218","a219","a220","a221","a222","a223","a224","a225","a226","a227","a228","a229","a230","a231","a232","a233","a234","a235","a236","a237","a238","a239","a240","a241","a242","a243","a244","a245","a246","a247","a248","a249","a250","a251","a252","a253","a254","a255"
#define MF "v_mfma_f32_16x16x32_bf16 "
DEV void mfma_vv(f32x4& c, u32x4 a, u32x4 b) { asm(MF "%0, %1, %2, %0" : "+v"(c) : "v"(a), "v"(b)); }
DEV void mfma_v0(f32x4& c, u32x4 a, u32x4 b) { asm(MF "%0, %1, %2, 0" : "=&v"(c) : "v"(a), "v"(b)); }
template <bool FIRST> DEV void mfma_v(f32x4& c, u32x4 a, u32x4 b) { if constexpr (FIRST) mfma_v0(c, a, b); else mfma_vv(c, a, b); }
template <int R, bool FIRST> DEV void mfma_aA(f32x4& c, u32x4 b) {
  if constexpr (FIRST) asm(MF "%0, a[%c2:%c3], %1, 0" : "=&v"(c) : "v"(b), "i"(R), "i"(R + 3));
  else asm(MF "%0, a[%c2:%c3], %1, %0" : "+v"(c) : "v"(b), "i"(R), "i"(R + 3));
}
template <int R, bool FIRST> DEV void mfma_aB(f32x4& c, u32x4 a) {
  if constexpr (FIRST) asm(MF "%0, %1, a[%c2:%c3], 0" : "=&v"(c) : "v"(a), "i"(R), "i"(R + 3));
  else asm(MF "%0, %1, a[%c2:%c3], %0" : "+v"(c) : "v"(a), "i"(R), "i"(R + 3));
}
template <bool FIRST> DEV void mfma_h(f32x4& c, u32x4 a, u32x4 b) {
  if constexpr (FIRST) asm("v_mfma_f32_16x16x32_f16 %0, %1, %2, 0" : "=&v"(c) : "v"(a), "v"(b));
  else asm("v_mfma_f32_16x16x32_f16 %0, %1, %2, %0" : "+v"(c) : "v"(a), "v"(b));
}
DEV void mfma_done4(f32x4& a, f32x4& b, f32x4& c, f32x4& d) { asm volatile("s_nop 7" : "+v"(a), "+v"(b), "+v"(c), "+v"(d)); }
DEV void tie4(f32x4& a, f32x4& b, f32x4& c, f32x4& d) { asm volatile("" : "+v"(a), "+v"(b), "+v"(c), "+v"(d)); }
DEV void bar() { asm volatile("s_waitcnt lgkmcnt(0)\n\ts_barrier" ::: "memory", ACLOB); }

DEV u32x4 ld16(rsrc_t rs, int voff, int soff) { return __builtin_amdgcn_raw_buffer_load_b128(rs, voff, soff, SC1); }
DEV u32x2 ld8(rsrc_t rs, int voff, int soff) { return __builtin_amdgcn_raw_buffer_load_b64(rs, voff, soff, SC1); }
DEV void st16(rsrc_t rs, int voff, int soff, u32x4 v) { __builtin_amdgcn_raw_buffer_store_b128(v, rs, voff, soff, SC1); }
DEV int fresh_lane() {
  int l;
  asm volatile("v_mbcnt_lo_u32_b32 %0, -1, 0\n\tv_mbcnt_hi_u32_b32 %0, -1, %0" : "=v"(l));
  return l;
}
DEV u32x4 splat4(u32 c) {
  u32x4 r;
  asm volatile("v_mov_b32 %0, %4\n\tv_mov_b32 %1, %4\n\tv_mov_b32 %2, %4\n\tv_mov_b32 %3, %4"
               : "=v"(r[0]), "=v"(r[1]), "=v"(r[2]), "=v"(r[3]) : "i"(c));
  asm volatile("s_nop 1" : "+v"(r));
  return r;
}
DEV int opq(int x) { asm volatile("" : "+s"(x)); return x; }
DEV u32x2 tr_half(const char* p) { return __builtin_bit_cast(u32x2, __builtin_amdgcn_ds_read_tr16_b64_v4i16((LAS s16x4*)p)); }
DEV u32x4 tr_frag(const char* p, int o0, int o1) {
  u32x2 a = tr_half(p + o0), c = tr_half(p + o1);
  return (u32x4){a[0], a[1], c[0], c[1]};
}
template <int N, typename FN, int I = 0>
DEV void static_for(FN&& fn) {
  if constexpr (I < N) { fn(std::integral_constant<int, I>{}); static_for<N, FN, I + 1>(static_cast<FN&&>(fn)); }
}

constexpr int OOB = 0x7ffffff0;
constexpr int SPIN_CAP = 400000;
#define ACC_SLEEP 2
#define POLL_PAUSE "s_nop 4\n\t"
DEV int all_off(int edge, int b) { return OFF_CA + (edge * 8 + (b & 7)) * 4096; }
DEV int grp_off(int idx) { return OFF_CH + idx * 256; }
DEV u32 poll_ld(rsrc_t rs, int soff, bool all, int w) {
  const int voff = (all ? (fresh_lane() & (NSH - 1)) * 256 : 0) | (w == 3 ? 0 : 0x7ff00000);
  return __builtin_amdgcn_raw_buffer_load_b32(rs, voff, soff, SC1);
}
DEV void poll_spin(rsrc_t rs, int soff, bool all, u32 f) {
  int spins;
  u32 voff;
  asm volatile("v_cmp_gt_u32 vcc, 64, %0\n\t"
               "s_mov_b32 %1, %c5\n\t"
               "s_nop 3\n\t"
               "s_cbranch_vccz 2f\n\t"
               "v_mbcnt_lo_u32_b32 %2, -1, 0\n\t"
               "v_and_b32 %2, %c6, %2\n\t"
               "v_lshlrev_b32 %2, 8, %2\n"
               "1:\n\t"
               "buffer_load_dword %0, %2, %3, %4 offen sc1\n\t"
               "s_sub_u32 %1, %1, 1\n\t"
               "s_waitcnt vmcnt(0)\n\t"
               "s_cbranch_scc1 2f\n\t"
               "v_cmp_gt_u32 vcc, 64, %0\n\t"
               "s_nop 4\n\t"
               "s_cbranch_vccnz 1b\n"
               "2:"
               : "+v"(f), "=&s"(spins), "=&v"(voff) : "s"(rs), "s"(soff), "i"(SPIN_CAP), "i"(all ? NSH - 1 : 0) : "memory", "vcc", "scc");
}
DEV void poll_wait(rsrc_t rs, int soff, bool all, u32 f, int w) {
  if (w == 3) poll_spin(rs, soff, all, f);
  asm volatile("s_barrier" ::: "memory", ACLOB);
}
DEV void poll_wait2(rsrc_t rs, int soff, bool all, u32 f0, u32 f1, int w) {
  if (w == 3) { if (!__all(f0 >= 64u)) poll_spin(rs, soff, all, f1); }
  asm volatile("s_barrier" ::: "memory", ACLOB);
}
template <int YOUNGER, bool LDSW>
DEV void drain_bar() {
  if constexpr (LDSW) asm volatile("s_waitcnt vmcnt(%c0) lgkmcnt(0)\n\ts_barrier" :: "i"(YOUNGER) : "memory", ACLOB);
  else asm volatile("s_waitcnt vmcnt(%c0)\n\ts_barrier" :: "i"(YOUNGER) : "memory", ACLOB);
}
DEV void signal_all(rsrc_t rs, int edge, int b, int w) {
  if (w == 0) {
    const int lane = fresh_lane();
    __builtin_amdgcn_raw_ptr_buffer_atomic_add_i32(NSH / 4, rs, lane < 8 ? lane * 4096 : OOB, OFF_CA + edge * 32768 + (SHSEL) * 256, SC1);
  }
}
DEV void signal_grp(rsrc_t rs, int idx, int w) {
  if (w == 0) {
    const int lane = fresh_lane();
    __builtin_amdgcn_raw_ptr_buffer_atomic_add_i32(4, rs, lane == 0 ? 0 : OOB, OFF_CH + idx * 256, SC1);
  }
}

DEV void init_bump(rsrc_t rs) {
  const int lane = fresh_lane();
  __builtin_amdgcn_raw_ptr_buffer_atomic_add_i32(1, rs, lane < 8 ? lane * 256 : OOB, OFF_INIT, SC1);
}
DEV void init_wait(rsrc_t rs, int soff, u32 f0, u32 f) {
  int spins;
  asm volatile("v_cmp_eq_u32 vcc, %2, %0\n\t"
               "s_mov_b32 %1, %c5\n\t"
               "s_nop 3\n\t"
               "s_cbranch_vccz 2f\n"
               "1:\n\t"
               "buffer_load_dword %0, off, %3, %4 sc1\n\t"
               "s_sub_u32 %1, %1, 1\n\t"
               "s_waitcnt vmcnt(0)\n\t"
               "s_cbranch_scc1 2f\n\t"
               "v_cmp_eq_u32 vcc, %2, %0\n\t"
               "s_nop 4\n\t"
               "s_cbranch_vccnz 1b\n"
               "2:"
               : "+v"(f), "=&s"(spins) : "v"(f0), "s"(rs), "s"(soff), "i"(SPIN_CAP) : "memory", "vcc", "scc");
}

template <int P> struct PrepRegs { u32x4 pa[P], pb[P]; u32x2 xv[2]; };
template <int P>
DEV void prep_ld1(rsrc_t rs, int i, int tid, int xso, int sso, PrepRegs<P>& pr) {
  if (i < 2 * P) {
    if (i & 1) pr.pb[i >> 1] = ld16(rs, tid * 32 + 16, sso + (i >> 1) * (L * 8));
    else pr.pa[i >> 1] = ld16(rs, tid * 32, sso + (i >> 1) * (L * 8));
  } else pr.xv[i - 2 * P] = ld8(rs, tid * 8, xso + (i - 2 * P) * (LP * 2));
}
template <int P>
DEV void prep_load(rsrc_t rs, int tid, int xso, int sso, PrepRegs<P>& pr) {
#pragma unroll
  for (int i = 0; i < 2 * P + 2; ++i) prep_ld1<P>(rs, i, tid, xso, sso, pr);
  SB();
}
DEV void kill4(u32x4& x) { asm volatile("" : "=v"(x)); }
DEV void kill2(u32x2& x) { asm volatile("" : "=v"(x)); }
template <int P> DEV void prep_kill(PrepRegs<P>& pr) {
#pragma unroll
  for (int p = 0; p < P; ++p) { kill4(pr.pa[p]); kill4(pr.pb[p]); }
  kill2(pr.xv[0]); kill2(pr.xv[1]);
}
DEV void ln_apply(char* smem, int h, int tid, u32 dsk, f32x4 s0, f32x4 s1, const u32x2 (&xv)[2], f32x4 (&z)[2]) {
  const f32x2 t0 = ln_mv(s0.xy), t1 = ln_mv(s0.zw), t2 = ln_mv(s1.xy), t3 = ln_mv(s1.zw);
  const f32x2 rr[2] = {{__builtin_amdgcn_rsqf(t0[1]), __builtin_amdgcn_rsqf(t1[1])}, {__builtin_amdgcn_rsqf(t2[1]), __builtin_amdgcn_rsqf(t3[1])}};
  const f32x2 mr[2] = {{-t0[0] * rr[0][0], -t1[0] * rr[0][1]}, {-t2[0] * rr[1][0], -t3[0] * rr[1][1]}};
#pragma unroll
  for (int c = 0; c < 2; ++c) {
    const float dk = c ? hi_f(dsk) : lo_f(dsk);
    u32x2 u;
    f32x2 zz[2];
#pragma unroll
    for (int i = 0; i < 2; ++i) {
      const f32x2 x = unpk(xv[c][i]);
      const f32x2 uf = __builtin_elementwise_fma(x, rr[i], mr[i]);
      u[i] = pk_bf16(uf);
      zz[i] = __builtin_elementwise_fma(uf, (f32x2){dk, dk}, x);
    }
    z[c] = (f32x4){zz[0][0], zz[0][1], zz[1][0], zz[1][1]};
    *(u32x2*)(smem + LD_U + c * UCH + (256 + h * 1024) * 2 + tid * 8) = u;
  }
}
DEV void prep_ln(char* smem, int h, int tid, u32 dsk, const PrepRegs<1>& pr, f32x4 (&z)[2]) {
  ln_apply(smem, h, tid, dsk, __builtin_bit_cast(f32x4, pr.pa[0]), __builtin_bit_cast(f32x4, pr.pb[0]), pr.xv, z);
}
struct AccRegs { u32x4 a[4]; u32x2 xv[2]; };
DEV void acc_ld1(rsrc_t rs, int i, int tid, int xso, int h, AccRegs& r) {
  if (i < 4) r.a[i] = ld16(rs, tid * 16, OFF_AC + h * 16384 + i * 4096);
  else r.xv[i - 4] = ld8(rs, tid * 8, xso + (i - 4) * (LP * 2));
}
DEV u32x2 to_fix(float t) {
  const float hi = __builtin_floorf(t * 0x1p-32f);
  const u32 lo = (u32)fminf(__builtin_fmaf(hi, -0x1p32f, t), 4294967040.f);
  return (u32x2){(lo & ~15u) | 1u, (u32)(int)hi};
}
DEV bool acc_ready(const AccRegs& r) {
  u32 o = 0u, n = ~0u;
#pragma unroll
  for (int i = 0; i < 4; ++i) { o |= r.a[i][0] | r.a[i][2]; n &= r.a[i][0] & r.a[i][2]; }
  const u32 mx = max(max(r.xv[0][0], r.xv[0][1]), max(r.xv[1][0], r.xv[1][1]));
  return __all(((o & 7u) | (~n & 8u)) == 0u && mx != ~0u);
}
DEV void acc_wait(rsrc_t rs, int tid, int xso, int h, int poll_off, AccRegs& r, bool ready) {
  if (!ready) {
#pragma unroll 1
    for (int spins = 0; spins < SPIN_CAP; ++spins) {
      bool done = false;
      if ((spins & 7) == 7) done = __all(__builtin_amdgcn_raw_buffer_load_b32(rs, (tid & (NSH - 1)) * 256, poll_off, SC1) >= 64u);
      asm volatile("" ::: "memory");
#pragma unroll
      for (int i = 0; i < 6; ++i) acc_ld1(rs, i, tid, xso, h, r);
      if (done || acc_ready(r)) break;
      __builtin_amdgcn_s_sleep(ACC_SLEEP);
    }
    __builtin_amdgcn_s_waitcnt(0x0F70);
  }
}
DEV void acc_ln(rsrc_t rs, char* smem, int h, int tid, u32 dsk, const AccRegs& r, f32x4 (&z)[2]) {
  f32x2 st[4];
#pragma unroll
  for (int i = 0; i < 4; ++i) {
    const f32x2 hi = {(float)(int)r.a[i][1], (float)(int)r.a[i][3]}, lo = {(float)r.a[i][0], (float)r.a[i][2]};
    st[i] = __builtin_elementwise_fma(hi, (f32x2){0x1p2f, 0x1p6f}, lo * (f32x2){0x1p-30f, 0x1p-26f});
  }
  f32x4 s0 = {st[0][0], st[0][1], st[1][0], st[1][1]}, s1 = {st[2][0], st[2][1], st[3][0], st[3][1]};
  const u32 mk = (r.a[0][3] | r.a[1][3]) | (r.a[2][3] | r.a[3][3]);
  if (__builtin_expect(__any(mk >= (1u << 27)), 0)) {
    poll_spin(rs, all_off(2 + h, blockIdx.x), true, 0u);
    s0 = s1 = (f32x4){0.f, 0.f, 0.f, 0.f};
#pragma unroll 1
    for (int p = 0; p < 8; ++p) {
      s0 += __builtin_bit_cast(f32x4, ld16(rs, tid * 32, OFF_ST1 + h * 8192 + p * (L * 8)));
      s1 += __builtin_bit_cast(f32x4, ld16(rs, tid * 32 + 16, OFF_ST1 + h * 8192 + p * (L * 8)));
    }
  }
  ln_apply(smem, h, tid, dsk, s0, s1, r.xv, z);
}
template <int M0, int I0, int I1> struct ConvPlan {
  static constexpr int Q0 = (M0 - I1) > 0 ? (M0 - I1) : 0, Q1 = M0 + 3 - I0;
  static constexpr int NQ = Q1 - Q0 + 1, NU = I1 - I0 + 1, NF = NQ + NU;
  static constexpr int count() { int n = 0; for (int i = I0; i <= I1; ++i) for (int m = 0; m < 4; ++m) n += (M0 + m - i >= 0); return n; }
  static constexpr int NM = count();
  static constexpr int im(int t) { int n = 0; for (int i = I0; i <= I1; ++i) for (int m = 0; m < 4; ++m) if (M0 + m - i >= 0) { if (n == t) return i * 4 + m; ++n; } return 0; }
  static constexpr int frag(int f) {
    if (f == 0) return 0;
    if (f <= 4) return -(Q1 - 4 + f + 1);
    int g = f - 5, nlow = NQ - 4;
    if (g < 2 * nlow) return (g & 1) ? -(Q1 - 4 - (g >> 1) + 1) : 1 + (g >> 1);
    return 1 + nlow + (g - 2 * nlow);
  }
};
template <int M0, int I0, int I1, bool FIRST, typename FN>
DEV void conv_mfma(char* smem, int w, int lane, f32x4 (&acc)[4], FN&& fill) {
  using P = ConvPlan<M0, I0, I1>;
  const int c16 = lane & 15, kq = lane >> 4;
  const int cch = w >> 1, wr = w & 1;
  const char* tb = smem + cch * KCH + (c16 >> 2) * KCOPY + (16 + 16 * (1 + 8 * wr) - 8 * kq + 4 * (c16 & 3) - 4) * 2;
  const char* ub = smem + LD_U + cch * UCH + (256 - 16 * (1 + 8 * wr + 6) + 16 * c16 + 8 * kq) * 2;
  u32x4 T[2][P::NQ], U[2][P::NU];
  auto ldf = [&](auto rc, auto fc) {
    constexpr int rr = IC(rc), f = P::frag(IC(fc));
    if constexpr (f >= 0) U[rr & 1][f] = *(const u32x4*)(ub + (3 - rr) * 64 + (I0 + f) * 512);
    else T[rr & 1][-f - 1 - P::Q0] = tr_frag(tb, rr * 64 + (-f - 1) * 512 + 8, rr * 64 + (-f - 1) * 512);
  };
  static_for<P::NF>([&](auto fc) { ldf(std::integral_constant<int, 0>{}, fc); });
  SB();
  static_for<4 * P::NM>([&](auto nc) {
    constexpr int n = IC(nc), rr = n / P::NM, t = n % P::NM, i = P::im(t) >> 2, m = P::im(t) & 3, q = M0 + m - i;
    mfma_v<FIRST && rr == 0 && i == I0>(acc[m], T[rr & 1][q - P::Q0], U[rr & 1][i - I0]);
    if constexpr (rr < 3 && t < P::NF) ldf(std::integral_constant<int, rr + 1>{}, std::integral_constant<int, t>{});
    SB();
    fill(nc);
    SB();
  });
}
DEV void conv_out(rsrc_t rs, char* smem, int xso, int w, int lane, f32x4 (&acc)[4], const f32x4 (&z)[2]) {
  const int tid = w * 64 + lane;
  char* yw = smem + LD_Y + w * YW + (lane & 15) * 80 + (lane >> 4) * 16;
  const char* yb = smem + LD_Y + tid * 16 + (tid >> 2) * 16;
  const int vo = ((tid & 1) * LP + (tid >> 1) * 8) * 2;
  mfma_done4(acc[0], acc[1], acc[2], acc[3]);
#pragma unroll
  for (int m = 0; m < 4; ++m) *(f32x4*)(yw + m * 1280) = acc[m];
  bar();
  u32x2 o[2];
#pragma unroll
  for (int c = 0; c < 2; ++c) {
    const f32x4 y = (*(const f32x4*)(yb + (2 * c) * YW) + z[c]) + *(const f32x4*)(yb + (2 * c + 1) * YW);
    o[c] = (u32x2){pk_bf16(y[0], y[1]), pk_bf16(y[2], y[3])};
  }
  const bool odd = tid & 1;
  u32 s0 = odd ? o[0][0] : o[1][0], s1 = odd ? o[0][1] : o[1][1];
  u32 g0 = __builtin_amdgcn_update_dpp(0u, s0, 0xB1, 0xf, 0xf, true);
  u32 g1 = __builtin_amdgcn_update_dpp(0u, s1, 0xB1, 0xf, 0xf, true);
  u32x4 v = odd ? (u32x4){g0, g1, o[1][0], o[1][1]} : (u32x4){o[0][0], o[0][1], g0, g1};
  st16(rs, vo, xso, v);
  SB();
}

template <int R, int IMM>
DEV void ld_agpr(rsrc_t rs, int voff, int sbase) {
  int tmp;
  asm volatile("s_add_u32 %0, %3, %c6\n\tbuffer_load_dwordx4 a[%c4:%c5], %1, %2, %0 offen sc1"
               : "=&s"(tmp) : "v"(voff), "s"(rs), "s"(sbase), "i"(R), "i"(R + 3), "i"(IMM) : "memory", "scc");
}
template <int I> DEV void ld_w1(rsrc_t rs, int voff, int so01, int so2) {
  constexpr int ks = I / 3, i = I % 3;
  if constexpr (i < 2) ld_agpr<4 * I, (i * KS1 + ks) * 1024>(rs, voff, so01);
  else ld_agpr<4 * I, ks * 1024>(rs, voff, so2);
}
template <int R, int IMM>
DEV void ld_agpr_imm(rsrc_t rs, int voff, int soff) {
  asm volatile("buffer_load_dwordx4 a[%c3:%c4], %0, %1, %2 offen offset:%c5 sc1"
               :: "v"(voff), "s"(rs), "s"(soff), "i"(R), "i"(R + 3), "i"(IMM) : "memory");
}

constexpr int XS_STEP = 32 * LP * 2;
DEV int xs_voff(int w, int lane) {
  const int fsw = ((lane >> 4) & 1) | ((w & 1) << 1);
  return (w * 8 + (lane >> 3)) * (LP * 2) + ((((lane & 7) >> 1) ^ fsw) * 32) + (lane & 1) * 16;
}
template <int KS>
DEV void xs_dma(rsrc_t rs, int voff, int sbase, int ldsw) {
  int tmp;
  asm volatile("s_add_u32 m0, %4, %c5\n\ts_add_u32 %0, %3, %c6\n\tbuffer_load_dwordx4 %1, %2, %0 offen sc1 lds"
               : "=&s"(tmp) : "v"(voff), "s"(rs), "s"(sbase), "s"(ldsw), "i"(LD_XT + KS * 4096), "i"(KS * XS_STEP) : "memory", "scc");
}

__global__ __launch_bounds__(256, 1) void main_kernel(
    const u16* __restrict__ x0, const u16* __restrict__ Are, const u16* __restrict__ Aim,
    const u16* __restrict__ Bre, const u16* __restrict__ Bim, const u16* __restrict__ Cre,
    const u16* __restrict__ Cim, const u16* __restrict__ logdt, const u16* __restrict__ dskip,
    const u16* __restrict__ wg, const u16* __restrict__ wu, const u16* __restrict__ wd,
    char* __restrict__ ws, u16* __restrict__ out) {
  extern __shared__ __attribute__((aligned(16))) char smem[];
  const int w_ = __builtin_amdgcn_readfirstlane(threadIdx.x >> 6);
  const int b_ = blockIdx.x;

  const rsrc_t rs = __builtin_amdgcn_make_buffer_rsrc(ws, 0, WS_END, 0x00020000);
  STAMP_DECL
  STAMPE();
  asm volatile("" ::: ACLOB);
  u32 cpf;
  {
    unsigned long long pc; int len;
    asm volatile("s_getpc_b64 %0\n.Lmk_code_begin:\n\ts_mov_b32 %1, .Lmk_code_end-.Lmk_code_begin\n\t"
                 "s_max_i32 %1, %1, .Lmk_text_end-.Lmk_code_begin" : "=s"(pc), "=s"(len) :: "scc");
    const int off = min((int)((min(threadIdx.x & 63, 4u) * 4 + (threadIdx.x >> 6)) * 32 + (blockIdx.x >> 3)) * 128, len - 4) & ~3;
    cpf = *(const GAS u32*)(pc + (unsigned long long)(u32)off);
    SB();
    asm volatile("" :: "s"(x0), "s"(Are), "s"(Aim), "s"(Bre), "s"(Bim), "s"(Cre), "s"(Cim), "s"(logdt), "s"(dskip), "s"(wg), "s"(wu), "s"(wd), "s"(ws));
  }
#define MAPG1 1
  const int rt_ = ((b_ & 7) >> 2) * 8 + ((b_ >> 3) & 7), cb_ = (b_ & 3) * 4 + (b_ >> 6);
  const int rt2_ = (b_ & 7) * 4 + ((b_ >> 3) & 3), ct_ = b_ >> 5;
  const int start_ = cb_ * 11 - (cb_ >> 2), cnt_ = (cb_ & 3) == 3 ? 10 : 11;
  const int nbase_ = (w_ < 3) ? w_ * 3 : cnt_ - 2;
  const int nt0_ = start_ + nbase_;
  const int ok2_ = __builtin_amdgcn_readfirstlane((w_ < 3) && (nbase_ + 2 < cnt_ - 2));
#define ROLES(LANE) \
  const int w = w_, b = b_, rt = rt_, rt2 = rt2_, ct = ct_, nt0 = nt0_, ok2 = ok2_; \
  const int lane = LANE, tid = w * 64 + lane, c16 = lane & 15, kq = lane >> 4, wvo = lane * 16; \
  const int x1_off = OFF_X1 + layer * ACT2, h_off = OFF_H + layer * HID2; \
  const int hb_e = h_off + (rt2 * 2 * NT1 * 16 + w * 11 * 64) * 16, hb_l = hb_e + 64 * NT1 * 256; \
  const int s10 = (w == 3) ? 9 * 1024 : 10 * 1024; \
  (void)b; (void)rt; (void)ct; (void)nt0; (void)ok2; (void)tid; (void)c16; (void)kq; (void)wvo; (void)x1_off; (void)hb_e; (void)hb_l; (void)s10;

  u32 dsk;
  const int ldsb = (int)(u32)(size_t)(LAS char*)smem, ldsw = ldsb + w_ * 1024;
  {
    constexpr int layer = 0;
    ROLES(fresh_lane())
    const int d0 = 2 * b;
    u32x4 xf[4], xt[2], wr[4];
    const bool initw = (b == 0) && (w == 3);
    const int init_off = OFF_INIT + (b & 7) * 256;
    const u32 f0 = __builtin_amdgcn_raw_buffer_load_b32(rs, 0, init_off, SC1);
    if (initw) {
      __builtin_amdgcn_raw_buffer_store_b32(0u, rs, lane * 256, OFF_CA + 6 * 32768, SC1);
      __builtin_amdgcn_raw_buffer_store_b32(0u, rs, lane * 256, OFF_CA + 6 * 32768 + 16384, SC1);
    }
    SB();
    {
      const rsrc_t rx = __builtin_amdgcn_make_buffer_rsrc((void*)x0, 0, L * D * 2, 0x00020000);
      const rsrc_t rg = __builtin_amdgcn_make_buffer_rsrc((void*)wg, 0, D * F * 2, 0x00020000);
      const rsrc_t ru = __builtin_amdgcn_make_buffer_rsrc((void*)wu, 0, D * F * 2, 0x00020000);
      const int fvo = (c16 & 7) * (D * 2) + kq * 16, fso = (b * 8 * D + w * 128) * 2;
#pragma unroll
      for (int i = 0; i < 4; ++i) xf[i] = __builtin_amdgcn_raw_buffer_load_b128(rx, fvo + i * 64, fso, 0);
      SB();
      const int gvo = kq * (F * 2) + (c16 < cnt_ ? c16 : 0) * 16, gso = ((rt * 32 + w * 8) * F + start_ * 8) * 2;
      wr[0] = __builtin_amdgcn_raw_buffer_load_b128(rg, gvo, gso, 0);
      wr[1] = __builtin_amdgcn_raw_buffer_load_b128(ru, gvo, gso, 0);
      wr[2] = __builtin_amdgcn_raw_buffer_load_b128(rg, gvo, gso + 4 * F * 2, 0);
      wr[3] = __builtin_amdgcn_raw_buffer_load_b128(ru, gvo, gso + 4 * F * 2, 0);
      SB();
      const int tvo = (lane >> 1) * (D * 2) + (lane & 1) * 16, tso = (rt2 * 64 * D + ct * 64 + w * 16) * 2;
      xt[0] = __builtin_amdgcn_raw_buffer_load_b128(rx, tvo, tso, 0);
      xt[1] = __builtin_amdgcn_raw_buffer_load_b128(rx, tvo, tso + 32 * D * 2, 0);
    }
    SB();
    STAMP();
    {
      u32x4 z = splat4(0u);
      if (tid < 16) *(u32x4*)(smem + (tid >> 1) * KCOPY + (tid & 1) * 16) = z;
      if (tid >= 64 && tid < 128) *(u32x4*)(smem + LD_U + (lane >> 5) * UCH + (lane & 31) * 16) = z;
      if (tid == 128) *(u32x4*)(smem + LD_KT + 760 * 16) = z;
    }
    int ix[12];
#pragma unroll
    for (int i = 0; i < 12; i += 2) {
      const int l0 = tid * 8 - 4 + i;
      const u32 bits = pk_bf16((float)l0, (float)(l0 + 1));
      const int i0 = l0 < 256 ? (l0 < 0 ? 676 : l0) : (int)(bits & 0xffffu) - 17024;
      const int i1 = l0 < 256 ? (l0 < 0 ? 676 : l0 + 1) : (int)(bits >> 16) - 17024;
      ix[i] = ldsb + LD_KT + (i0 + (i0 >> 3)) * 16; ix[i + 1] = ldsb + LD_KT + (i1 + (i1 >> 3)) * 16;
      asm volatile("" : "+v"(ix[i]), "+v"(ix[i + 1]));
    }
    SB();
    if (initw) { asm volatile("s_waitcnt vmcnt(10)" ::: "memory"); init_bump(rs); }
    const u32 pn = b * (2 * NS) + tid;
    const u32 p_ar = Are[pn], p_ai = Aim[pn], p_br = Bre[pn], p_bi = Bim[pn];
    SB();
    const int ktw = ldsb + LD_KT + (w & 1) * 8 + (w >> 1) * 4;
    int kx[2][2][4];
#pragma unroll
    for (int c = 0; c < 2; ++c) {
      const int B = 16 * c + c16;
#pragma unroll
      for (int i = 0; i < 4; ++i) {
        const int i0 = kq < 2 ? 8 * B + 4 * kq + i : kq == 2 ? 256 + 4 * B + i : 384 + (i >> 1) * 64 + 2 * B + (i & 1);
        const int i1 = kq == 0 ? 512 + 32 * i + B : (kq == 1 && i == 0) ? 640 + B : 680;
        kx[0][c][i] = ktw + (i0 + (i0 >> 3)) * 16; kx[1][c][i] = ktw + (i1 + (i1 >> 3)) * 16;
        asm volatile("" : "+v"(kx[0][c][i]), "+v"(kx[1][c][i]));
      }
    }
    SB();
    const u32 p_cr = Cre[pn], p_ci = Cim[pn];
    const u32 p_ld = logdt[(u32)(d0 + (tid >> 7))];
    dsk = ((const u32*)dskip)[(u32)(b + (lane >> 6))];
    const int chunk = b + 256 * (w ^ 2);
    u32x4 w2r[4];
    SB();
    STAMP();
    {
      const u32x4 ones = splat4(0x3f803f80u);
      f32x4 sa, qa;
      static_for<4>([&](auto ic) { mfma_v<IC(ic) == 0>(sa, ones, xf[IC(ic)]); mfma_v<IC(ic) == 0>(qa, xf[IC(ic)], xf[IC(ic)]); });
      asm volatile("s_nop 7" : "+v"(sa), "+v"(qa));
      if ((c16 >> 2) == kq && c16 < 8) {
        const int r = c16 & 3;
        float q = r == 0 ? qa[0] : r == 1 ? qa[1] : r == 2 ? qa[2] : qa[3];
        *(f32x2*)(smem + LD_SS + (w * 8 + c16) * 8) = (f32x2){sa[0], q};
      }
    }
    SB();
    constexpr int WROW = 368;
    if (c16 < 11) {
      char* wdst = smem + LD_XT + (w * 8 + kq) * WROW + c16 * 32;
#pragma unroll
      for (int i = 0; i < 2; ++i) {
        const u32x4 g = wr[2 * i], u = wr[2 * i + 1];
        *(u32x4*)(wdst + i * 4 * WROW) = (u32x4){g[0], u[0], g[1], u[1]};
        *(u32x4*)(wdst + i * 4 * WROW + 16) = (u32x4){g[2], u[2], g[3], u[3]};
      }
    }
    bar();
    asm volatile("" :: "v"(cpf));
    if (initw) init_bump(rs);
    const int so01 = OFF_WGU + nt0 * KS1 * 1024, so2 = ok2 ? so01 + 2 * KS1 * 1024 : w == 3 ? OFF_WGU + NT1 * KS1 * 1024 : WS_END;
    {
      const char* wsrc = smem + LD_XT + (8 * kq + (c16 >> 2)) * WROW + nbase_ * 32 + (c16 & 3) * 8;
      u32x4 f0 = tr_frag(wsrc, 0, 4 * WROW), f1 = tr_frag(wsrc, 32, 32 + 4 * WROW), f2 = tr_frag(wsrc, 64, 64 + 4 * WROW);
      if (tid < 8) {
        const f32x2* sp = (const f32x2*)(smem + LD_SS) + tid;
        f32x2 v = (sp[0] + sp[8]) + (sp[16] + sp[24]);
        __builtin_amdgcn_raw_buffer_store_b64(__builtin_bit_cast(u32x2, v), rs, tid * 8, OFF_ST0 + b * 64, SC1);
      }
      st16(rs, wvo, so01 + rt * 1024, f0);
      st16(rs, wvo, so01 + (KS1 + rt) * 1024, f1);
      if (w == 2) {
        const int ln = b * 4 + lane - 8;
        st16(rs, lane < 8 ? OFF_AC + b * 128 + wvo : (lane < 12 && ln < 832) ? (ln < 768 ? ln : ln + 128) * 256 : OOB, 0, splat4(0u));
      }
      const u32x4 fillp = splat4(~0u);
      st16(rs, tid * 16, OFF_XT + ACT2 + b * 4 * LP, fillp);
      st16(rs, tid * 16, OFF_XT + ACT2 + b * 4 * LP + LP * 2, fillp);
      if (w == 3) f2 = splat4(0x3f803f80u);
      if (w != 3 || cb_ == 15) st16(rs, wvo, so2 + rt * 1024, f2);
    }
    SB();
    {
      char* img = smem + LD_Y + w * YW;
      *(u32x4*)(img + wvo) = xt[0];
      *(u32x4*)(img + 1024 + wvo) = xt[1];
      const char* tp = img + (kq * 8 + (c16 >> 2)) * 32 + (c16 & 3) * 8;
      u32x4 t0 = tr_frag(tp, 0, 128), t1 = tr_frag(tp, 1024, 1024 + 128);
      const int so = OFF_XT + ((ct * 64 + w * 16) * LP + rt2 * 64) * 2;
      st16(rs, c16 * (LP * 2) + kq * 16, so, t0);
      st16(rs, c16 * (LP * 2) + kq * 16 + 64, so, t1);
    }
    SB();
    STAMP();
    PrepRegs<1> pe, pl;
    u32 pv, pv2;
    const u32 f1 = __builtin_amdgcn_raw_buffer_load_b32(rs, 0, init_off, SC1);
    SB();
    {
      constexpr int TROW = 272;
      char* tab = smem + LD_XT + 32768 - 4096 + w * (32 * TROW);
      char* tar = smem + LD_Y + w * YW;
      char* tar2 = smem + LD_W2 + w * 4096;
      const float dt = rbf(expf(lo_f(p_ld)));
      const float ar = dt * lo_f(p_ar), ai = dt * lo_f(p_ai), br = dt * lo_f(p_br), bi = dt * lo_f(p_bi);
      const float cr = lo_f(p_cr), ci = lo_f(p_ci);
      const float I2PI_HI = 0.15915494309189535f;
      const float I2PI_LO = (float)(0.15915494309189535 - (double)0.15915494309189535f);
      const float a2 = ar * 1.4426950408889634f;
      const float rh = ai * I2PI_HI;
      const float rl = __builtin_fmaf(ai, I2PI_LO, __builtin_fmaf(ai, I2PI_HI, -rh));
      auto cexp = [&](float l) {
        const float e = __builtin_amdgcn_exp2f(a2 * l);
        const float fr = __builtin_fmaf(l, rl, __builtin_amdgcn_fractf(rh * l));
        return (f32x2){__builtin_amdgcn_cosf(fr), __builtin_amdgcn_sinf(fr)} * e;
      };
      const float kr = cr * br - ci * bi, ki = cr * bi + ci * br;
      u32 mx = max(__float_as_uint(kr) & 0x7f800000u, __float_as_uint(ki) & 0x7f800000u);
      mx = max(mx, (u32)__builtin_amdgcn_update_dpp(0, (int)mx, 0xB1, 0xf, 0xf, true));
      mx = max(mx, (u32)__builtin_amdgcn_update_dpp(0, (int)mx, 0x4E, 0xf, 0xf, true));
      mx = max(mx, (u32)__builtin_amdgcn_update_dpp(0, (int)mx, 0x124, 0xf, 0xf, true));
      mx = max(mx, (u32)__builtin_amdgcn_update_dpp(0, (int)mx, 0x128, 0xf, 0xf, true));
      mx = max(max((u32)__builtin_amdgcn_readlane((int)mx, 0), (u32)__builtin_amdgcn_readlane((int)mx, 16)),
               max((u32)__builtin_amdgcn_readlane((int)mx, 32), (u32)__builtin_amdgcn_readlane((int)mx, 48)));
      mx = min(max(mx, 60u << 23), 200u << 23);
      const float up = __uint_as_float((267u << 23) - mx);
      const float kscale = __uint_as_float(mx - (27u << 23));
      const f32x2 e1 = cexp(1.f), e8 = cexp(8.f), e128 = cexp(128.f), e256 = cexp(256.f), e1024 = cexp(1024.f);
      const CM c8 = mkcm((f32x2){e8[0], -e8[1]}), c128 = mkcm((f32x2){e128[0], -e128[1]}), m1 = mkcm(e1), m256 = mkcm(e256);
      u32 hv;
      auto putr = [&](int r, u32 v) { *(u32*)((r < 16 ? tar + r * TROW : tar2 + (r - 16) * TROW) + lane * 4) = v; };
      auto putc = [&](int c, u32 v) { *(u32*)(tab + c * TROW + lane * 4) = v; };
      f32x2 p[8];
      p[0] = (f32x2){16384.f, 0.f}; putr(0, pk_f16(p[0]));
      p[1] = e1 * 16384.f; putr(1, pk_f16(p[1]));
#pragma unroll
      for (int i = 2; i < 8; ++i) { p[i] = cmul_h(p[i - 1], m1, hv); putr(i, hv); }
      f32x2 q0 = e256 * 16384.f; putr(8, pk_f16(q0));
      (void)cmul_h(p[2], m256, hv); putr(9, hv);
      f32x2 q4 = cmul_h(p[4], m256, hv); putr(10, hv);
      (void)cmul_h(p[6], m256, hv); putr(11, hv);
      q0 = cmul_h(q0, m256, hv); putr(12, hv);
      q4 = cmul_h(q4, m256, hv); putr(13, hv);
      (void)cmul_h(q0, m256, hv); putr(14, hv);
      (void)cmul_h(q4, m256, hv); putr(15, hv);
      q0 = e1024 * 16384.f; putr(16, pk_f16(q0));
#pragma unroll
      for (int i = 17; i < 21; ++i) { q0 = cmul_h(q0, m256, hv); putr(i, hv); }
      SB();
      STAMP();
      if (initw) init_bump(rs);
      init_wait(rs, init_off, f0, f1);
      drain_bar<0, false>();
      signal_all(rs, 6, b, w);
    {
      const rsrc_t rwd = __builtin_amdgcn_make_buffer_rsrc((void*)wd, 0, F * D * 2, 0x00020000);
#pragma unroll
      for (int i = 0; i < 4; ++i)
        w2r[i] = __builtin_amdgcn_raw_buffer_load_b128(rwd, (lane >> 3) * (D * 2) + (lane & 7) * 16, ((chunk >> 3) * 32 * D + (chunk & 7) * 64 + i * 8 * D) * 2, 0);
    }
      SB();
      STAMP();
      u32x4 fa[4][2];
      {
        const char* ra0 = tar + c16 * TROW + kq * 16;
        const char* ra1 = tar2 + (c16 < 5 ? c16 : 0) * TROW + kq * 16;
#pragma unroll
        for (int s = 0; s < 4; ++s) { fa[s][0] = *(const u32x4*)(ra0 + s * 64); fa[s][1] = *(const u32x4*)(ra1 + s * 64); }
      }
      SB();
      f32x2 v0 = (f32x2){kr, -ki} * up; putc(0, pk_f16(v0));
      f32x2 v1 = cmul_h(v0, c128, hv); putc(16, hv);
#pragma unroll
      for (int i = 1; i < 16; ++i) {
        v0 = cmul_h(v0, c8, hv); putc(i, hv);
        v1 = cmul_h(v1, c8, hv); putc(16 + i, hv);
      }
      f32x4 ka[2][2];
      const char* rb0 = tab + c16 * TROW + kq * 16;
      u32x4 fb[4][2];
#pragma unroll
      for (int s = 0; s < 4; ++s) { fb[s][0] = *(const u32x4*)(rb0 + s * 64); fb[s][1] = *(const u32x4*)(rb0 + 16 * TROW + s * 64); }
      SB();
      static_for<4>([&](auto sc) {
        constexpr int s = IC(sc);
        mfma_h<s == 0>(ka[0][0], fa[s][0], fb[s][0]); mfma_h<s == 0>(ka[0][1], fa[s][0], fb[s][1]);
        mfma_h<s == 0>(ka[1][0], fa[s][1], fb[s][0]); mfma_h<s == 0>(ka[1][1], fa[s][1], fb[s][1]);
      });
      pv = poll_ld(rs, all_off(6, b), true, w);
      mfma_done4(ka[0][0], ka[0][1], ka[1][0], ka[1][1]);
#pragma unroll
      for (int r = 0; r < 2; ++r)
#pragma unroll
        for (int c = 0; c < 2; ++c) {
          const f32x4 v = ka[r][c] * kscale;
#pragma unroll
          for (int i = 0; i < 4; ++i) *(LAS float*)(size_t)(u32)kx[r][c][i] = v[i];
        }
    }
    bar();
    STAMP();
    {
      f32x4 tv[12];
#pragma unroll
      for (int i = 0; i < 12; ++i) tv[i] = *(const LAS f32x4*)(size_t)(u32)ix[i];
      SB();
      u32 e[2][6];
#pragma unroll
      for (int i = 0; i < 6; ++i) {
        const f32x2 a = tv[2 * i].xy + tv[2 * i].zw, c = tv[2 * i + 1].xy + tv[2 * i + 1].zw;
        e[0][i] = pk_bf16(a[0], c[0]);
        e[1][i] = pk_bf16(a[1], c[1]);
      }
#pragma unroll
      for (int c = 0; c < 2; ++c) {
        char* kb = smem + c * KCH + tid * 16 + 32;
        u32 o[5];
#pragma unroll
        for (int i = 0; i < 5; ++i) o[i] = __builtin_amdgcn_alignbit(e[c][i + 1], e[c][i], 16);
        *(u32x4*)(kb) = (u32x4){e[c][2], e[c][3], e[c][4], e[c][5]};
        *(u32x4*)(kb + KCOPY) = (u32x4){o[1], o[2], o[3], o[4]};
        *(u32x4*)(kb + 2 * KCOPY) = (u32x4){e[c][1], e[c][2], e[c][3], e[c][4]};
        *(u32x4*)(kb + 3 * KCOPY) = (u32x4){o[0], o[1], o[2], o[3]};
      }
    }
    SB();
    if (chunk < KS2P * 8) {
      char* img = smem + LD_W2 + 16384 + w * 4096;
#pragma unroll
      for (int i = 0; i < 4; ++i)
        *(u32x4*)(img + i * 1024 + (lane >> 3) * 128 + ((((lane & 7) >> 1) ^ (((lane >> 4) & 1) | ((i & 1) << 1))) * 32) + (lane & 1) * 16) = w2r[i];
      const int fr = ((c16 >> 3) & 1) | ((kq & 1) << 1);
#pragma unroll
      for (int i = 0; i < 4; ++i)
        st16(rs, wvo, OFF_WD + (((chunk & 7) * 4 + i) * KS2P + (chunk >> 3)) * 1024,
             tr_frag(img + (8 * kq + (c16 >> 2)) * 128 + ((i ^ fr) * 32) + (c16 & 3) * 8, 0, 512));
    }
    SB();
    STAMP();
    if (initw) {
#pragma unroll 1
      for (int spins = 0; spins < SPIN_CAP && !__all(pv >= 64u); ++spins) { init_bump(rs); pv = poll_ld(rs, all_off(6, b), true, w); }
    }
    poll_wait(rs, all_off(6, b), true, pv, w);
    prep_load<1>(rs, tid, OFF_XT + d0 * LP * 2, OFF_ST0, pe);
    prep_load<1>(rs, tid, OFF_XT + d0 * LP * 2 + 2048, OFF_ST0 + 8192, pl);
    STAMP();
    f32x4 ze[2], zl[2];
    prep_ln(smem, 0, tid, dsk, pe, ze);
    asm volatile("" : "+v"(pl.pa[0]), "+v"(pl.pb[0]), "+v"(pl.xv[0]), "+v"(pl.xv[1]));
    SB();
    static_for<4>([&](auto ic) { ld_w1<IC(ic)>(rs, wvo, so01, so2); });
    bar();
    STAMP();
    f32x4 acc[4];
    conv_mfma<0, 0, 3, true>(smem, w, lane, acc, [&](auto nc) {
      constexpr int n = IC(nc);
      if constexpr (n % 3 == 0 && n < 36) ld_w1<4 + n / 3>(rs, wvo, so01, so2);
    });
    conv_out(rs, smem, OFF_X1 + d0 * LP * 2, w, lane, acc, ze);
    STAMP();
    static_for<4>([&](auto ic) { ld_w1<16 + IC(ic)>(rs, wvo, so01, so2); });
    prep_ln(smem, 1, tid, dsk, pl, zl);
    SB();
    static_for<4>([&](auto ic) { ld_w1<20 + IC(ic)>(rs, wvo, so01, so2); });
    drain_bar<8, true>();
    signal_all(rs, 0, b, w);
    conv_mfma<4, 0, 7, true>(smem, w, lane, acc, [&](auto nc) {
      constexpr int n = IC(nc);
      if constexpr (n % 4 == 0 && n < 96) ld_w1<24 + n / 4>(rs, wvo, so01, so2);
    });
    STAMP();
    pv = poll_ld(rs, all_off(0, b), true, w);
    conv_out(rs, smem, OFF_X1 + d0 * LP * 2 + 2048, w, lane, acc, zl);
    pv2 = poll_ld(rs, all_off(0, b), true, w);
    STAMP();
    SB();
    asm volatile("s_waitcnt vmcnt(3)" ::: "memory");
    poll_wait2(rs, all_off(0, b), true, pv, pv2, w);
    STAMP();
    {
      const int gx = xs_voff(w, lane), gs = OFF_X1 + rt * 128;
      const u32x4 nones = splat4(0xbf80bf80u);
      f32x4 c[3];
      static_for<48>([&](auto ic) {
        mfma_aA<4 * IC(ic), (IC(ic) < 3)>(c[IC(ic) % 3], nones); SB();
        if constexpr (IC(ic) < 16 && !(IC(ic) & 1)) { xs_dma<IC(ic) / 2>(rs, gx, gs, ldsw); SB(); }
      });
      asm volatile("s_nop 7" : "+v"(c[0]), "+v"(c[1]), "+v"(c[2]));
#pragma unroll
      for (int i = 0; i < 3; ++i) *(f32x4*)(smem + LD_CS + w * 192 + i * 64 + kq * 16) = c[i];
    }
    drain_bar<5, true>();
    signal_all(rs, 1, b, w);
    STAMP();
  }
  const int lane_g = fresh_lane();
  static_for<2>([&](auto lc) {
    constexpr int layer = IC(lc);
    AccRegs pr;
    bool rdy = false;
    u32x4 W2[5][4];
    u32x4 hf[11][2];
    {
      ROLES(lane_g)
      int sw2[4];
#pragma unroll
      for (int i = 0; i < 4; ++i) sw2[i] = OFF_WD + ((ct * 4 + ((w + i) & 3)) * KS2P + w * 11) * 1024;
      const int fr = ((c16 >> 3) & 1) | ((kq & 1) << 1);
      const char* xr[4];
#pragma unroll
      for (int j = 0; j < 4; ++j) xr[j] = smem + LD_XT + (8 * kq + (c16 >> 2)) * 128 + ((((w + j) & 3) ^ fr) * 32) + (c16 & 3) * 8;
      const int gx = xs_voff(w, lane), gs = x1_off + rt * 128;
      u32x4 xs[8];
      char* ssq = smem + ((c16 >> 2) == kq ? LD_SS + w * 128 : LD_Y) + c16 * 8 + 4;
      char* sss = smem + (w == 3 ? LD_SS : LD_Y) + c16 * 8;
      u32x2 xb[2][4][2];
#pragma unroll
      for (int j = 0; j < 4; ++j) { xb[0][j][0] = tr_half(xr[j]); xb[0][j][1] = tr_half(xr[j] + 512); }
      SB();
      static_for<2>([&](auto hc) {
        constexpr int h = IC(hc);
        f32x4 ga[3][4], qa;
        const int nxt_poll = h == 0 ? all_off(layer * 4 + 1, b) : grp_off(layer * 32 + (rt2 >> 1));
        u32 pv, pv2;
        {
          auto fill = [&](auto gc) {
            constexpr int g = IC(gc);
            if constexpr (g < 8) xs_dma<8 + g>(rs, gx, gs + h * 2048, ldsw);
            if constexpr (g == (h == 0 ? 7 : SIG_HE)) {
              drain_bar<8, false>();
              if constexpr (h == 1) signal_grp(rs, layer * 32 + rt, w); else if constexpr (layer == 1) signal_all(rs, layer * 4 + 1, b, w);
            }
            if constexpr (g == 27) drain_bar<(h == 0 && layer == 0) ? 16 : (h == 1 ? 7 : 0), true>();
            if constexpr (h == 0 && layer == 0) {
              if constexpr (g >= 10 && g < 26) ld_agpr_imm<192 + 16 * ((g - 10) & 3) + 4 * ((g - 10) >> 2), ((g - 10) & 3) * 1024>(rs, wvo, sw2[(g - 10) >> 2]);
              if constexpr (g >= 28 && g < 36) xs[g - 28] = ld16(rs, wvo + ((g - 28) >> 2) * 1024, sw2[(g - 28) & 3] + 4096);
              if constexpr (g >= 43 && g < 51) *(u32x4*)(smem + LD_W2 + w * 8192 + (g - 43) * 1024 + wvo) = xs[g - 43];
            }
            if constexpr (h == 1) {
              if constexpr (g >= 14 && g < 47 && (g > 27 || !(g & 1)) && (g < 34 || !(g & 1))) {
                constexpr int n = g < 28 ? (g - 14) / 2 : g < 34 ? g - 21 : 13 + (g - 34) / 2, i = n / 5, s = n % 5;
                W2[s][i] = ld16(rs, wvo + (s & 3) * 1024, sw2[i] + (6 + (s >> 2) * 4) * 1024);
              }
            }
            if constexpr (g == POLL_G - 12) pv = poll_ld(rs, nxt_poll, h == 0, w);
            if constexpr (g == POLL_G - 5) pv2 = poll_ld(rs, nxt_poll, h == 0, w);
            if constexpr (g == POLL_G) poll_wait2(rs, nxt_poll, h == 0, pv, pv2, w);
            if constexpr (g >= POLL_G && g < POLL_G + 8) {
              if constexpr (h == 0) xs_dma<g - POLL_G>(rs, gx, gs + 2048, ldsw);
              else hf[g - POLL_G][0] = ld16(rs, wvo + ((g - POLL_G) & 3) * 1024, hb_e + ((g - POLL_G) >> 2) * 4096);
            }
          };
          static_for<60>([&](auto gc) {
            constexpr int g = IC(gc), ks = g >> 2, j = g & 3;
            const u32x4 x = {xb[ks & 1][j][0][0], xb[ks & 1][j][0][1], xb[ks & 1][j][1][0], xb[ks & 1][j][1][1]};
            mfma_aA<12 * ks, ks == 0>(ga[0][j], x);
            xb[(ks + 1) & 1][j][0] = tr_half(xr[j] + (ks + 1) * 4096);
            SB();
            mfma_aA<12 * ks + 4, ks == 0>(ga[1][j], x);
            xb[(ks + 1) & 1][j][1] = tr_half(xr[j] + (ks + 1) * 4096 + 512);
            SB();
            mfma_aA<12 * ks + 8, ks == 0>(ga[2][j], x);
            SB();
            fill(gc);
            SB();
            if constexpr (j == 0) { mfma_v<ks == 0>(qa, x, x); SB(); }
          });
          u32x4 x[4];
#pragma unroll
          for (int j = 0; j < 4; ++j) x[j] = (u32x4){xb[1][j][0][0], xb[1][j][0][1], xb[1][j][1][0], xb[1][j][1][1]};
          mfma_vv(qa, x[0], x[0]); SB();
#pragma unroll
          for (int j = 0; j < 4; ++j) { mfma_aA<188, false>(ga[2][j], x[j]); SB(); }
#pragma unroll
          for (int j = 0; j < 4; ++j) { mfma_aA<180, false>(ga[0][j], x[j]); SB(); }
          asm volatile("" : "+v"(qa));
          {
            const int r = c16 & 3;
            const float q01 = (r & 1) ? qa[1] : qa[0], q23 = (r & 1) ? qa[3] : qa[2];
            SB();
            mfma_aA<184, false>(ga[1][0], x[0]); SB();
            const float q = (r & 2) ? q23 : q01;
            *(float*)ssq = q;
            SB();
            mfma_aA<184, false>(ga[1][1], x[1]); SB();
            tie4(ga[2][0], ga[2][1], ga[2][2], ga[2][3]);
            *(float*)(sss + 384) = ga[2][0][0];
            *(float*)(sss) = ga[2][1][0];
            SB();
            mfma_aA<184, false>(ga[1][2], x[2]); SB();
            *(float*)(sss + 128) = ga[2][2][0];
            *(float*)(sss + 256) = ga[2][3][0];
            SB();
            mfma_aA<184, false>(ga[1][3], x[3]); SB();
          }
        }
        STAMP();
        f32x4 cs[3];
#pragma unroll
        for (int i = 0; i < 3; ++i) cs[i] = *(const f32x4*)(smem + LD_CS + w * 192 + i * 64 + kq * 16);
        bar();
        f32x2 st[4];
#pragma unroll
        for (int j = 0; j < 4; ++j) st[j] = *(const f32x2*)(smem + LD_SS + (((w + j) & 3) * 16 + c16) * 8);
        tie4(ga[0][0], ga[0][1], ga[0][2], ga[0][3]);
        tie4(ga[1][0], ga[1][1], ga[1][2], ga[1][3]);
        tie4(ga[2][0], ga[2][1], ga[2][2], ga[2][3]);
        SB();
        u32 hd[3][4];
#pragma unroll
        for (int j = 0; j < 4; ++j) {
          const f32x2 mv = ln_mv(st[j]);
          const float nl = __builtin_amdgcn_rsqf(mv[1]) * -1.4426950408889634f;
#pragma unroll
          for (int i = 0; i < 3; ++i) {
            const f32x4 v = __builtin_elementwise_fma(cs[i], (f32x4){mv[0], mv[0], mv[0], mv[0]}, ga[i][j]);
            const f32x2 g = {v[0], v[1]}, u = {v[2], v[3]};
            const f32x2 a = g * nl;
            const f32x2 e = {__builtin_amdgcn_exp2f(a[0]), __builtin_amdgcn_exp2f(a[1])};
            const f32x2 d = __builtin_elementwise_fma(e, (f32x2){mv[1], mv[1]}, (f32x2){mv[1], mv[1]});
            const f32x2 r = {__builtin_amdgcn_rcpf(d[0]), __builtin_amdgcn_rcpf(d[1])};
            hd[i][j] = pk_bf16((g * u) * r);
            if constexpr (h == 1) {
              const int t = j * 3 + i;
              if (t < 3) hf[8 + t][0] = ld16(rs, wvo, hb_e + (t == 2 ? s10 : (8 + t) * 1024));
              else hf[t - 3][1] = ld16(rs, wvo + ((t - 3) & 3) * 1024, hb_e + NT1 * 256 + ((t - 3) >> 2) * 4096);
            }
          }
          if constexpr (h == 1) SB();
        }
        const int hb = (((w + kq) & 3) * NT1 * 16 + c16) * 16, hs = h_off + ((h * 64 + rt * 4) * NT1 + nt0) * 256;
#pragma unroll
        for (int i = 0; i < 3; ++i) {
          auto a02 = __builtin_amdgcn_permlane32_swap(hd[i][0], hd[i][2], false, false);
          auto a13 = __builtin_amdgcn_permlane32_swap(hd[i][1], hd[i][3], false, false);
          auto o01 = __builtin_amdgcn_permlane16_swap(a02[0], a13[0], false, false);
          auto o23 = __builtin_amdgcn_permlane16_swap(a02[1], a13[1], false, false);
          u32x4 v = {o01[0], o01[1], o23[0], o23[1]};
          st16(rs, (i < 2 || ok2) ? hb + i * 256 : OOB, hs, v);
        }
        SB();
        if constexpr (h == 0) {
          drain_bar<3, false>();
#pragma unroll
          for (int j = 0; j < 4; ++j) { xb[0][j][0] = tr_half(xr[j]); xb[0][j][1] = tr_half(xr[j] + 512); }
        }
        SB();
        STAMP();
      });
    }
    {
      ROLES(fresh_lane())
      char* rw[3];
#pragma unroll
      for (int i = 0; i < 3; ++i) rw[i] = smem + LD_XT + (((w + 1 + i) & 3) * 3 + i) * 2048 + wvo;
      const char* rb = smem + LD_XT + w * 6144 + wvo;
      const int rvo = c16 * (LP * 2) + kq * 8;
      const int ovo = c16 * (LP * 2) + (kq & 1) * 32 + (kq >> 1) * 16;
      char* ow = smem + LD_OT + (w * 16 + c16) * 64 + (kq & 1) * 32 + (kq >> 1) * 16;
      const char* ob = smem + LD_OT + (8 * kq + (c16 >> 2)) * 64 + (w & 1) * 32 + (c16 & 3) * 8;
      const char* wlp = smem + LD_W2 + w * 8192 + wvo;
      static_for<2>([&](auto hc) {
        constexpr int h = IC(hc);
        const int tok0 = h * 1024 + rt2 * 32;
        const int eso = ((ct * 64 + w * 16) * LP + tok0) * 2;
        const int hb = h ? hb_l : hb_e;
        f32x4 da[4][2];
        u32x4 wl[2][4];
        u32x2 res[2];
        u32 pv, pv2;
        static_for<88>([&](auto nc) {
          constexpr int n = IC(nc), j = n / 44, s = (n % 44) >> 2, i = (n + 1) & 3;
          if constexpr (s < 4) mfma_aB<192 + 16 * s + 4 * i, s == 0>(da[i][j], hf[s][j]);
          else if constexpr (s < 6) mfma_vv(da[i][j], hf[s][j], wl[s - 4][i]);
          else mfma_vv(da[i][j], hf[s][j], W2[s - 6][i]);
          SB();
          if constexpr (h == 0) {
            if constexpr (n == 3 || n == 7) hf[9 + (n >> 2)][1] = ld16(rs, wvo, hb + NT1 * 256 + (n == 7 ? s10 : 9 * 1024));
            if constexpr (n == 10) res[0] = ld8(rs, rvo, x1_off + eso);
            if constexpr (n == 13) res[1] = ld8(rs, rvo + 32, x1_off + eso);
            if constexpr (n == SIG_HL) { drain_bar<4, false>(); signal_grp(rs, layer * 32 + 16 + rt, w); }
            if constexpr (n == 86) pv = poll_ld(rs, grp_off(layer * 32 + 16 + (rt2 >> 1)), false, w);
          } else {
            if constexpr (n < 33 && n % 3 == 2) hf[n / 3][1] = ld16(rs, wvo + (n / 3 < 8 ? ((n / 3) & 3) * 1024 : 0), hb + NT1 * 256 + (n / 3 < 8 ? (n / 12) * 4096 : n / 3 == 10 ? s10 : (n / 3) * 1024));
            if constexpr (layer == 0 && n == SIG_XE) { drain_bar<(SIG_XE + 1) / 3, false>(); signal_all(rs, 2, b, w); }
            if constexpr (n == 35) res[0] = ld8(rs, rvo, x1_off + eso);
            if constexpr (n == 38) res[1] = ld8(rs, rvo + 32, x1_off + eso);
            if constexpr (layer == 0 && n >= ACC_E && n < ACC_E + 12 && !((n - ACC_E) & 1)) acc_ld1(rs, (n - ACC_E) >> 1, tid, OFF_XT + ACT2 + b * 2 * LP * 2, 0, pr);
          }
          if constexpr (n % 44 >= 6 && n % 44 < 14) wl[(n % 44 - 6) >> 2][(n % 44 - 5) & 3] = *(const u32x4*)(wlp + (((n % 44 - 6) & 4) + ((n % 44 - 5) & 3)) * 1024);
          if constexpr (n == 52 || n == 56 || n == 60) { asm volatile("" : "+v"(da[(n - 48) >> 2][0])); *(f32x4*)(rw[(n - 52) >> 2]) = da[(n - 48) >> 2][0]; }
          SB();
        });
        mfma_done4(da[0][1], da[1][1], da[2][1], da[3][1]);
        SB();
        STAMP();
#pragma unroll
        for (int i = 0; i < 3; ++i) *(f32x4*)(rw[i] + 1024) = da[i + 1][1];
        if constexpr (h == 0) pv2 = poll_ld(rs, grp_off(layer * 32 + 16 + (rt2 >> 1)), false, w);
        bar();
        u32x2 xo[2];
#pragma unroll
        for (int j = 0; j < 2; ++j) {
          const f32x4 rf = {lo_f(res[j][0]), hi_f(res[j][0]), lo_f(res[j][1]), hi_f(res[j][1])};
          const f32x4 v = ((da[0][j] + rf) + *(const f32x4*)(rb + j * 1024)) + (*(const f32x4*)(rb + 2048 + j * 1024) + *(const f32x4*)(rb + 4096 + j * 1024));
          xo[j] = (u32x2){pk_bf16(v[0], v[1]), pk_bf16(v[2], v[3])};
        }
        auto s0 = __builtin_amdgcn_permlane16_swap(xo[0][0], xo[1][0], false, false);
        auto s1 = __builtin_amdgcn_permlane16_swap(xo[0][1], xo[1][1], false, false);
        const u32x4 xv = {s0[0], s1[0], s0[1], s1[1]};
        *(u32x4*)ow = xv;
        if constexpr (layer == 0 && h == 1) { SB(); rdy = acc_ready(pr); SB(); }
        if constexpr (layer == 0) st16(rs, ovo, OFF_XT + ACT2 + eso, xv);
        SB();
        if constexpr (h == 0) {
          poll_wait2(rs, grp_off(layer * 32 + 16 + (rt2 >> 1)), false, pv, pv2, w);
#pragma unroll
          for (int s = 0; s < 6; ++s) hf[s][0] = ld16(rs, wvo + (s & 3) * 1024, hb_l + (s >> 2) * 4096);
        }
        SB();
        bar();
        if constexpr (h == 0) {
#pragma unroll
          for (int s = 6; s < 11; ++s) hf[s][0] = ld16(rs, wvo + (s < 8 ? (s & 3) * 1024 : 0), hb_l + (s < 8 ? 4096 : s == 10 ? s10 : s * 1024));
          SB();
        }
        if constexpr (layer == 0) {
          if (w < 2) {
            u32x4 f0 = tr_frag(ob, 0, 256), f1 = tr_frag(ob, 2048, 2048 + 256);
            const u32x4 ones = splat4(0x3f803f80u);
            f32x4 sa, qa;
            mfma_v0(sa, ones, f0); mfma_v0(qa, f0, f0); mfma_vv(sa, ones, f1); mfma_vv(qa, f1, f1);
            asm volatile("s_nop 7" : "+v"(sa), "+v"(qa));
            const int r = c16 & 3;
            float q = r == 0 ? qa[0] : r == 1 ? qa[1] : r == 2 ? qa[2] : qa[3];
            u32x2 v = {__float_as_uint(sa[0]), __float_as_uint(q)};
            const bool diag = (c16 >> 2) == kq;
            const bool fits = q < 0x1p29f;
            const u32x2 fs = to_fix(fits ? sa[0] * 0x1p30f : 0.f), fq = to_fix(fits ? q * 0x1p26f : 0x1p59f);
            const int avo = diag ? (c16 & 3) * 4096 + kq * 16 : OOB, aso = OFF_AC + h * 16384 + (rt2 * 8 + w * 4) * 16;
            asm volatile("s_nop 4\n\tbuffer_atomic_add_x2 %0, %2, %3, %4 offen sc1\n\tbuffer_atomic_add_x2 %1, %2, %3, %4 offen offset:8 sc1"
                         :: "v"(fs), "v"(fq), "v"(avo), "s"(rs), "s"(aso) : "memory");
            __builtin_amdgcn_raw_buffer_store_b64(v, rs, diag ? c16 * 8 : OOB, OFF_ST1 + (ct * L + tok0 + w * 16) * 8, SC1);
          }
        } else {
          u32x4 f = tr_frag(ob, (w >> 1) * 2048, (w >> 1) * 2048 + 256);
          const rsrc_t ro = __builtin_amdgcn_make_buffer_rsrc(out, 0, L * D * 2, 0x00020000);
          __builtin_amdgcn_raw_buffer_store_b128(f, ro, (c16 * D + kq * 8) * 2, ((tok0 + (w & 1) * 16) * D + ct * 64 + (w >> 1) * 32) * 2, SC1);
        }
        SB();
        STAMP();
      });
    }
    if constexpr (layer == 0) {
      ROLES(fresh_lane())
      const int xo = b * 2 * LP * 2;
      f32x4 z[2], acc[4];
      u32 pv, pv2;
      acc_wait(rs, tid, OFF_XT + ACT2 + xo, 0, all_off(2, b), pr, rdy);
      STAMP();
      acc_ln(rs, smem, 0, tid, dsk, pr, z);
      bar();
      STAMP();
      conv_mfma<0, 0, 3, true>(smem, w, lane, acc, [&](auto nc) { if constexpr (IC(nc) == 30) { drain_bar<0, false>(); signal_all(rs, 3, b, w); } });
      conv_out(rs, smem, OFF_X1 + ACT2 + xo, w, lane, acc, z);
      STAMP();
      conv_mfma<4, 0, 3, true>(smem, w, lane, acc, [&](auto nc) {
        constexpr int n = IC(nc);
        if constexpr (n >= ACC_L && n < ACC_L + 12 && !((n - ACC_L) & 1)) acc_ld1(rs, (n - ACC_L) >> 1, tid, OFF_XT + ACT2 + xo + 2048, 1, pr);
        if constexpr (n == 36) { drain_bar<6, false>(); signal_all(rs, 4, b, w); }
      });
      STAMP();
      acc_wait(rs, tid, OFF_XT + ACT2 + xo + 2048, 1, all_off(3, b), pr, acc_ready(pr));
      acc_ln(rs, smem, 1, tid, dsk, pr, z);
      bar();
      STAMP();
      const int gx = xs_voff(w, lane), gs = OFF_X1 + ACT2 + rt * 128;
      conv_mfma<4, 4, 7, false>(smem, w, lane, acc, [&](auto nc) {
        constexpr int n = IC(nc);
        if constexpr (n == CE_P) pv = poll_ld(rs, all_off(4, b), true, w);
        if constexpr (n == CE_P + 14) pv2 = poll_ld(rs, all_off(4, b), true, w);
      });
      STAMP();
      poll_wait2(rs, all_off(4, b), true, pv, pv2, w);
      static_for<8>([&](auto ic) { xs_dma<IC(ic)>(rs, gx, gs, ldsw); });
      STAMP();
      conv_out(rs, smem, OFF_X1 + ACT2 + xo + 2048, w, lane, acc, z);
      drain_bar<6, false>();
      STAMP();
    }
  });
  STAMPE();
  STAMP_OUT();
  asm volatile(".Lmk_code_end:" ::: "memory");
}

__device__ __attribute__((used, noinline)) void text_end_marker() { asm volatile(".Lmk_text_end:"); }

extern "C" void kernel_launch(void* const* d_in, const int* in_sizes, int n_in, void* d_out, int out_size,
                              void* d_ws, size_t ws_size, hipStream_t stream) {
  char* ws = (char*)d_ws;
  (void)hipFuncSetAttribute((const void*)main_kernel, hipFuncAttributeMaxDynamicSharedMemorySize, LDS_BYTES + LDS_EXTRA);
  main_kernel<<<256, 256, LDS_BYTES + LDS_EXTRA, stream>>>(
      (const u16*)d_in[0], (const u16*)d_in[1], (const u16*)d_in[2], (const u16*)d_in[3], (const u16*)d_in[4],
      (const u16*)d_in[5], (const u16*)d_in[6], (const u16*)d_in[7], (const u16*)d_in[8], (const u16*)d_in[10],
      (const u16*)d_in[11], (const u16*)d_in[12], ws, (u16*)d_out);
}
```

```cpp
#include <hip/hip_runtime.h>
#include <stdint.h>
#include <type_traits>

typedef unsigned short u16;
typedef unsigned int u32;
typedef u32 u32x4 __attribute__((ext_vector_type(4)));
typedef u32 u32x2 __attribute__((ext_vector_type(2)));
typedef float f32x4 __attribute__((ext_vector_type(4)));
typedef float f32x2 __attribute__((ext_vector_type(2)));
typedef short s16x4 __attribute__((ext_vector_type(4)));
typedef __bf16 bf16x2 __attribute__((ext_vector_type(2)));
typedef _Float16 f16x2 __attribute__((ext_vector_type(2)));
#define LAS __attribute__((address_space(3)))
#define GAS __attribute__((address_space(1)))
#define DEV __device__ __forceinline__
#define SB() __builtin_amdgcn_sched_barrier(0)
#define IC(x) decltype(x)::value
typedef __amdgpu_buffer_rsrc_t rsrc_t;

constexpr int L = 2048, D = 512, NS = 128, F = 1376;
constexpr int NT1 = 172;
constexpr int KS1 = 16;
constexpr int KS2P = 44;
constexpr float EPS = 1e-5f;
constexpr int SC1 = 16;
constexpr int NSH = 16;
constexpr int SIG_HE = 12, SIG_HL = 44, SIG_XE = 30, POLL_G = 52, CE_P = 14, ACC_E = 74, ACC_L = 20;

constexpr int XPAD = 64;
constexpr int LP = L + XPAD;
constexpr int ACT = D * LP * 2;
constexpr int HID = F * L * 2;
constexpr int N_ALL = 7;
constexpr int OFF_CA  = 0;
constexpr int OFF_CH  = OFF_CA + N_ALL * 128 * 256;
constexpr int OFF_INIT = OFF_CH + 64 * 256;
constexpr int CTRL_BYTES = OFF_INIT + 8 * 256;
constexpr int OFF_XT  = CTRL_BYTES;
constexpr int ACT2 = ACT, HID2 = HID;
constexpr int OFF_X1  = OFF_XT + ACT + ACT2;
constexpr int OFF_H   = OFF_X1 + ACT + ACT2;
constexpr int OFF_WGU = OFF_H + HID + HID2;
constexpr int OFF_WD  = OFF_WGU + (NT1 + 1) * KS1 * 1024;
constexpr int OFF_ST0 = OFF_WD + 32 * KS2P * 1024;
constexpr int OFF_ST1 = OFF_ST0 + L * 8;
constexpr int OFF_AC  = OFF_ST1 + 8 * L * 8;
constexpr int WS_END  = OFF_AC + L * 16;

DEV float lo_f(u32 v) { return __uint_as_float(v << 16); }
DEV float hi_f(u32 v) { return __uint_as_float(v & 0xffff0000u); }
DEV u32 pk_bf16(float a, float b) {
  f32x2 v = {a, b};
  return __builtin_bit_cast(u32, __builtin_convertvector(v, bf16x2));
}
DEV u32 pk_bf16(f32x2 v) { return __builtin_bit_cast(u32, __builtin_convertvector(v, bf16x2)); }
DEV u32 pk_f16(f32x2 v) { return __builtin_bit_cast(u32, __builtin_convertvector(v, f16x2)); }
DEV f32x2 unpk(u32 v) { return (f32x2){lo_f(v), hi_f(v)}; }
DEV float rbf(float a) { return lo_f(pk_bf16(a, a)); }
DEV f32x2 ln_mv(f32x2 sq) {
  f32x2 t = __builtin_elementwise_fma(sq, (f32x2){1.0f / D, 1.0f / D}, (f32x2){0.f, EPS});
  t[1] = fmaxf(__builtin_fmaf(-t[0], t[0], t[1]), EPS);
  return t;
}
struct CM { f32x2 re, im; };
DEV CM mkcm(f32x2 c) { return CM{(f32x2){c[0], c[0]}, (f32x2){-c[1], c[1]}}; }
DEV f32x2 cmul_h(f32x2 x, const CM& c, u32& h) {
  const f32x2 r = __builtin_elementwise_fma(x.yx, c.im, x * c.re);
  h = pk_f16(r);
  return r;
}

constexpr int KCOPY = 4160, KCH = 4 * KCOPY;
constexpr int LD_U = 2 * KCH;
constexpr int UCH = (256 + L) * 2;
constexpr int LD_XT = LD_U + 2 * UCH;
constexpr int LD_Y = LD_XT + 65536;
constexpr int YW = 5120;
constexpr int LD_SS = LD_Y + 4 * YW;
constexpr int LD_OT = LD_Y;
constexpr int LD_CS = LD_SS + 512;
constexpr int LD_W2 = LD_CS + 768;
constexpr int LDS_BYTES = LD_W2 + 32768;
static_assert(LDS_BYTES <= 163840, "LDS");
constexpr int LD_KT = LD_XT + 16384;

#define ACLOB "a0","a1","a2","a3","a4","a5","a6","a7","a8","a9","a10","a11","a12","a13","a14","a15","a16","a17","a18","a19","a20","a21","a22","a23","a24","a25","a26","a27","a28","a29","a30","a31","a32","a33","a34","a35","a36","a37","a38","a39","a40","a41","a42","a43","a44","a45","a46","a47","a48","a49","a50","a51","a52","a53","a54","a55","a56","a57","a58","a59","a60","a61","a62","a63","a64","a65","a66","a67","a68","a69","a70","a71","a72","a73","a74","a75","a76","a77","a78","a79","a80","a81","a82","a83","a84","a85","a86","a87","a88","a89","a90","a91","a92","a93","a94","a95","a96","a97","a98","a99","a100","a101","a102","a103","a104","a105","a106","a107","a108","a109","a110","a111","a112","a113","a114","a115","a116","a117","a118","a119","a120","a121","a122","a123","a124","a125","a126","a127","a128","a129","a130","a131","a132","a133","a134","a135","a136","a137","a138","a139","a140","a141","a142","a143","a144","a145","a146","a147","a148","a149","a150","a151","a152","a153","a154","a155","a156","a157","a158","a159","a160","a161","a162","a163","a164","a165","a166","a167","a168","a169","a170","a171","a172","a173","a174","a175","a176","a177","a178","a179","a180","a181","a182","a183","a184","a185","a186","a187","a188","a189","a190","a191","a192","a193","a194","a195","a196","a197","a198","a199","a200","a201","a202","a203","a204","a205","a206","a207","a208","a209","a210","a211","a212","a213","a214","a215","a216","a217","a218","a219","a220","a221","a222","a223","a224","a225","a226","a227","a228","a229","a230","a231","a232","a233","a234","a235","a236","a237","a238","a239","a240","a241","a242","a243","a244","a245","a246","a247","a248","a249","a250","a251","a252","a253","a254","a255"
#define MF "v_mfma_f32_16x16x32_bf16 "
DEV void mfma_vv(f32x4& c, u32x4 a, u32x4 b) { asm(MF "%0, %1, %2, %0" : "+v"(c) : "v"(a), "v"(b)); }
DEV void mfma_v0(f32x4& c, u32x4 a, u32x4 b) { asm(MF "%0, %1, %2, 0" : "=&v"(c) : "v"(a), "v"(b)); }
template <bool FIRST> DEV void mfma_v(f32x4& c, u32x4 a, u32x4 b) { if constexpr (FIRST) mfma_v0(c, a, b); else mfma_vv(c, a, b); }
template <int R, bool FIRST> DEV void mfma_aA(f32x4& c, u32x4 b) {
  if constexpr (FIRST) asm(MF "%0, a[%c2:%c3], %1, 0" : "=&v"(c) : "v"(b), "i"(R), "i"(R + 3));
  else asm(MF "%0, a[%c2:%c3], %1, %0" : "+v"(c) : "v"(b), "i"(R), "i"(R + 3));
}
template <int R, bool FIRST> DEV void mfma_aB(f32x4& c, u32x4 a) {
  if constexpr (FIRST) asm(MF "%0, %1, a[%c2:%c3], 0" : "=&v"(c) : "v"(a), "i"(R), "i"(R + 3));
  else asm(MF "%0, %1, a[%c2:%c3], %0" : "+v"(c) : "v"(a), "i"(R), "i"(R + 3));
}
template <bool FIRST> DEV void mfma_h(f32x4& c, u32x4 a, u32x4 b) {
  if constexpr (FIRST) asm("v_mfma_f32_16x16x32_f16 %0, %1, %2, 0" : "=&v"(c) : "v"(a), "v"(b));
  else asm("v_mfma_f32_16x16x32_f16 %0, %1, %2, %0" : "+v"(c) : "v"(a), "v"(b));
}
DEV void mfma_done4(f32x4& a, f32x4& b, f32x4& c, f32x4& d) { asm volatile("s_nop 7" : "+v"(a), "+v"(b), "+v"(c), "+v"(d)); }
DEV void tie4(f32x4& a, f32x4& b, f32x4& c, f32x4& d) { asm volatile("" : "+v"(a), "+v"(b), "+v"(c), "+v"(d)); }
DEV void bar() { asm volatile("s_waitcnt lgkmcnt(0)\n\ts_barrier" ::: "memory", ACLOB); }

DEV u32x4 ld16(rsrc_t rs, int voff, int soff) { return __builtin_amdgcn_raw_buffer_load_b128(rs, voff, soff, SC1); }
DEV u32x2 ld8(rsrc_t rs, int voff, int soff) { return __builtin_amdgcn_raw_buffer_load_b64(rs, voff, soff, SC1); }
DEV void st16(rsrc_t rs, int voff, int soff, u32x4 v) { __builtin_amdgcn_raw_buffer_store_b128(v, rs, voff, soff, SC1); }
DEV int fresh_lane() {
  int l;
  asm volatile("v_mbcnt_lo_u32_b32 %0, -1, 0\n\tv_mbcnt_hi_u32_b32 %0, -1, %0" : "=v"(l));
  return l;
}
DEV u32x4 splat4(u32 c) {
  u32x4 r;
  asm volatile("v_mov_b32 %0, %4\n\tv_mov_b32 %1, %4\n\tv_mov_b32 %2, %4\n\tv_mov_b32 %3, %4"
               : "=v"(r[0]), "=v"(r[1]), "=v"(r[2]), "=v"(r[3]) : "i"(c));
  asm volatile("s_nop 1" : "+v"(r));
  return r;
}
DEV u32x2 tr_half(const char* p) { return __builtin_bit_cast(u32x2, __builtin_amdgcn_ds_read_tr16_b64_v4i16((LAS s16x4*)p)); }
DEV u32x4 tr_frag(const char* p, int o0, int o1) {
  u32x2 a = tr_half(p + o0), c = tr_half(p + o1);
  return (u32x4){a[0], a[1], c[0], c[1]};
}
template <int N, typename FN, int I = 0>
DEV void static_for(FN&& fn) {
  if constexpr (I < N) { fn(std::integral_constant<int, I>{}); static_for<N, FN, I + 1>(static_cast<FN&&>(fn)); }
}

constexpr int OOB = 0x7ffffff0;
constexpr int SPIN_CAP = 400000;
constexpr int ACC_SLEEP = 2;
DEV int all_off(int edge, int b) { return OFF_CA + (edge * 8 + (b & 7)) * 4096; }
DEV int grp_off(int idx) { return OFF_CH + idx * 256; }
DEV u32 poll_ld(rsrc_t rs, int soff, bool all, int w) {
  const int voff = (all ? (fresh_lane() & (NSH - 1)) * 256 : 0) | (w == 3 ? 0 : 0x7ff00000);
  return __builtin_amdgcn_raw_buffer_load_b32(rs, voff, soff, SC1);
}
DEV void poll_spin(rsrc_t rs, int soff, bool all, u32 f) {
  int spins;
  u32 voff;
  asm volatile("v_cmp_gt_u32 vcc, 64, %0\n\t"
               "s_mov_b32 %1, %c5\n\t"
               "s_nop 3\n\t"
               "s_cbranch_vccz 2f\n\t"
               "v_mbcnt_lo_u32_b32 %2, -1, 0\n\t"
               "v_and_b32 %2, %c6, %2\n\t"
               "v_lshlrev_b32 %2, 8, %2\n"
               "1:\n\t"
               "buffer_load_dword %0, %2, %3, %4 offen sc1\n\t"
               "s_sub_u32 %1, %1, 1\n\t"
               "s_waitcnt vmcnt(0)\n\t"
               "s_cbranch_scc1 2f\n\t"
               "v_cmp_gt_u32 vcc, 64, %0\n\t"
               "s_nop 4\n\t"
               "s_cbranch_vccnz 1b\n"
               "2:"
               : "+v"(f), "=&s"(spins), "=&v"(voff) : "s"(rs), "s"(soff), "i"(SPIN_CAP), "i"(all ? NSH - 1 : 0) : "memory", "vcc", "scc");
}
DEV void poll_wait(rsrc_t rs, int soff, bool all, u32 f, int w) {
  if (w == 3) poll_spin(rs, soff, all, f);
  asm volatile("s_barrier" ::: "memory", ACLOB);
}
DEV void poll_wait2(rsrc_t rs, int soff, bool all, u32 f0, u32 f1, int w) {
  if (w == 3) { if (!__all(f0 >= 64u)) poll_spin(rs, soff, all, f1); }
  asm volatile("s_barrier" ::: "memory", ACLOB);
}
template <int YOUNGER, bool LDSW>
DEV void drain_bar() {
  if constexpr (LDSW) asm volatile("s_waitcnt vmcnt(%c0) lgkmcnt(0)\n\ts_barrier" :: "i"(YOUNGER) : "memory", ACLOB);
  else asm volatile("s_waitcnt vmcnt(%c0)\n\ts_barrier" :: "i"(YOUNGER) : "memory", ACLOB);
}
DEV void signal_all(rsrc_t rs, int edge, int b, int w) {
  if (w == 0) {
    const int lane = fresh_lane();
    __builtin_amdgcn_raw_ptr_buffer_atomic_add_i32(NSH / 4, rs, lane < 8 ? lane * 4096 : OOB, OFF_CA + edge * 32768 + (b & (NSH - 1)) * 256, SC1);
  }
}
DEV void signal_grp(rsrc_t rs, int idx, int w) {
  if (w == 0) {
    const int lane = fresh_lane();
    __builtin_amdgcn_raw_ptr_buffer_atomic_add_i32(4, rs, lane == 0 ? 0 : OOB, OFF_CH + idx * 256, SC1);
  }
}

DEV void init_bump(rsrc_t rs) {
  const int lane = fresh_lane();
  __builtin_amdgcn_raw_ptr_buffer_atomic_add_i32(1, rs, lane < 8 ? lane * 256 : OOB, OFF_INIT, SC1);
}
DEV void init_wait(rsrc_t rs, int soff, u32 f0, u32 f) {
  int spins;
  asm volatile("v_cmp_eq_u32 vcc, %2, %0\n\t"
               "s_mov_b32 %1, %c5\n\t"
               "s_nop 3\n\t"
               "s_cbranch_vccz 2f\n"
               "1:\n\t"
               "buffer_load_dword %0, off, %3, %4 sc1\n\t"
               "s_sub_u32 %1, %1, 1\n\t"
               "s_waitcnt vmcnt(0)\n\t"
               "s_cbranch_scc1 2f\n\t"
               "v_cmp_eq_u32 vcc, %2, %0\n\t"
               "s_nop 4\n\t"
               "s_cbranch_vccnz 1b\n"
               "2:"
               : "+v"(f), "=&s"(spins) : "v"(f0), "s"(rs), "s"(soff), "i"(SPIN_CAP) : "memory", "vcc", "scc");
}

template <int P> struct PrepRegs { u32x4 pa[P], pb[P]; u32x2 xv[2]; };
template <int P>
DEV void prep_ld1(rsrc_t rs, int i, int tid, int xso, int sso, PrepRegs<P>& pr) {
  if (i < 2 * P) {
    if (i & 1) pr.pb[i >> 1] = ld16(rs, tid * 32 + 16, sso + (i >> 1) * (L * 8));
    else pr.pa[i >> 1] = ld16(rs, tid * 32, sso + (i >> 1) * (L * 8));
  } else pr.xv[i - 2 * P] = ld8(rs, tid * 8, xso + (i - 2 * P) * (LP * 2));
}
template <int P>
DEV void prep_load(rsrc_t rs, int tid, int xso, int sso, PrepRegs<P>& pr) {
#pragma unroll
  for (int i = 0; i < 2 * P + 2; ++i) prep_ld1<P>(rs, i, tid, xso, sso, pr);
  SB();
}
DEV void ln_apply(char* smem, int h, int tid, u32 dsk, f32x4 s0, f32x4 s1, const u32x2 (&xv)[2], f32x4 (&z)[2]) {
  const f32x2 t0 = ln_mv(s0.xy), t1 = ln_mv(s0.zw), t2 = ln_mv(s1.xy), t3 = ln_mv(s1.zw);
  const f32x2 rr[2] = {{__builtin_amdgcn_rsqf(t0[1]), __builtin_amdgcn_rsqf(t1[1])}, {__builtin_amdgcn_rsqf(t2[1]), __builtin_amdgcn_rsqf(t3[1])}};
  const f32x2 mr[2] = {{-t0[0] * rr[0][0], -t1[0] * rr[0][1]}, {-t2[0] * rr[1][0], -t3[0] * rr[1][1]}};
#pragma unroll
  for (int c = 0; c < 2; ++c) {
    const float dk = c ? hi_f(dsk) : lo_f(dsk);
    u32x2 u;
    f32x2 zz[2];
#pragma unroll
    for (int i = 0; i < 2; ++i) {
      const f32x2 x = unpk(xv[c][i]);
      const f32x2 uf = __builtin_elementwise_fma(x, rr[i], mr[i]);
      u[i] = pk_bf16(uf);
      zz[i] = __builtin_elementwise_fma(uf, (f32x2){dk, dk}, x);
    }
    z[c] = (f32x4){zz[0][0], zz[0][1], zz[1][0], zz[1][1]};
    *(u32x2*)(smem + LD_U + c * UCH + (256 + h * 1024) * 2 + tid * 8) = u;
  }
}
DEV void prep_ln(char* smem, int h, int tid, u32 dsk, const PrepRegs<1>& pr, f32x4 (&z)[2]) {
  ln_apply(smem, h, tid, dsk, __builtin_bit_cast(f32x4, pr.pa[0]), __builtin_bit_cast(f32x4, pr.pb[0]), pr.xv, z);
}
struct AccRegs { u32x4 a[4]; u32x2 xv[2]; };
DEV void acc_ld1(rsrc_t rs, int i, int tid, int xso, int h, AccRegs& r) {
  if (i < 4) r.a[i] = ld16(rs, tid * 16, OFF_AC + h * 16384 + i * 4096);
  else r.xv[i - 4] = ld8(rs, tid * 8, xso + (i - 4) * (LP * 2));
}
DEV u32x2 to_fix(float t) {
  const float hi = __builtin_floorf(t * 0x1p-32f);
  const u32 lo = (u32)fminf(__builtin_fmaf(hi, -0x1p32f, t), 4294967040.f);
  return (u32x2){(lo & ~15u) | 1u, (u32)(int)hi};
}
DEV bool acc_ready(const AccRegs& r) {
  u32 o = 0u, n = ~0u;
#pragma unroll
  for (int i = 0; i < 4; ++i) { o |= r.a[i][0] | r.a[i][2]; n &= r.a[i][0] & r.a[i][2]; }
  const u32 mx = max(max(r.xv[0][0], r.xv[0][1]), max(r.xv[1][0], r.xv[1][1]));
  return __all(((o & 7u) | (~n & 8u)) == 0u && mx != ~0u);
}
DEV void acc_wait(rsrc_t rs, int tid, int xso, int h, int poll_off, AccRegs& r, bool ready) {
  if (!ready) {
#pragma unroll 1
    for (int spins = 0; spins < SPIN_CAP; ++spins) {
      bool done = false;
      if ((spins & 7) == 7) done = __all(__builtin_amdgcn_raw_buffer_load_b32(rs, (tid & (NSH - 1)) * 256, poll_off, SC1) >= 64u);
      asm volatile("" ::: "memory");
#pragma unroll
      for (int i = 0; i < 6; ++i) acc_ld1(rs, i, tid, xso, h, r);
      if (done || acc_ready(r)) break;
      __builtin_amdgcn_s_sleep(ACC_SLEEP);
    }
    __builtin_amdgcn_s_waitcnt(0x0F70);
  }
}
DEV void acc_ln(rsrc_t rs, char* smem, int h, int tid, u32 dsk, const AccRegs& r, f32x4 (&z)[2]) {
  f32x2 st[4];
#pragma unroll
  for (int i = 0; i < 4; ++i) {
    const f32x2 hi = {(float)(int)r.a[i][1], (float)(int)r.a[i][3]}, lo = {(float)r.a[i][0], (float)r.a[i][2]};
    st[i] = __builtin_elementwise_fma(hi, (f32x2){0x1p2f, 0x1p6f}, lo * (f32x2){0x1p-30f, 0x1p-26f});
  }
  f32x4 s0 = {st[0][0], st[0][1], st[1][0], st[1][1]}, s1 = {st[2][0], st[2][1], st[3][0], st[3][1]};
  const u32 mk = (r.a[0][3] | r.a[1][3]) | (r.a[2][3] | r.a[3][3]);
  if (__builtin_expect(__any(mk >= (1u << 27)), 0)) {
    poll_spin(rs, all_off(2 + h, blockIdx.x), true, 0u);
    s0 = s1 = (f32x4){0.f, 0.f, 0.f, 0.f};
#pragma unroll 1
    for (int p = 0; p < 8; ++p) {
      s0 += __builtin_bit_cast(f32x4, ld16(rs, tid * 32, OFF_ST1 + h * 8192 + p * (L * 8)));
      s1 += __builtin_bit_cast(f32x4, ld16(rs, tid * 32 + 16, OFF_ST1 + h * 8192 + p * (L * 8)));
    }
  }
  ln_apply(smem, h, tid, dsk, s0, s1, r.xv, z);
}
template <int M0, int I0, int I1> struct ConvPlan {
  static constexpr int Q0 = (M0 - I1) > 0 ? (M0 - I1) : 0, Q1 = M0 + 3 - I0;
  static constexpr int NQ = Q1 - Q0 + 1, NU = I1 - I0 + 1, NF = NQ + NU;
  static constexpr int count() { int n = 0; for (int i = I0; i <= I1; ++i) for (int m = 0; m < 4; ++m) n += (M0 + m - i >= 0); return n; }
  static constexpr int NM = count();
  static constexpr int im(int t) { int n = 0; for (int i = I0; i <= I1; ++i) for (int m = 0; m < 4; ++m) if (M0 + m - i >= 0) { if (n == t) return i * 4 + m; ++n; } return 0; }
  static constexpr int frag(int f) {
    if (f == 0) return 0;
    if (f <= 4) return -(Q1 - 4 + f + 1);
    int g = f - 5, nlow = NQ - 4;
    if (g < 2 * nlow) return (g & 1) ? -(Q1 - 4 - (g >> 1) + 1) : 1 + (g >> 1);
    return 1 + nlow + (g - 2 * nlow);
  }
};
template <int M0, int I0, int I1, bool FIRST, typename FN>
DEV void conv_mfma(char* smem, int w, int lane, f32x4 (&acc)[4], FN&& fill) {
  using P = ConvPlan<M0, I0, I1>;
  const int c16 = lane & 15, kq = lane >> 4;
  const int cch = w >> 1, wr = w & 1;
  const char* tb = smem + cch * KCH + (c16 >> 2) * KCOPY + (16 + 16 * (1 + 8 * wr) - 8 * kq + 4 * (c16 & 3) - 4) * 2;
  const char* ub = smem + LD_U + cch * UCH + (256 - 16 * (1 + 8 * wr + 6) + 16 * c16 + 8 * kq) * 2;
  u32x4 T[2][P::NQ], U[2][P::NU];
  auto ldf = [&](auto rc, auto fc) {
    constexpr int rr = IC(rc), f = P::frag(IC(fc));
    if constexpr (f >= 0) U[rr & 1][f] = *(const u32x4*)(ub + (3 - rr) * 64 + (I0 + f) * 512);
    else T[rr & 1][-f - 1 - P::Q0] = tr_frag(tb, rr * 64 + (-f - 1) * 512 + 8, rr * 64 + (-f - 1) * 512);
  };
  static_for<P::NF>([&](auto fc) { ldf(std::integral_constant<int, 0>{}, fc); });
  SB();
  static_for<4 * P::NM>([&](auto nc) {
    constexpr int n = IC(nc), rr = n / P::NM, t = n % P::NM, i = P::im(t) >> 2, m = P::im(t) & 3, q = M0 + m - i;
    mfma_v<FIRST && rr == 0 && i == I0>(acc[m], T[rr & 1][q - P::Q0], U[rr & 1][i - I0]);
    if constexpr (rr < 3 && t < P::NF) ldf(std::integral_constant<int, rr + 1>{}, std::integral_constant<int, t>{});
    SB();
    fill(nc);
    SB();
  });
}
DEV void conv_out(rsrc_t rs, char* smem, int xso, int w, int lane, f32x4 (&acc)[4], const f32x4 (&z)[2]) {
  const int tid = w * 64 + lane;
  char* yw = smem + LD_Y + w * YW + (lane & 15) * 80 + (lane >> 4) * 16;
  const char* yb = smem + LD_Y + tid * 16 + (tid >> 2) * 16;
  const int vo = ((tid & 1) * LP + (tid >> 1) * 8) * 2;
  mfma_done4(acc[0], acc[1], acc[2], acc[3]);
#pragma unroll
  for (int m = 0; m < 4; ++m) *(f32x4*)(yw + m * 1280) = acc[m];
  bar();
  u32x2 o[2];
#pragma unroll
  for (int c = 0; c < 2; ++c) {
    const f32x4 y = (*(const f32x4*)(yb + (2 * c) * YW) + z[c]) + *(const f32x4*)(yb + (2 * c + 1) * YW);
    o[c] = (u32x2){pk_bf16(y[0], y[1]), pk_bf16(y[2], y[3])};
  }
  const bool odd = tid & 1;
  u32 s0 = odd ? o[0][0] : o[1][0], s1 = odd ? o[0][1] : o[1][1];
  u32 g0 = __builtin_amdgcn_update_dpp(0u, s0, 0xB1, 0xf, 0xf, true);
  u32 g1 = __builtin_amdgcn_update_dpp(0u, s1, 0xB1, 0xf, 0xf, true);
  u32x4 v = odd ? (u32x4){g0, g1, o[1][0], o[1][1]} : (u32x4){o[0][0], o[0][1], g0, g1};
  st16(rs, vo, xso, v);
  SB();
}

template <int R, int IMM>
DEV void ld_agpr(rsrc_t rs, int voff, int sbase) {
  int tmp;
  asm volatile("s_add_u32 %0, %3, %c6\n\tbuffer_load_dwordx4 a[%c4:%c5], %1, %2, %0 offen sc1"
               : "=&s"(tmp) : "v"(voff), "s"(rs), "s"(sbase), "i"(R), "i"(R + 3), "i"(IMM) : "memory", "scc");
}
template <int I> DEV void ld_w1(rsrc_t rs, int voff, int so01, int so2) {
  constexpr int ks = I / 3, i = I % 3;
  if constexpr (i < 2) ld_agpr<4 * I, (i * KS1 + ks) * 1024>(rs, voff, so01);
  else ld_agpr<4 * I, ks * 1024>(rs, voff, so2);
}
template <int R, int IMM>
DEV void ld_agpr_imm(rsrc_t rs, int voff, int soff) {
  asm volatile("buffer_load_dwordx4 a[%c3:%c4], %0, %1, %2 offen offset:%c5 sc1"
               :: "v"(voff), "s"(rs), "s"(soff), "i"(R), "i"(R + 3), "i"(IMM) : "memory");
}

constexpr int XS_STEP = 32 * LP * 2;
DEV int xs_voff(int w, int lane) {
  const int fsw = ((lane >> 4) & 1) | ((w & 1) << 1);
  return (w * 8 + (lane >> 3)) * (LP * 2) + ((((lane & 7) >> 1) ^ fsw) * 32) + (lane & 1) * 16;
}
template <int KS>
DEV void xs_dma(rsrc_t rs, int voff, int sbase, int ldsw) {
  int tmp;
  asm volatile("s_add_u32 m0, %4, %c5\n\ts_add_u32 %0, %3, %c6\n\tbuffer_load_dwordx4 %1, %2, %0 offen sc1 lds"
               : "=&s"(tmp) : "v"(voff), "s"(rs), "s"(sbase), "s"(ldsw), "i"(LD_XT + KS * 4096), "i"(KS * XS_STEP) : "memory", "scc");
}

__global__ __launch_bounds__(256, 1) void main_kernel(
    const u16* __restrict__ x0, const u16* __restrict__ Are, const u16* __restrict__ Aim,
    const u16* __restrict__ Bre, const u16* __restrict__ Bim, const u16* __restrict__ Cre,
    const u16* __restrict__ Cim, const u16* __restrict__ logdt, const u16* __restrict__ dskip,
    const u16* __restrict__ wg, const u16* __restrict__ wu, const u16* __restrict__ wd,
    char* __restrict__ ws, u16* __restrict__ out) {
  extern __shared__ __attribute__((aligned(16))) char smem[];
  const int w_ = __builtin_amdgcn_readfirstlane(threadIdx.x >> 6);
  const int b_ = blockIdx.x;

  const rsrc_t rs = __builtin_amdgcn_make_buffer_rsrc(ws, 0, WS_END, 0x00020000);
  asm volatile("" ::: ACLOB);
  u32 cpf;
  {
    unsigned long long pc; int len;
    asm volatile("s_getpc_b64 %0\n.Lmk_code_begin:\n\ts_mov_b32 %1, .Lmk_code_end-.Lmk_code_begin\n\t"
                 "s_max_i32 %1, %1, .Lmk_text_end-.Lmk_code_begin" : "=s"(pc), "=s"(len) :: "scc");
    const int off = min((int)((min(threadIdx.x & 63, 4u) * 4 + (threadIdx.x >> 6)) * 32 + (blockIdx.x >> 3)) * 128, len - 4) & ~3;
    cpf = *(const GAS u32*)(pc + (unsigned long long)(u32)off);
    SB();
    asm volatile("" :: "s"(x0), "s"(Are), "s"(Aim), "s"(Bre), "s"(Bim), "s"(Cre), "s"(Cim), "s"(logdt), "s"(dskip), "s"(wg), "s"(wu), "s"(wd), "s"(ws));
  }
  const int rt_ = ((b_ & 7) >> 2) * 8 + ((b_ >> 3) & 7), cb_ = (b_ & 3) * 4 + (b_ >> 6);
  const int rt2_ = (b_ & 7) * 4 + ((b_ >> 3) & 3), ct_ = b_ >> 5;
  const int start_ = cb_ * 11 - (cb_ >> 2), cnt_ = (cb_ & 3) == 3 ? 10 : 11;
  const int nbase_ = (w_ < 3) ? w_ * 3 : cnt_ - 2;
  const int nt0_ = start_ + nbase_;
  const int ok2_ = __builtin_amdgcn_readfirstlane((w_ < 3) && (nbase_ + 2 < cnt_ - 2));
#define ROLES(LANE) \
  const int w = w_, b = b_, rt = rt_, rt2 = rt2_, ct = ct_, nt0 = nt0_, ok2 = ok2_; \
  const int lane = LANE, tid = w * 64 + lane, c16 = lane & 15, kq = lane >> 4, wvo = lane * 16; \
  const int x1_off = OFF_X1 + layer * ACT2, h_off = OFF_H + layer * HID2; \
  const int hb_e = h_off + (rt2 * 2 * NT1 * 16 + w * 11 * 64) * 16, hb_l = hb_e + 64 * NT1 * 256; \
  const int s10 = (w == 3) ? 9 * 1024 : 10 * 1024; \
  (void)b; (void)rt; (void)ct; (void)nt0; (void)ok2; (void)tid; (void)c16; (void)kq; (void)wvo; (void)x1_off; (void)hb_e; (void)hb_l; (void)s10;

  u32 dsk;
  const int ldsb = (int)(u32)(size_t)(LAS char*)smem, ldsw = ldsb + w_ * 1024;
  {
    constexpr int layer = 0;
    ROLES(fresh_lane())
    const int d0 = 2 * b;
    u32x4 xf[4], xt[2], wr[4];
    const bool initw = (b == 0) && (w == 3);
    const int init_off = OFF_INIT + (b & 7) * 256;
    const u32 f0 = __builtin_amdgcn_raw_buffer_load_b32(rs, 0, init_off, SC1);
    if (initw) {
      __builtin_amdgcn_raw_buffer_store_b32(0u, rs, lane * 256, OFF_CA + 6 * 32768, SC1);
      __builtin_amdgcn_raw_buffer_store_b32(0u, rs, lane * 256, OFF_CA + 6 * 32768 + 16384, SC1);
    }
    SB();
    {
      const rsrc_t rx = __builtin_amdgcn_make_buffer_rsrc((void*)x0, 0, L * D * 2, 0x00020000);
      const rsrc_t rg = __builtin_amdgcn_make_buffer_rsrc((void*)wg, 0, D * F * 2, 0x00020000);
      const rsrc_t ru = __builtin_amdgcn_make_buffer_rsrc((void*)wu, 0, D * F * 2, 0x00020000);
      const int fvo = (c16 & 7) * (D * 2) + kq * 16, fso = (b * 8 * D + w * 128) * 2;
#pragma unroll
      for (int i = 0; i < 4; ++i) xf[i] = __builtin_amdgcn_raw_buffer_load_b128(rx, fvo + i * 64, fso, 0);
      SB();
      const int gvo = kq * (F * 2) + (c16 < cnt_ ? c16 : 0) * 16, gso = ((rt * 32 + w * 8) * F + start_ * 8) * 2;
      wr[0] = __builtin_amdgcn_raw_buffer_load_b128(rg, gvo, gso, 0);
      wr[1] = __builtin_amdgcn_raw_buffer_load_b128(ru, gvo, gso, 0);
      wr[2] = __builtin_amdgcn_raw_buffer_load_b128(rg, gvo, gso + 4 * F * 2, 0);
      wr[3] = __builtin_amdgcn_raw_buffer_load_b128(ru, gvo, gso + 4 * F * 2, 0);
      SB();
      const int tvo = (lane >> 1) * (D * 2) + (lane & 1) * 16, tso = (rt2 * 64 * D + ct * 64 + w * 16) * 2;
      xt[0] = __builtin_amdgcn_raw_buffer_load_b128(rx, tvo, tso, 0);
      xt[1] = __builtin_amdgcn_raw_buffer_load_b128(rx, tvo, tso + 32 * D * 2, 0);
    }
    SB();
    {
      u32x4 z = splat4(0u);
      if (tid < 16) *(u32x4*)(smem + (tid >> 1) * KCOPY + (tid & 1) * 16) = z;
      if (tid >= 64 && tid < 128) *(u32x4*)(smem + LD_U + (lane >> 5) * UCH + (lane & 31) * 16) = z;
      if (tid == 128) *(u32x4*)(smem + LD_KT + 760 * 16) = z;
    }
    int ix[12];
#pragma unroll
    for (int i = 0; i < 12; i += 2) {
      const int l0 = tid * 8 - 4 + i;
      const u32 bits = pk_bf16((float)l0, (float)(l0 + 1));
      const int i0 = l0 < 256 ? (l0 < 0 ? 676 : l0) : (int)(bits & 0xffffu) - 17024;
      const int i1 = l0 < 256 ? (l0 < 0 ? 676 : l0 + 1) : (int)(bits >> 16) - 17024;
      ix[i] = ldsb + LD_KT + (i0 + (i0 >> 3)) * 16; ix[i + 1] = ldsb + LD_KT + (i1 + (i1 >> 3)) * 16;
      asm volatile("" : "+v"(ix[i]), "+v"(ix[i + 1]));
    }
    SB();
    if (initw) { asm volatile("s_waitcnt vmcnt(10)" ::: "memory"); init_bump(rs); }
    const u32 pn = b * (2 * NS) + tid;
    const u32 p_ar = Are[pn], p_ai = Aim[pn], p_br = Bre[pn], p_bi = Bim[pn];
    SB();
    const int ktw = ldsb + LD_KT + (w & 1) * 8 + (w >> 1) * 4;
    int kx[2][2][4];
#pragma unroll
    for (int c = 0; c < 2; ++c) {
      const int B = 16 * c + c16;
#pragma unroll
      for (int i = 0; i < 4; ++i) {
        const int i0 = kq < 2 ? 8 * B + 4 * kq + i : kq == 2 ? 256 + 4 * B + i : 384 + (i >> 1) * 64 + 2 * B + (i & 1);
        const int i1 = kq == 0 ? 512 + 32 * i + B : (kq == 1 && i == 0) ? 640 + B : 680;
        kx[0][c][i] = ktw + (i0 + (i0 >> 3)) * 16; kx[1][c][i] = ktw + (i1 + (i1 >> 3)) * 16;
        asm volatile("" : "+v"(kx[0][c][i]), "+v"(kx[1][c][i]));
      }
    }
    SB();
    const u32 p_cr = Cre[pn], p_ci = Cim[pn];
    const u32 p_ld = logdt[(u32)(d0 + (tid >> 7))];
    dsk = ((const u32*)dskip)[(u32)(b + (lane >> 6))];
    const int chunk = b + 256 * (w ^ 2);
    u32x4 w2r[4];
    SB();
    {
      const u32x4 ones = splat4(0x3f803f80u);
      f32x4 sa, qa;
      static_for<4>([&](auto ic) { mfma_v<IC(ic) == 0>(sa, ones, xf[IC(ic)]); mfma_v<IC(ic) == 0>(qa, xf[IC(ic)], xf[IC(ic)]); });
      asm volatile("s_nop 7" : "+v"(sa), "+v"(qa));
      if ((c16 >> 2) == kq && c16 < 8) {
        const int r = c16 & 3;
        float q = r == 0 ? qa[0] : r == 1 ? qa[1] : r == 2 ? qa[2] : qa[3];
        *(f32x2*)(smem + LD_SS + (w * 8 + c16) * 8) = (f32x2){sa[0], q};
      }
    }
    SB();
    constexpr int WROW = 368;
    if (c16 < 11) {
      char* wdst = smem + LD_XT + (w * 8 + kq) * WROW + c16 * 32;
#pragma unroll
      for (int i = 0; i < 2; ++i) {
        const u32x4 g = wr[2 * i], u = wr[2 * i + 1];
        *(u32x4*)(wdst + i * 4 * WROW) = (u32x4){g[0], u[0], g[1], u[1]};
        *(u32x4*)(wdst + i * 4 * WROW + 16) = (u32x4){g[2], u[2], g[3], u[3]};
      }
    }
    bar();
    asm volatile("" :: "v"(cpf));
    if (initw) init_bump(rs);
    const int so01 = OFF_WGU + nt0 * KS1 * 1024, so2 = ok2 ? so01 + 2 * KS1 * 1024 : w == 3 ? OFF_WGU + NT1 * KS1 * 1024 : WS_END;
    {
      const char* wsrc = smem + LD_XT + (8 * kq + (c16 >> 2)) * WROW + nbase_ * 32 + (c16 & 3) * 8;
      u32x4 f0 = tr_frag(wsrc, 0, 4 * WROW), f1 = tr_frag(wsrc, 32, 32 + 4 * WROW), f2 = tr_frag(wsrc, 64, 64 + 4 * WROW);
      if (tid < 8) {
        const f32x2* sp = (const f32x2*)(smem + LD_SS) + tid;
        f32x2 v = (sp[0] + sp[8]) + (sp[16] + sp[24]);
        __builtin_amdgcn_raw_buffer_store_b64(__builtin_bit_cast(u32x2, v), rs, tid * 8, OFF_ST0 + b * 64, SC1);
      }
      st16(rs, wvo, so01 + rt * 1024, f0);
      st16(rs, wvo, so01 + (KS1 + rt) * 1024, f1);
      if (w == 2) {
        const int ln = b * 4 + lane - 8;
        st16(rs, lane < 8 ? OFF_AC + b * 128 + wvo : (lane < 12 && ln < 832) ? (ln < 768 ? ln : ln + 128) * 256 : OOB, 0, splat4(0u));
      }
      const u32x4 fillp = splat4(~0u);
      st16(rs, tid * 16, OFF_XT + ACT2 + b * 4 * LP, fillp);
      st16(rs, tid * 16, OFF_XT + ACT2 + b * 4 * LP + LP * 2, fillp);
      if (w == 3) f2 = splat4(0x3f803f80u);
      if (w != 3 || cb_ == 15) st16(rs, wvo, so2 + rt * 1024, f2);
    }
    SB();
    {
      char* img = smem + LD_Y + w * YW;
      *(u32x4*)(img + wvo) = xt[0];
      *(u32x4*)(img + 1024 + wvo) = xt[1];
      const char* tp = img + (kq * 8 + (c16 >> 2)) * 32 + (c16 & 3) * 8;
      u32x4 t0 = tr_frag(tp, 0, 128), t1 = tr_frag(tp, 1024, 1024 + 128);
      const int so = OFF_XT + ((ct * 64 + w * 16) * LP + rt2 * 64) * 2;
      st16(rs, c16 * (LP * 2) + kq * 16, so, t0);
      st16(rs, c16 * (LP * 2) + kq * 16 + 64, so, t1);
    }
    SB();
    PrepRegs<1> pe, pl;
    u32 pv, pv2;
    const u32 f1 = __builtin_amdgcn_raw_buffer_load_b32(rs, 0, init_off, SC1);
    SB();
    {
      constexpr int TROW = 272;
      char* tab = smem + LD_XT + 32768 - 4096 + w * (32 * TROW);
      char* tar = smem + LD_Y + w * YW;
      char* tar2 = smem + LD_W2 + w * 4096;
      const float dt = rbf(expf(lo_f(p_ld)));
      const float ar = dt * lo_f(p_ar), ai = dt * lo_f(p_ai), br = dt * lo_f(p_br), bi = dt * lo_f(p_bi);
      const float cr = lo_f(p_cr), ci = lo_f(p_ci);
      const float I2PI_HI = 0.15915494309189535f;
      const float I2PI_LO = (float)(0.15915494309189535 - (double)0.15915494309189535f);
      const float a2 = ar * 1.4426950408889634f;
      const float rh = ai * I2PI_HI;
      const float rl = __builtin_fmaf(ai, I2PI_LO, __builtin_fmaf(ai, I2PI_HI, -rh));
      auto cexp = [&](float l) {
        const float e = __builtin_amdgcn_exp2f(a2 * l);
        const float fr = __builtin_fmaf(l, rl, __builtin_amdgcn_fractf(rh * l));
        return (f32x2){__builtin_amdgcn_cosf(fr), __builtin_amdgcn_sinf(fr)} * e;
      };
      const float kr = cr * br - ci * bi, ki = cr * bi + ci * br;
      u32 mx = max(__float_as_uint(kr) & 0x7f800000u, __float_as_uint(ki) & 0x7f800000u);
      mx = max(mx, (u32)__builtin_amdgcn_update_dpp(0, (int)mx, 0xB1, 0xf, 0xf, true));
      mx = max(mx, (u32)__builtin_amdgcn_update_dpp(0, (int)mx, 0x4E, 0xf, 0xf, true));
      mx = max(mx, (u32)__builtin_amdgcn_update_dpp(0, (int)mx, 0x124, 0xf, 0xf, true));
      mx = max(mx, (u32)__builtin_amdgcn_update_dpp(0, (int)mx, 0x128, 0xf, 0xf, true));
      mx = max(max((u32)__builtin_amdgcn_readlane((int)mx, 0), (u32)__builtin_amdgcn_readlane((int)mx, 16)),
               max((u32)__builtin_amdgcn_readlane((int)mx, 32), (u32)__builtin_amdgcn_readlane((int)mx, 48)));
      mx = min(max(mx, 60u << 23), 200u << 23);
      const float up = __uint_as_float((267u << 23) - mx);
      const float kscale = __uint_as_float(mx - (27u << 23));
      const f32x2 e1 = cexp(1.f), e8 = cexp(8.f), e128 = cexp(128.f), e256 = cexp(256.f), e1024 = cexp(1024.f);
      const CM c8 = mkcm((f32x2){e8[0], -e8[1]}), c128 = mkcm((f32x2){e128[0], -e128[1]}), m1 = mkcm(e1), m256 = mkcm(e256);
      u32 hv;
      auto putr = [&](int r, u32 v) { *(u32*)((r < 16 ? tar + r * TROW : tar2 + (r - 16) * TROW) + lane * 4) = v; };
      auto putc = [&](int c, u32 v) { *(u32*)(tab + c * TROW + lane * 4) = v; };
      f32x2 p[8];
      p[0] = (f32x2){16384.f, 0.f}; putr(0, pk_f16(p[0]));
      p[1] = e1 * 16384.f; putr(1, pk_f16(p[1]));
#pragma unroll
      for (int i = 2; i < 8; ++i) { p[i] = cmul_h(p[i - 1], m1, hv); putr(i, hv); }
      f32x2 q0 = e256 * 16384.f; putr(8, pk_f16(q0));
      (void)cmul_h(p[2], m256, hv); putr(9, hv);
      f32x2 q4 = cmul_h(p[4], m256, hv); putr(10, hv);
      (void)cmul_h(p[6], m256, hv); putr(11, hv);
      q0 = cmul_h(q0, m256, hv); putr(12, hv);
      q4 = cmul_h(q4, m256, hv); putr(13, hv);
      (void)cmul_h(q0, m256, hv); putr(14, hv);
      (void)cmul_h(q4, m256, hv); putr(15, hv);
      q0 = e1024 * 16384.f; putr(16, pk_f16(q0));
#pragma unroll
      for (int i = 17; i < 21; ++i) { q0 = cmul_h(q0, m256, hv); putr(i, hv); }
      SB();
      if (initw) init_bump(rs);
      init_wait(rs, init_off, f0, f1);
      drain_bar<0, false>();
      signal_all(rs, 6, b, w);
    {
      const rsrc_t rwd = __builtin_amdgcn_make_buffer_rsrc((void*)wd, 0, F * D * 2, 0x00020000);
#pragma unroll
      for (int i = 0; i < 4; ++i)
        w2r[i] = __builtin_amdgcn_raw_buffer_load_b128(rwd, (lane >> 3) * (D * 2) + (lane & 7) * 16, ((chunk >> 3) * 32 * D + (chunk & 7) * 64 + i * 8 * D) * 2, 0);
    }
      SB();
      u32x4 fa[4][2];
      {
        const char* ra0 = tar + c16 * TROW + kq * 16;
        const char* ra1 = tar2 + (c16 < 5 ? c16 : 0) * TROW + kq * 16;
#pragma unroll
        for (int s = 0; s < 4; ++s) { fa[s][0] = *(const u32x4*)(ra0 + s * 64); fa[s][1] = *(const u32x4*)(ra1 + s * 64); }
      }
      SB();
      f32x2 v0 = (f32x2){kr, -ki} * up; putc(0, pk_f16(v0));
      f32x2 v1 = cmul_h(v0, c128, hv); putc(16, hv);
#pragma unroll
      for (int i = 1; i < 16; ++i) {
        v0 = cmul_h(v0, c8, hv); putc(i, hv);
        v1 = cmul_h(v1, c8, hv); putc(16 + i, hv);
      }
      f32x4 ka[2][2];
      const char* rb0 = tab + c16 * TROW + kq * 16;
      u32x4 fb[4][2];
#pragma unroll
      for (int s = 0; s < 4; ++s) { fb[s][0] = *(const u32x4*)(rb0 + s * 64); fb[s][1] = *(const u32x4*)(rb0 + 16 * TROW + s * 64); }
      SB();
      static_for<4>([&](auto sc) {
        constexpr int s = IC(sc);
        mfma_h<s == 0>(ka[0][0], fa[s][0], fb[s][0]); mfma_h<s == 0>(ka[0][1], fa[s][0], fb[s][1]);
        mfma_h<s == 0>(ka[1][0], fa[s][1], fb[s][0]); mfma_h<s == 0>(ka[1][1], fa[s][1], fb[s][1]);
      });
      pv = poll_ld(rs, all_off(6, b), true, w);
      mfma_done4(ka[0][0], ka[0][1], ka[1][0], ka[1][1]);
#pragma unroll
      for (int r = 0; r < 2; ++r)
#pragma unroll
        for (int c = 0; c < 2; ++c) {
          const f32x4 v = ka[r][c] * kscale;
#pragma unroll
          for (int i = 0; i < 4; ++i) *(LAS float*)(size_t)(u32)kx[r][c][i] = v[i];
        }
    }
    bar();
    {
      f32x4 tv[12];
#pragma unroll
      for (int i = 0; i < 12; ++i) tv[i] = *(const LAS f32x4*)(size_t)(u32)ix[i];
      SB();
      u32 e[2][6];
#pragma unroll
      for (int i = 0; i < 6; ++i) {
        const f32x2 a = tv[2 * i].xy + tv[2 * i].zw, c = tv[2 * i + 1].xy + tv[2 * i + 1].zw;
        e[0][i] = pk_bf16(a[0], c[0]);
        e[1][i] = pk_bf16(a[1], c[1]);
      }
#pragma unroll
      for (int c = 0; c < 2; ++c) {
        char* kb = smem + c * KCH + tid * 16 + 32;
        u32 o[5];
#pragma unroll
        for (int i = 0; i < 5; ++i) o[i] = __builtin_amdgcn_alignbit(e[c][i + 1], e[c][i], 16);
        *(u32x4*)(kb) = (u32x4){e[c][2], e[c][3], e[c][4], e[c][5]};
        *(u32x4*)(kb + KCOPY) = (u32x4){o[1], o[2], o[3], o[4]};
        *(u32x4*)(kb + 2 * KCOPY) = (u32x4){e[c][1], e[c][2], e[c][3], e[c][4]};
        *(u32x4*)(kb + 3 * KCOPY) = (u32x4){o[0], o[1], o[2], o[3]};
      }
    }
    SB();
    if (chunk < KS2P * 8) {
      char* img = smem + LD_W2 + 16384 + w * 4096;
#pragma unroll
      for (int i = 0; i < 4; ++i)
        *(u32x4*)(img + i * 1024 + (lane >> 3) * 128 + ((((lane & 7) >> 1) ^ (((lane >> 4) & 1) | ((i & 1) << 1))) * 32) + (lane & 1) * 16) = w2r[i];
      const int fr = ((c16 >> 3) & 1) | ((kq & 1) << 1);
#pragma unroll
      for (int i = 0; i < 4; ++i)
        st16(rs, wvo, OFF_WD + (((chunk & 7) * 4 + i) * KS2P + (chunk >> 3)) * 1024,
             tr_frag(img + (8 * kq + (c16 >> 2)) * 128 + ((i ^ fr) * 32) + (c16 & 3) * 8, 0, 512));
    }
    SB();
    if (initw) {
#pragma unroll 1
      for (int spins = 0; spins < SPIN_CAP && !__all(pv >= 64u); ++spins) { init_bump(rs); pv = poll_ld(rs, all_off(6, b), true, w); }
    }
    poll_wait(rs, all_off(6, b), true, pv, w);
    prep_load<1>(rs, tid, OFF_XT + d0 * LP * 2, OFF_ST0, pe);
    prep_load<1>(rs, tid, OFF_XT + d0 * LP * 2 + 2048, OFF_ST0 + 8192, pl);
    f32x4 ze[2], zl[2];
    prep_ln(smem, 0, tid, dsk, pe, ze);
    asm volatile("" : "+v"(pl.pa[0]), "+v"(pl.pb[0]), "+v"(pl.xv[0]), "+v"(pl.xv[1]));
    SB();
    static_for<4>([&](auto ic) { ld_w1<IC(ic)>(rs, wvo, so01, so2); });
    bar();
    f32x4 acc[4];
    conv_mfma<0, 0, 3, true>(smem, w, lane, acc, [&](auto nc) {
      constexpr int n = IC(nc);
      if constexpr (n % 3 == 0 && n < 36) ld_w1<4 + n / 3>(rs, wvo, so01, so2);
    });
    conv_out(rs, smem, OFF_X1 + d0 * LP * 2, w, lane, acc, ze);
    static_for<4>([&](auto ic) { ld_w1<16 + IC(ic)>(rs, wvo, so01, so2); });
    prep_ln(smem, 1, tid, dsk, pl, zl);
    SB();
    static_for<4>([&](auto ic) { ld_w1<20 + IC(ic)>(rs, wvo, so01, so2); });
    drain_bar<8, true>();
    signal_all(rs, 0, b, w);
    conv_mfma<4, 0, 7, true>(smem, w, lane, acc, [&](auto nc) {
      constexpr int n = IC(nc);
      if constexpr (n % 4 == 0 && n < 96) ld_w1<24 + n / 4>(rs, wvo, so01, so2);
    });
    pv = poll_ld(rs, all_off(0, b), true, w);
    conv_out(rs, smem, OFF_X1 + d0 * LP * 2 + 2048, w, lane, acc, zl);
    pv2 = poll_ld(rs, all_off(0, b), true, w);
    SB();
    asm volatile("s_waitcnt vmcnt(3)" ::: "memory");
    poll_wait2(rs, all_off(0, b), true, pv, pv2, w);
    {
      const int gx = xs_voff(w, lane), gs = OFF_X1 + rt * 128;
      const u32x4 nones = splat4(0xbf80bf80u);
      f32x4 c[3];
      static_for<48>([&](auto ic) {
        mfma_aA<4 * IC(ic), (IC(ic) < 3)>(c[IC(ic) % 3], nones); SB();
        if constexpr (IC(ic) < 16 && !(IC(ic) & 1)) { xs_dma<IC(ic) / 2>(rs, gx, gs, ldsw); SB(); }
      });
      asm volatile("s_nop 7" : "+v"(c[0]), "+v"(c[1]), "+v"(c[2]));
#pragma unroll
      for (int i = 0; i < 3; ++i) *(f32x4*)(smem + LD_CS + w * 192 + i * 64 + kq * 16) = c[i];
    }
    drain_bar<5, true>();
    signal_all(rs, 1, b, w);
  }
  const int lane_g = fresh_lane();
  static_for<2>([&](auto lc) {
    constexpr int layer = IC(lc);
    AccRegs pr;
    bool rdy = false;
    u32x4 W2[5][4];
    u32x4 hf[11][2];
    {
      ROLES(lane_g)
      int sw2[4];
#pragma unroll
      for (int i = 0; i < 4; ++i) sw2[i] = OFF_WD + ((ct * 4 + ((w + i) & 3)) * KS2P + w * 11) * 1024;
      const int fr = ((c16 >> 3) & 1) | ((kq & 1) << 1);
      const char* xr[4];
#pragma unroll
      for (int j = 0; j < 4; ++j) xr[j] = smem + LD_XT + (8 * kq + (c16 >> 2)) * 128 + ((((w + j) & 3) ^ fr) * 32) + (c16 & 3) * 8;
      const int gx = xs_voff(w, lane), gs = x1_off + rt * 128;
      u32x4 xs[8];
      char* ssq = smem + ((c16 >> 2) == kq ? LD_SS + w * 128 : LD_Y) + c16 * 8 + 4;
      char* sss = smem + (w == 3 ? LD_SS : LD_Y) + c16 * 8;
      u32x2 xb[2][4][2];
#pragma unroll
      for (int j = 0; j < 4; ++j) { xb[0][j][0] = tr_half(xr[j]); xb[0][j][1] = tr_half(xr[j] + 512); }
      SB();
      static_for<2>([&](auto hc) {
        constexpr int h = IC(hc);
        f32x4 ga[3][4], qa;
        const int nxt_poll = h == 0 ? all_off(layer * 4 + 1, b) : grp_off(layer * 32 + (rt2 >> 1));
        u32 pv, pv2;
        {
          auto fill = [&](auto gc) {
            constexpr int g = IC(gc);
            if constexpr (g < 8) xs_dma<8 + g>(rs, gx, gs + h * 2048, ldsw);
            if constexpr (g == (h == 0 ? 7 : SIG_HE)) {
              drain_bar<8, false>();
              if constexpr (h == 1) signal_grp(rs, layer * 32 + rt, w); else if constexpr (layer == 1) signal_all(rs, layer * 4 + 1, b, w);
            }
            if constexpr (g == 27) drain_bar<(h == 0 && layer == 0) ? 16 : (h == 1 ? 7 : 0), true>();
            if constexpr (h == 0 && layer == 0) {
              if constexpr (g >= 10 && g < 26) ld_agpr_imm<192 + 16 * ((g - 10) & 3) + 4 * ((g - 10) >> 2), ((g - 10) & 3) * 1024>(rs, wvo, sw2[(g - 10) >> 2]);
              if constexpr (g >= 28 && g < 36) xs[g - 28] = ld16(rs, wvo + ((g - 28) >> 2) * 1024, sw2[(g - 28) & 3] + 4096);
              if constexpr (g >= 43 && g < 51) *(u32x4*)(smem + LD_W2 + w * 8192 + (g - 43) * 1024 + wvo) = xs[g - 43];
            }
            if constexpr (h == 1) {
              if constexpr (g >= 14 && g < 47 && (g > 27 || !(g & 1)) && (g < 34 || !(g & 1))) {
                constexpr int n = g < 28 ? (g - 14) / 2 : g < 34 ? g - 21 : 13 + (g - 34) / 2, i = n / 5, s = n % 5;
                W2[s][i] = ld16(rs, wvo + (s & 3) * 1024, sw2[i] + (6 + (s >> 2) * 4) * 1024);
              }
            }
            if constexpr (g == POLL_G - 12) pv = poll_ld(rs, nxt_poll, h == 0, w);
            if constexpr (g == POLL_G - 5) pv2 = poll_ld(rs, nxt_poll, h == 0, w);
            if constexpr (g == POLL_G) poll_wait2(rs, nxt_poll, h == 0, pv, pv2, w);
            if constexpr (g >= POLL_G && g < POLL_G + 8) {
              if constexpr (h == 0) xs_dma<g - POLL_G>(rs, gx, gs + 2048, ldsw);
              else hf[g - POLL_G][0] = ld16(rs, wvo + ((g - POLL_G) & 3) * 1024, hb_e + ((g - POLL_G) >> 2) * 4096);
            }
          };
          static_for<60>([&](auto gc) {
            constexpr int g = IC(gc), ks = g >> 2, j = g & 3;
            const u32x4 x = {xb[ks & 1][j][0][0], xb[ks & 1][j][0][1], xb[ks & 1][j][1][0], xb[ks & 1][j][1][1]};
            mfma_aA<12 * ks, ks == 0>(ga[0][j], x);
            xb[(ks + 1) & 1][j][0] = tr_half(xr[j] + (ks + 1) * 4096);
            SB();
            mfma_aA<12 * ks + 4, ks == 0>(ga[1][j], x);
            xb[(ks + 1) & 1][j][1] = tr_half(xr[j] + (ks + 1) * 4096 + 512);
            SB();
            mfma_aA<12 * ks + 8, ks == 0>(ga[2][j], x);
            SB();
            fill(gc);
            SB();
            if constexpr (j == 0) { mfma_v<ks == 0>(qa, x, x); SB(); }
          });
          u32x4 x[4];
#pragma unroll
          for (int j = 0; j < 4; ++j) x[j] = (u32x4){xb[1][j][0][0], xb[1][j][0][1], xb[1][j][1][0], xb[1][j][1][1]};
          mfma_vv(qa, x[0], x[0]); SB();
#pragma unroll
          for (int j = 0; j < 4; ++j) { mfma_aA<188, false>(ga[2][j], x[j]); SB(); }
#pragma unroll
          for (int j = 0; j < 4; ++j) { mfma_aA<180, false>(ga[0][j], x[j]); SB(); }
          asm volatile("" : "+v"(qa));
          {
            const int r = c16 & 3;
            const float q01 = (r & 1) ? qa[1] : qa[0], q23 = (r & 1) ? qa[3] : qa[2];
            SB();
            mfma_aA<184, false>(ga[1][0], x[0]); SB();
            const float q = (r & 2) ? q23 : q01;
            *(float*)ssq = q;
            SB();
            mfma_aA<184, false>(ga[1][1], x[1]); SB();
            tie4(ga[2][0], ga[2][1], ga[2][2], ga[2][3]);
            *(float*)(sss + 384) = ga[2][0][0];
            *(float*)(sss) = ga[2][1][0];
            SB();
            mfma_aA<184, false>(ga[1][2], x[2]); SB();
            *(float*)(sss + 128) = ga[2][2][0];
            *(float*)(sss + 256) = ga[2][3][0];
            SB();
            mfma_aA<184, false>(ga[1][3], x[3]); SB();
          }
        }
        f32x4 cs[3];
#pragma unroll
        for (int i = 0; i < 3; ++i) cs[i] = *(const f32x4*)(smem + LD_CS + w * 192 + i * 64 + kq * 16);
        bar();
        f32x2 st[4];
#pragma unroll
        for (int j = 0; j < 4; ++j) st[j] = *(const f32x2*)(smem + LD_SS + (((w + j) & 3) * 16 + c16) * 8);
        tie4(ga[0][0], ga[0][1], ga[0][2], ga[0][3]);
        tie4(ga[1][0], ga[1][1], ga[1][2], ga[1][3]);
        tie4(ga[2][0], ga[2][1], ga[2][2], ga[2][3]);
        SB();
        u32 hd[3][4];
#pragma unroll
        for (int j = 0; j < 4; ++j) {
          const f32x2 mv = ln_mv(st[j]);
          const float nl = __builtin_amdgcn_rsqf(mv[1]) * -1.4426950408889634f;
#pragma unroll
          for (int i = 0; i < 3; ++i) {
            const f32x4 v = __builtin_elementwise_fma(cs[i], (f32x4){mv[0], mv[0], mv[0], mv[0]}, ga[i][j]);
            const f32x2 g = {v[0], v[1]}, u = {v[2], v[3]};
            const f32x2 a = g * nl;
            const f32x2 e = {__builtin_amdgcn_exp2f(a[0]), __builtin_amdgcn_exp2f(a[1])};
            const f32x2 d = __builtin_elementwise_fma(e, (f32x2){mv[1], mv[1]}, (f32x2){mv[1], mv[1]});
            const f32x2 r = {__builtin_amdgcn_rcpf(d[0]), __builtin_amdgcn_rcpf(d[1])};
            hd[i][j] = pk_bf16((g * u) * r);
            if constexpr (h == 1) {
              const int t = j * 3 + i;
              if (t < 3) hf[8 + t][0] = ld16(rs, wvo, hb_e + (t == 2 ? s10 : (8 + t) * 1024));
              else hf[t - 3][1] = ld16(rs, wvo + ((t - 3) & 3) * 1024, hb_e + NT1 * 256 + ((t - 3) >> 2) * 4096);
            }
          }
          if constexpr (h == 1) SB();
        }
        const int hb = (((w + kq) & 3) * NT1 * 16 + c16) * 16, hs = h_off + ((h * 64 + rt * 4) * NT1 + nt0) * 256;
#pragma unroll
        for (int i = 0; i < 3; ++i) {
          auto a02 = __builtin_amdgcn_permlane32_swap(hd[i][0], hd[i][2], false, false);
          auto a13 = __builtin_amdgcn_permlane32_swap(hd[i][1], hd[i][3], false, false);
          auto o01 = __builtin_amdgcn_permlane16_swap(a02[0], a13[0], false, false);
          auto o23 = __builtin_amdgcn_permlane16_swap(a02[1], a13[1], false, false);
          u32x4 v = {o01[0], o01[1], o23[0], o23[1]};
          st16(rs, (i < 2 || ok2) ? hb + i * 256 : OOB, hs, v);
        }
        SB();
        if constexpr (h == 0) {
          drain_bar<3, false>();
#pragma unroll
          for (int j = 0; j < 4; ++j) { xb[0][j][0] = tr_half(xr[j]); xb[0][j][1] = tr_half(xr[j] + 512); }
        }
        SB();
      });
    }
    {
      ROLES(fresh_lane())
      char* rw[3];
#pragma unroll
      for (int i = 0; i < 3; ++i) rw[i] = smem + LD_XT + (((w + 1 + i) & 3) * 3 + i) * 2048 + wvo;
      const char* rb = smem + LD_XT + w * 6144 + wvo;
      const int rvo = c16 * (LP * 2) + kq * 8;
      const int ovo = c16 * (LP * 2) + (kq & 1) * 32 + (kq >> 1) * 16;
      char* ow = smem + LD_OT + (w * 16 + c16) * 64 + (kq & 1) * 32 + (kq >> 1) * 16;
      const char* ob = smem + LD_OT + (8 * kq + (c16 >> 2)) * 64 + (w & 1) * 32 + (c16 & 3) * 8;
      const char* wlp = smem + LD_W2 + w * 8192 + wvo;
      static_for<2>([&](auto hc) {
        constexpr int h = IC(hc);
        const int tok0 = h * 1024 + rt2 * 32;
        const int eso = ((ct * 64 + w * 16) * LP + tok0) * 2;
        const int hb = h ? hb_l : hb_e;
        f32x4 da[4][2];
        u32x4 wl[2][4];
        u32x2 res[2];
        u32 pv, pv2;
        static_for<88>([&](auto nc) {
          constexpr int n = IC(nc), j = n / 44, s = (n % 44) >> 2, i = (n + 1) & 3;
          if constexpr (s < 4) mfma_aB<192 + 16 * s + 4 * i, s == 0>(da[i][j], hf[s][j]);
          else if constexpr (s < 6) mfma_vv(da[i][j], hf[s][j], wl[s - 4][i]);
          else mfma_vv(da[i][j], hf[s][j], W2[s - 6][i]);
          SB();
          if constexpr (h == 0) {
            if constexpr (n == 3 || n == 7) hf[9 + (n >> 2)][1] = ld16(rs, wvo, hb + NT1 * 256 + (n == 7 ? s10 : 9 * 1024));
            if constexpr (n == 10) res[0] = ld8(rs, rvo, x1_off + eso);
            if constexpr (n == 13) res[1] = ld8(rs, rvo + 32, x1_off + eso);
            if constexpr (n == SIG_HL) { drain_bar<4, false>(); signal_grp(rs, layer * 32 + 16 + rt, w); }
            if constexpr (n == 86) pv = poll_ld(rs, grp_off(layer * 32 + 16 + (rt2 >> 1)), false, w);
          } else {
            if constexpr (n < 33 && n % 3 == 2) hf[n / 3][1] = ld16(rs, wvo + (n / 3 < 8 ? ((n / 3) & 3) * 1024 : 0), hb + NT1 * 256 + (n / 3 < 8 ? (n / 12) * 4096 : n / 3 == 10 ? s10 : (n / 3) * 1024));
            if constexpr (layer == 0 && n == SIG_XE) { drain_bar<(SIG_XE + 1) / 3, false>(); signal_all(rs, 2, b, w); }
            if constexpr (n == 35) res[0] = ld8(rs, rvo, x1_off + eso);
            if constexpr (n == 38) res[1] = ld8(rs, rvo + 32, x1_off + eso);
            if constexpr (layer == 0 && n >= ACC_E && n < ACC_E + 12 && !((n - ACC_E) & 1)) acc_ld1(rs, (n - ACC_E) >> 1, tid, OFF_XT + ACT2 + b * 2 * LP * 2, 0, pr);
          }
          if constexpr (n % 44 >= 6 && n % 44 < 14) wl[(n % 44 - 6) >> 2][(n % 44 - 5) & 3] = *(const u32x4*)(wlp + (((n % 44 - 6) & 4) + ((n % 44 - 5) & 3)) * 1024);
          if constexpr (n == 52 || n == 56 || n == 60) { asm volatile("" : "+v"(da[(n - 48) >> 2][0])); *(f32x4*)(rw[(n - 52) >> 2]) = da[(n - 48) >> 2][0]; }
          SB();
        });
        mfma_done4(da[0][1], da[1][1], da[2][1], da[3][1]);
        SB();
#pragma unroll
        for (int i = 0; i < 3; ++i) *(f32x4*)(rw[i] + 1024) = da[i + 1][1];
        if constexpr (h == 0) pv2 = poll_ld(rs, grp_off(layer * 32 + 16 + (rt2 >> 1)), false, w);
        bar();
        u32x2 xo[2];
#pragma unroll
        for (int j = 0; j < 2; ++j) {
          const f32x4 rf = {lo_f(res[j][0]), hi_f(res[j][0]), lo_f(res[j][1]), hi_f(res[j][1])};
          const f32x4 v = ((da[0][j] + rf) + *(const f32x4*)(rb + j * 1024)) + (*(const f32x4*)(rb + 2048 + j * 1024) + *(const f32x4*)(rb + 4096 + j * 1024));
          xo[j] = (u32x2){pk_bf16(v[0], v[1]), pk_bf16(v[2], v[3])};
        }
        auto s0 = __builtin_amdgcn_permlane16_swap(xo[0][0], xo[1][0], false, false);
        auto s1 = __builtin_amdgcn_permlane16_swap(xo[0][1], xo[1][1], false, false);
        const u32x4 xv = {s0[0], s1[0], s0[1], s1[1]};
        *(u32x4*)ow = xv;
        if constexpr (layer == 0 && h == 1) { SB(); rdy = acc_ready(pr); SB(); }
        if constexpr (layer == 0) st16(rs, ovo, OFF_XT + ACT2 + eso, xv);
        SB();
        if constexpr (h == 0) {
          poll_wait2(rs, grp_off(layer * 32 + 16 + (rt2 >> 1)), false, pv, pv2, w);
#pragma unroll
          for (int s = 0; s < 6; ++s) hf[s][0] = ld16(rs, wvo + (s & 3) * 1024, hb_l + (s >> 2) * 4096);
        }
        SB();
        bar();
        if constexpr (h == 0) {
#pragma unroll
          for (int s = 6; s < 11; ++s) hf[s][0] = ld16(rs, wvo + (s < 8 ? (s & 3) * 1024 : 0), hb_l + (s < 8 ? 4096 : s == 10 ? s10 : s * 1024));
          SB();
        }
        if constexpr (layer == 0) {
          if (w < 2) {
            u32x4 f0 = tr_frag(ob, 0, 256), f1 = tr_frag(ob, 2048, 2048 + 256);
            const u32x4 ones = splat4(0x3f803f80u);
            f32x4 sa, qa;
            mfma_v0(sa, ones, f0); mfma_v0(qa, f0, f0); mfma_vv(sa, ones, f1); mfma_vv(qa, f1, f1);
            asm volatile("s_nop 7" : "+v"(sa), "+v"(qa));
            const int r = c16 & 3;
            float q = r == 0 ? qa[0] : r == 1 ? qa[1] : r == 2 ? qa[2] : qa[3];
            u32x2 v = {__float_as_uint(sa[0]), __float_as_uint(q)};
            const bool diag = (c16 >> 2) == kq;
            const bool fits = q < 0x1p29f;
            const u32x2 fs = to_fix(fits ? sa[0] * 0x1p30f : 0.f), fq = to_fix(fits ? q * 0x1p26f : 0x1p59f);
            const int avo = diag ? (c16 & 3) * 4096 + kq * 16 : OOB, aso = OFF_AC + h * 16384 + (rt2 * 8 + w * 4) * 16;
            asm volatile("s_nop 4\n\tbuffer_atomic_add_x2 %0, %2, %3, %4 offen sc1\n\tbuffer_atomic_add_x2 %1, %2, %3, %4 offen offset:8 sc1"
                         :: "v"(fs), "v"(fq), "v"(avo), "s"(rs), "s"(aso) : "memory");
            __builtin_amdgcn_raw_buffer_store_b64(v, rs, diag ? c16 * 8 : OOB, OFF_ST1 + (ct * L + tok0 + w * 16) * 8, SC1);
          }
        } else {
          u32x4 f = tr_frag(ob, (w >> 1) * 2048, (w >> 1) * 2048 + 256);
          const rsrc_t ro = __builtin_amdgcn_make_buffer_rsrc(out, 0, L * D * 2, 0x00020000);
          __builtin_amdgcn_raw_buffer_store_b128(f, ro, (c16 * D + kq * 8) * 2, ((tok0 + (w & 1) * 16) * D + ct * 64 + (w >> 1) * 32) * 2, SC1);
        }
        SB();
      });
    }
    if constexpr (layer == 0) {
      ROLES(fresh_lane())
      const int xo = b * 2 * LP * 2;
      f32x4 z[2], acc[4];
      u32 pv, pv2;
      acc_wait(rs, tid, OFF_XT + ACT2 + xo, 0, all_off(2, b), pr, rdy);
      acc_ln(rs, smem, 0, tid, dsk, pr, z);
      bar();
      conv_mfma<0, 0, 3, true>(smem, w, lane, acc, [&](auto nc) { if constexpr (IC(nc) == 30) { drain_bar<0, false>(); signal_all(rs, 3, b, w); } });
      conv_out(rs, smem, OFF_X1 + ACT2 + xo, w, lane, acc, z);
      conv_mfma<4, 0, 3, true>(smem, w, lane, acc, [&](auto nc) {
        constexpr int n = IC(nc);
        if constexpr (n >= ACC_L && n < ACC_L + 12 && !((n - ACC_L) & 1)) acc_ld1(rs, (n - ACC_L) >> 1, tid, OFF_XT + ACT2 + xo + 2048, 1, pr);
        if constexpr (n == 36) { drain_bar<6, false>(); signal_all(rs, 4, b, w); }
      });
      acc_wait(rs, tid, OFF_XT + ACT2 + xo + 2048, 1, all_off(3, b), pr, acc_ready(pr));
      acc_ln(rs, smem, 1, tid, dsk, pr, z);
      bar();
      const int gx = xs_voff(w, lane), gs = OFF_X1 + ACT2 + rt * 128;
      conv_mfma<4, 4, 7, false>(smem, w, lane, acc, [&](auto nc) {
        constexpr int n = IC(nc);
        if constexpr (n == CE_P) pv = poll_ld(rs, all_off(4, b), true, w);
        if constexpr (n == CE_P + 14) pv2 = poll_ld(rs, all_off(4, b), true, w);
      });
      poll_wait2(rs, all_off(4, b), true, pv, pv2, w);
      static_for<8>([&](auto ic) { xs_dma<IC(ic)>(rs, gx, gs, ldsw); });
      conv_out(rs, smem, OFF_X1 + ACT2 + xo + 2048, w, lane, acc, z);
      drain_bar<6, false>();
    }
  });
  asm volatile(".Lmk_code_end:" ::: "memory");
}

__device__ __attribute__((used, noinline)) void text_end_marker() { asm volatile(".Lmk_text_end:"); }

extern "C" void kernel_launch(void* const* d_in, const int* in_sizes, int n_in, void* d_out, int out_size,
                              void* d_ws, size_t ws_size, hipStream_t stream) {
  char* ws = (char*)d_ws;
  (void)hipFuncSetAttribute((const void*)main_kernel, hipFuncAttributeMaxDynamicSharedMemorySize, LDS_BYTES);
  main_kernel<<<256, 256, LDS_BYTES, stream>>>(
      (const u16*)d_in[0], (const u16*)d_in[1], (const u16*)d_in[2], (const u16*)d_in[3], (const u16*)d_in[4],
      (const u16*)d_in[5], (const u16*)d_in[6], (const u16*)d_in[7], (const u16*)d_in[8], (const u16*)d_in[10],
      (const u16*)d_in[11], (const u16*)d_in[12], ws, (u16*)d_out);
}
```

```cpp
#include <hip/hip_runtime.h>
#include <stdint.h>
#include <type_traits>

typedef unsigned short u16;
typedef unsigned int u32;
typedef u32 u32x4 __attribute__((ext_vector_type(4)));
typedef u32 u32x2 __attribute__((ext_vector_type(2)));
typedef float f32x4 __attribute__((ext_vector_type(4)));
typedef float f32x2 __attribute__((ext_vector_type(2)));
typedef short s16x4 __attribute__((ext_vector_type(4)));
typedef __bf16 bf16x2 __attribute__((ext_vector_type(2)));
typedef _Float16 f16x2 __attribute__((ext_vector_type(2)));
#define LAS __attribute__((address_space(3)))
#define GAS __attribute__((address_space(1)))
#define DEV __device__ __forceinline__
#define SB() __builtin_amdgcn_sched_barrier(0)
#define IC(x) decltype(x)::value
typedef __amdgpu_buffer_rsrc_t rsrc_t;

constexpr int L = 2048, D = 512, NS = 128, F = 1376;
constexpr int NT1 = 172;
constexpr int KS1 = 16;
constexpr int KS2P = 44;
constexpr float EPS = 1e-5f;
constexpr int SC1 = 16;
constexpr int NSH = 16;
constexpr int SIG_C0 = 2, SIG_HE = 12, SIG_HL = 44, SIG_XE = 30, POLL_G = 52, CE_P = 14, ACC_E = 74, ACC_L = 20;

constexpr int XPAD = 64;
constexpr int LP = L + XPAD;
constexpr int ACT = D * LP * 2;
constexpr int HID = F * L * 2;
constexpr int N_ALL = 7;
constexpr int OFF_CA  = 0;
constexpr int OFF_CH  = OFF_CA + N_ALL * 128 * 256;
constexpr int OFF_INIT = OFF_CH + 64 * 256;
constexpr int OFF_CW  = OFF_INIT + 8 * 256;
constexpr int CTRL_BYTES = OFF_CW + 16 * 256;
constexpr int OFF_XT  = CTRL_BYTES;
constexpr int ACT2 = ACT, HID2 = HID;
constexpr int OFF_X1  = OFF_XT + ACT + ACT2;
constexpr int OFF_H   = OFF_X1 + ACT + ACT2;
constexpr int OFF_WGU = OFF_H + HID + HID2;
constexpr int OFF_WD  = OFF_WGU + (NT1 + 16) * KS1 * 1024;
constexpr int OFF_ST0 = OFF_WD + 32 * KS2P * 1024;
constexpr int OFF_ST1 = OFF_ST0 + L * 8;
constexpr int OFF_AC  = OFF_ST1 + 8 * L * 8;
constexpr int WS_END  = OFF_AC + L * 16;

DEV float lo_f(u32 v) { return __uint_as_float(v << 16); }
DEV float hi_f(u32 v) { return __uint_as_float(v & 0xffff0000u); }
DEV u32 pk_bf16(float a, float b) {
  f32x2 v = {a, b};
  return __builtin_bit_cast(u32, __builtin_convertvector(v, bf16x2));
}
DEV u32 pk_bf16(f32x2 v) { return __builtin_bit_cast(u32, __builtin_convertvector(v, bf16x2)); }
DEV u32 pk_f16(f32x2 v) { return __builtin_bit_cast(u32, __builtin_convertvector(v, f16x2)); }
DEV f32x2 unpk(u32 v) { return (f32x2){lo_f(v), hi_f(v)}; }
DEV float rbf(float a) { return lo_f(pk_bf16(a, a)); }
DEV f32x2 ln_mv(f32x2 sq) {
  f32x2 t = __builtin_elementwise_fma(sq, (f32x2){1.0f / D, 1.0f / D}, (f32x2){0.f, EPS});
  t[1] = fmaxf(__builtin_fmaf(-t[0], t[0], t[1]), EPS);
  return t;
}
struct CM { f32x2 re, im; };
DEV CM mkcm(f32x2 c) { return CM{(f32x2){c[0], c[0]}, (f32x2){-c[1], c[1]}}; }
DEV f32x2 cmul_h(f32x2 x, const CM& c, u32& h) {
  const f32x2 r = __builtin_elementwise_fma(x.yx, c.im, x * c.re);
  h = pk_f16(r);
  return r;
}

constexpr int KCOPY = 4160, KCH = 4 * KCOPY;
constexpr int LD_U = 2 * KCH;
constexpr int UCH = (256 + L) * 2;
constexpr int LD_XT = LD_U + 2 * UCH;
constexpr int LD_Y = LD_XT + 65536;
constexpr int YW = 5120;
constexpr int LD_SS = LD_Y + 4 * YW;
constexpr int LD_OT = LD_Y;
constexpr int LD_CS = LD_SS + 512;
constexpr int LD_W2 = LD_CS + 768;
constexpr int LDS_BYTES = LD_W2 + 32768;
static_assert(LDS_BYTES <= 163840, "LDS");
constexpr int LD_KT = LD_XT + 16384;

#define ACLOB "a0","a1","a2","a3","a4","a5","a6","a7","a8","a9","a10","a11","a12","a13","a14","a15","a16","a17","a18","a19","a20","a21","a22","a23","a24","a25","a26","a27","a28","a29","a30","a31","a32","a33","a34","a35","a36","a37","a38","a39","a40","a41","a42","a43","a44","a45","a46","a47","a48","a49","a50","a51","a52","a53","a54","a55","a56","a57","a58","a59","a60","a61","a62","a63","a64","a65","a66","a67","a68","a69","a70","a71","a72","a73","a74","a75","a76","a77","a78","a79","a80","a81","a82","a83","a84","a85","a86","a87","a88","a89","a90","a91","a92","a93","a94","a95","a96","a97","a98","a99","a100","a101","a102","a103","a104","a105","a106","a107","a108","a109","a110","a111","a112","a113","a114","a115","a116","a117","a118","a119","a120","a121","a122","a123","a124","a125","a126","a127","a128","a129","a130","a131","a132","a133","a134","a135","a136","a137","a138","a139","a140","a141","a142","a143","a144","a145","a146","a147","a148","a149","a150","a151","a152","a153","a154","a155","a156","a157","a158","a159","a160","a161","a162","a163","a164","a165","a166","a167","a168","a169","a170","a171","a172","a173","a174","a175","a176","a177","a178","a179","a180","a181","a182","a183","a184","a185","a186","a187","a188","a189","a190","a191","a192","a193","a194","a195","a196","a197","a198","a199","a200","a201","a202","a203","a204","a205","a206","a207","a208","a209","a210","a211","a212","a213","a214","a215","a216","a217","a218","a219","a220","a221","a222","a223","a224","a225","a226","a227","a228","a229","a230","a231","a232","a233","a234","a235","a236","a237","a238","a239","a240","a241","a242","a243","a244","a245","a246","a247","a248","a249","a250","a251","a252","a253","a254","a255"
#define MF "v_mfma_f32_16x16x32_bf16 "
DEV void mfma_vv(f32x4& c, u32x4 a, u32x4 b) { asm(MF "%0, %1, %2, %0" : "+v"(c) : "v"(a), "v"(b)); }
DEV void mfma_v0(f32x4& c, u32x4 a, u32x4 b) { asm(MF "%0, %1, %2, 0" : "=&v"(c) : "v"(a), "v"(b)); }
template <bool FIRST> DEV void mfma_v(f32x4& c, u32x4 a, u32x4 b) { if constexpr (FIRST) mfma_v0(c, a, b); else mfma_vv(c, a, b); }
template <int R, bool FIRST> DEV void mfma_aA(f32x4& c, u32x4 b) {
  if constexpr (FIRST) asm(MF "%0, a[%c2:%c3], %1, 0" : "=&v"(c) : "v"(b), "i"(R), "i"(R + 3));
  else asm(MF "%0, a[%c2:%c3], %1, %0" : "+v"(c) : "v"(b), "i"(R), "i"(R + 3));
}
template <int R, bool FIRST> DEV void mfma_aB(f32x4& c, u32x4 a) {
  if constexpr (FIRST) asm(MF "%0, %1, a[%c2:%c3], 0" : "=&v"(c) : "v"(a), "i"(R), "i"(R + 3));
  else asm(MF "%0, %1, a[%c2:%c3], %0" : "+v"(c) : "v"(a), "i"(R), "i"(R + 3));
}
template <bool FIRST> DEV void mfma_h(f32x4& c, u32x4 a, u32x4 b) {
  if constexpr (FIRST) asm("v_mfma_f32_16x16x32_f16 %0, %1, %2, 0" : "=&v"(c) : "v"(a), "v"(b));
  else asm("v_mfma_f32_16x16x32_f16 %0, %1, %2, %0" : "+v"(c) : "v"(a), "v"(b));
}
DEV void mfma_done4(f32x4& a, f32x4& b, f32x4& c, f32x4& d) { asm volatile("s_nop 7" : "+v"(a), "+v"(b), "+v"(c), "+v"(d)); }
DEV void tie4(f32x4& a, f32x4& b, f32x4& c, f32x4& d) { asm volatile("" : "+v"(a), "+v"(b), "+v"(c), "+v"(d)); }
DEV void bar() { asm volatile("s_waitcnt lgkmcnt(0)\n\ts_barrier" ::: "memory", ACLOB); }

DEV u32x4 ld16(rsrc_t rs, int voff, int soff) { return __builtin_amdgcn_raw_buffer_load_b128(rs, voff, soff, SC1); }
DEV u32x2 ld8(rsrc_t rs, int voff, int soff) { return __builtin_amdgcn_raw_buffer_load_b64(rs, voff, soff, SC1); }
DEV void st16(rsrc_t rs, int voff, int soff, u32x4 v) { __builtin_amdgcn_raw_buffer_store_b128(v, rs, voff, soff, SC1); }
DEV int fresh_lane() {
  int l;
  asm volatile("v_mbcnt_lo_u32_b32 %0, -1, 0\n\tv_mbcnt_hi_u32_b32 %0, -1, %0" : "=v"(l));
  return l;
}
DEV u32x4 splat4(u32 c) {
  u32x4 r;
  asm volatile("v_mov_b32 %0, %4\n\tv_mov_b32 %1, %4\n\tv_mov_b32 %2, %4\n\tv_mov_b32 %3, %4"
               : "=v"(r[0]), "=v"(r[1]), "=v"(r[2]), "=v"(r[3]) : "i"(c));
  asm volatile("s_nop 1" : "+v"(r));
  return r;
}
DEV u32x2 tr_half(const char* p) { return __builtin_bit_cast(u32x2, __builtin_amdgcn_ds_read_tr16_b64_v4i16((LAS s16x4*)p)); }
DEV u32x4 tr_frag(const char* p, int o0, int o1) {
  u32x2 a = tr_half(p + o0), c = tr_half(p + o1);
  return (u32x4){a[0], a[1], c[0], c[1]};
}
template <int N, typename FN, int I = 0>
DEV void static_for(FN&& fn) {
  if constexpr (I < N) { fn(std::integral_constant<int, I>{}); static_for<N, FN, I + 1>(static_cast<FN&&>(fn)); }
}

constexpr int OOB = 0x7ffffff0;
constexpr int SPIN_CAP = 400000;
constexpr int ACC_SLEEP = 2;
DEV int all_off(int edge, int b) { return OFF_CA + (edge * 8 + (b & 7)) * 4096; }
DEV int grp_off(int idx) { return OFF_CH + idx * 256; }
DEV u32 poll_ld(rsrc_t rs, int soff, bool all, int w) {
  const int voff = (all ? (fresh_lane() & (NSH - 1)) * 256 : 0) | (w == 3 ? 0 : 0x7ff00000);
  return __builtin_amdgcn_raw_buffer_load_b32(rs, voff, soff, SC1);
}
DEV void poll_spin(rsrc_t rs, int soff, bool all, u32 f) {
  int spins;
  u32 voff;
  asm volatile("v_cmp_gt_u32 vcc, 64, %0\n\t"
               "s_mov_b32 %1, %c5\n\t"
               "s_nop 3\n\t"
               "s_cbranch_vccz 2f\n\t"
               "v_mbcnt_lo_u32_b32 %2, -1, 0\n\t"
               "v_and_b32 %2, %c6, %2\n\t"
               "v_lshlrev_b32 %2, 8, %2\n"
               "1:\n\t"
               "buffer_load_dword %0, %2, %3, %4 offen sc1\n\t"
               "s_sub_u32 %1, %1, 1\n\t"
               "s_waitcnt vmcnt(0)\n\t"
               "s_cbranch_scc1 2f\n\t"
               "v_cmp_gt_u32 vcc, 64, %0\n\t"
               "s_nop 4\n\t"
               "s_cbranch_vccnz 1b\n"
               "2:"
               : "+v"(f), "=&s"(spins), "=&v"(voff) : "s"(rs), "s"(soff), "i"(SPIN_CAP), "i"(all ? NSH - 1 : 0) : "memory", "vcc", "scc");
}
DEV void poll_wait(rsrc_t rs, int soff, bool all, u32 f, int w) {
  if (w == 3) poll_spin(rs, soff, all, f);
  asm volatile("s_barrier" ::: "memory", ACLOB);
}
DEV void poll_wait2(rsrc_t rs, int soff, bool all, u32 f0, u32 f1, int w) {
  if (w == 3) { if (!__all(f0 >= 64u)) poll_spin(rs, soff, all, f1); }
  asm volatile("s_barrier" ::: "memory", ACLOB);
}
template <int YOUNGER, bool LDSW>
DEV void drain_bar() {
  if constexpr (LDSW) asm volatile("s_waitcnt vmcnt(%c0) lgkmcnt(0)\n\ts_barrier" :: "i"(YOUNGER) : "memory", ACLOB);
  else asm volatile("s_waitcnt vmcnt(%c0)\n\ts_barrier" :: "i"(YOUNGER) : "memory", ACLOB);
}
DEV void signal_all(rsrc_t rs, int edge, int b, int w) {
  if (w == 0) {
    const int lane = fresh_lane();
    __builtin_amdgcn_raw_ptr_buffer_atomic_add_i32(NSH / 4, rs, lane < 8 ? lane * 4096 : OOB, OFF_CA + edge * 32768 + (b & (NSH - 1)) * 256, SC1);
  }
}
DEV void signal_grp(rsrc_t rs, int idx, int w) {
  if (w == 0) {
    const int lane = fresh_lane();
    __builtin_amdgcn_raw_ptr_buffer_atomic_add_i32(4, rs, lane == 0 ? 0 : OOB, OFF_CH + idx * 256, SC1);
  }
}

DEV void init_bump(rsrc_t rs) {
  const int lane = fresh_lane();
  __builtin_amdgcn_raw_ptr_buffer_atomic_add_i32(1, rs, lane < 8 ? lane * 256 : OOB, OFF_INIT, SC1);
}
DEV void init_wait(rsrc_t rs, int soff, u32 f0, u32 f) {
  int spins;
  asm volatile("v_cmp_eq_u32 vcc, %2, %0\n\t"
               "s_mov_b32 %1, %c5\n\t"
               "s_nop 3\n\t"
               "s_cbranch_vccz 2f\n"
               "1:\n\t"
               "buffer_load_dword %0, off, %3, %4 sc1\n\t"
               "s_sub_u32 %1, %1, 1\n\t"
               "s_waitcnt vmcnt(0)\n\t"
               "s_cbranch_scc1 2f\n\t"
               "v_cmp_eq_u32 vcc, %2, %0\n\t"
               "s_nop 4\n\t"
               "s_cbranch_vccnz 1b\n"
               "2:"
               : "+v"(f), "=&s"(spins) : "v"(f0), "s"(rs), "s"(soff), "i"(SPIN_CAP) : "memory", "vcc", "scc");
}

template <int P> struct PrepRegs { u32x4 pa[P], pb[P]; u32x2 xv[2]; };
template <int P>
DEV void prep_ld1(rsrc_t rs, int i, int tid, int xso, int sso, PrepRegs<P>& pr) {
  if (i < 2 * P) {
    if (i & 1) pr.pb[i >> 1] = ld16(rs, tid * 32 + 16, sso + (i >> 1) * (L * 8));
    else pr.pa[i >> 1] = ld16(rs, tid * 32, sso + (i >> 1) * (L * 8));
  } else pr.xv[i - 2 * P] = ld8(rs, tid * 8, xso + (i - 2 * P) * (LP * 2));
}
template <int P>
DEV void prep_load(rsrc_t rs, int tid, int xso, int sso, PrepRegs<P>& pr) {
#pragma unroll
  for (int i = 0; i < 2 * P + 2; ++i) prep_ld1<P>(rs, i, tid, xso, sso, pr);
  SB();
}
DEV void ln_apply(char* smem, int h, int tid, u32 dsk, f32x4 s0, f32x4 s1, const u32x2 (&xv)[2], f32x4 (&z)[2]) {
  const f32x2 t0 = ln_mv(s0.xy), t1 = ln_mv(s0.zw), t2 = ln_mv(s1.xy), t3 = ln_mv(s1.zw);
  const f32x2 rr[2] = {{__builtin_amdgcn_rsqf(t0[1]), __builtin_amdgcn_rsqf(t1[1])}, {__builtin_amdgcn_rsqf(t2[1]), __builtin_amdgcn_rsqf(t3[1])}};
  const f32x2 mr[2] = {{-t0[0] * rr[0][0], -t1[0] * rr[0][1]}, {-t2[0] * rr[1][0], -t3[0] * rr[1][1]}};
#pragma unroll
  for (int c = 0; c < 2; ++c) {
    const float dk = c ? hi_f(dsk) : lo_f(dsk);
    u32x2 u;
    f32x2 zz[2];
#pragma unroll
    for (int i = 0; i < 2; ++i) {
      const f32x2 x = unpk(xv[c][i]);
      const f32x2 uf = __builtin_elementwise_fma(x, rr[i], mr[i]);
      u[i] = pk_bf16(uf);
      zz[i] = __builtin_elementwise_fma(uf, (f32x2){dk, dk}, x);
    }
    z[c] = (f32x4){zz[0][0], zz[0][1], zz[1][0], zz[1][1]};
    *(u32x2*)(smem + LD_U + c * UCH + (256 + h * 1024) * 2 + tid * 8) = u;
  }
}
DEV void prep_ln(char* smem, int h, int tid, u32 dsk, const PrepRegs<1>& pr, f32x4 (&z)[2]) {
  ln_apply(smem, h, tid, dsk, __builtin_bit_cast(f32x4, pr.pa[0]), __builtin_bit_cast(f32x4, pr.pb[0]), pr.xv, z);
}
struct AccRegs { u32x4 a[4]; u32x2 xv[2]; };
DEV void acc_ld1(rsrc_t rs, int i, int tid, int xso, int h, AccRegs& r) {
  if (i < 4) r.a[i] = ld16(rs, tid * 16, OFF_AC + h * 16384 + i * 4096);
  else r.xv[i - 4] = ld8(rs, tid * 8, xso + (i - 4) * (LP * 2));
}
DEV u32x2 to_fix(float t) {
  const float hi = __builtin_floorf(t * 0x1p-32f);
  const u32 lo = (u32)fminf(__builtin_fmaf(hi, -0x1p32f, t), 4294967040.f);
  return (u32x2){(lo & ~15u) | 1u, (u32)(int)hi};
}
DEV bool acc_ready(const AccRegs& r) {
  u32 o = 0u, n = ~0u;
#pragma unroll
  for (int i = 0; i < 4; ++i) { o |= r.a[i][0] | r.a[i][2]; n &= r.a[i][0] & r.a[i][2]; }
  const u32 mx = max(max(r.xv[0][0], r.xv[0][1]), max(r.xv[1][0], r.xv[1][1]));
  return __all(((o & 7u) | (~n & 8u)) == 0u && mx != ~0u);
}
DEV void acc_wait(rsrc_t rs, int tid, int xso, int h, int poll_off, AccRegs& r, bool ready) {
  if (!ready) {
#pragma unroll 1
    for (int spins = 0; spins < SPIN_CAP; ++spins) {
      bool done = false;
      if ((spins & 7) == 7) done = __all(__builtin_amdgcn_raw_buffer_load_b32(rs, (tid & (NSH - 1)) * 256, poll_off, SC1) >= 64u);
      asm volatile("s_waitcnt vmcnt(0)" ::: "memory");
#pragma unroll
      for (int i = 0; i < 6; ++i) acc_ld1(rs, i, tid, xso, h, r);
      if (done || acc_ready(r)) break;
      __builtin_amdgcn_s_sleep(ACC_SLEEP);
    }
    __builtin_amdgcn_s_waitcnt(0x0F70);
  }
}
DEV void acc_ln(rsrc_t rs, char* smem, int h, int tid, u32 dsk, const AccRegs& r, f32x4 (&z)[2]) {
  f32x2 st[4];
#pragma unroll
  for (int i = 0; i < 4; ++i) {
    const f32x2 hi = {(float)(int)r.a[i][1], (float)(int)r.a[i][3]}, lo = {(float)r.a[i][0], (float)r.a[i][2]};
    st[i] = __builtin_elementwise_fma(hi, (f32x2){0x1p2f, 0x1p6f}, lo * (f32x2){0x1p-30f, 0x1p-26f});
  }
  f32x4 s0 = {st[0][0], st[0][1], st[1][0], st[1][1]}, s1 = {st[2][0], st[2][1], st[3][0], st[3][1]};
  const u32 mk = (r.a[0][3] | r.a[1][3]) | (r.a[2][3] | r.a[3][3]);
  if (__builtin_expect(__any(mk >= (1u << 27)), 0)) {
    poll_spin(rs, all_off(2 + h, blockIdx.x), true, 0u);
    s0 = s1 = (f32x4){0.f, 0.f, 0.f, 0.f};
#pragma unroll 1
    for (int p = 0; p < 8; ++p) {
      s0 += __builtin_bit_cast(f32x4, ld16(rs, tid * 32, OFF_ST1 + h * 8192 + p * (L * 8)));
      s1 += __builtin_bit_cast(f32x4, ld16(rs, tid * 32 + 16, OFF_ST1 + h * 8192 + p * (L * 8)));
    }
  }
  ln_apply(smem, h, tid, dsk, s0, s1, r.xv, z);
}
template <int M0, int I0, int I1> struct ConvPlan {
  static constexpr int Q0 = (M0 - I1) > 0 ? (M0 - I1) : 0, Q1 = M0 + 3 - I0;
  static constexpr int NQ = Q1 - Q0 + 1, NU = I1 - I0 + 1, NF = NQ + NU;
  static constexpr int count() { int n = 0; for (int i = I0; i <= I1; ++i) for (int m = 0; m < 4; ++m) n += (M0 + m - i >= 0); return n; }
  static constexpr int NM = count();
  static constexpr int im(int t) { int n = 0; for (int i = I0; i <= I1; ++i) for (int m = 0; m < 4; ++m) if (M0 + m - i >= 0) { if (n == t) return i * 4 + m; ++n; } return 0; }
  static constexpr int frag(int f) {
    if (f == 0) return 0;
    if (f <= 4) return -(Q1 - 4 + f + 1);
    int g = f - 5, nlow = NQ - 4;
    if (g < 2 * nlow) return (g & 1) ? -(Q1 - 4 - (g >> 1) + 1) : 1 + (g >> 1);
    return 1 + nlow + (g - 2 * nlow);
  }
};
template <int M0, int I0, int I1, bool FIRST, typename FN>
DEV void conv_mfma(char* smem, int w, int lane, f32x4 (&acc)[4], FN&& fill) {
  using P = ConvPlan<M0, I0, I1>;
  const int c16 = lane & 15, kq = lane >> 4;
  const int cch = w >> 1, wr = w & 1;
  const char* tb = smem + cch * KCH + (c16 >> 2) * KCOPY + (16 + 16 * (1 + 8 * wr) - 8 * kq + 4 * (c16 & 3) - 4) * 2;
  const char* ub = smem + LD_U + cch * UCH + (256 - 16 * (1 + 8 * wr + 6) + 16 * c16 + 8 * kq) * 2;
  u32x4 T[2][P::NQ], U[2][P::NU];
  auto ldf = [&](auto rc, auto fc) {
    constexpr int rr = IC(rc), f = P::frag(IC(fc));
    if constexpr (f >= 0) U[rr & 1][f] = *(const u32x4*)(ub + (3 - rr) * 64 + (I0 + f) * 512);
    else T[rr & 1][-f - 1 - P::Q0] = tr_frag(tb, rr * 64 + (-f - 1) * 512 + 8, rr * 64 + (-f - 1) * 512);
  };
  static_for<P::NF>([&](auto fc) { ldf(std::integral_constant<int, 0>{}, fc); });
  SB();
  static_for<4 * P::NM>([&](auto nc) {
    constexpr int n = IC(nc), rr = n / P::NM, t = n % P::NM, i = P::im(t) >> 2, m = P::im(t) & 3, q = M0 + m - i;
    mfma_v<FIRST && rr == 0 && i == I0>(acc[m], T[rr & 1][q - P::Q0], U[rr & 1][i - I0]);
    if constexpr (rr < 3 && t < P::NF) ldf(std::integral_constant<int, rr + 1>{}, std::integral_constant<int, t>{});
    SB();
    fill(nc);
    SB();
  });
}
DEV void conv_out(rsrc_t rs, char* smem, int xso, int w, int lane, f32x4 (&acc)[4], const f32x4 (&z)[2]) {
  const int tid = w * 64 + lane;
  char* yw = smem + LD_Y + w * YW + (lane & 15) * 80 + (lane >> 4) * 16;
  const char* yb = smem + LD_Y + tid * 16 + (tid >> 2) * 16;
  const int vo = ((tid & 1) * LP + (tid >> 1) * 8) * 2;
  mfma_done4(acc[0], acc[1], acc[2], acc[3]);
#pragma unroll
  for (int m = 0; m < 4; ++m) *(f32x4*)(yw + m * 1280) = acc[m];
  bar();
  u32x2 o[2];
#pragma unroll
  for (int c = 0; c < 2; ++c) {
    const f32x4 y = (*(const f32x4*)(yb + (2 * c) * YW) + z[c]) + *(const f32x4*)(yb + (2 * c + 1) * YW);
    o[c] = (u32x2){pk_bf16(y[0], y[1]), pk_bf16(y[2], y[3])};
  }
  const bool odd = tid & 1;
  u32 s0 = odd ? o[0][0] : o[1][0], s1 = odd ? o[0][1] : o[1][1];
  u32 g0 = __builtin_amdgcn_update_dpp(0u, s0, 0xB1, 0xf, 0xf, true);
  u32 g1 = __builtin_amdgcn_update_dpp(0u, s1, 0xB1, 0xf, 0xf, true);
  u32x4 v = odd ? (u32x4){g0, g1, o[1][0], o[1][1]} : (u32x4){o[0][0], o[0][1], g0, g1};
  st16(rs, vo, xso, v);
  SB();
}

template <int R, int IMM>
DEV void ld_agpr(rsrc_t rs, int voff, int sbase) {
  int tmp;
  asm volatile("s_add_u32 %0, %3, %c6\n\tbuffer_load_dwordx4 a[%c4:%c5], %1, %2, %0 offen sc1"
               : "=&s"(tmp) : "v"(voff), "s"(rs), "s"(sbase), "i"(R), "i"(R + 3), "i"(IMM) : "memory", "scc");
}
template <int I> DEV void ld_w1(rsrc_t rs, int voff, int so01, int so2) {
  constexpr int ks = I / 3, i = I % 3;
  if constexpr (i < 2) ld_agpr<4 * I, (i * KS1 + ks) * 1024>(rs, voff, so01);
  else ld_agpr<4 * I, ks * 1024>(rs, voff, so2);
}
template <int R, int IMM>
DEV void ld_agpr_imm(rsrc_t rs, int voff, int soff) {
  asm volatile("buffer_load_dwordx4 a[%c3:%c4], %0, %1, %2 offen offset:%c5 sc1"
               :: "v"(voff), "s"(rs), "s"(soff), "i"(R), "i"(R + 3), "i"(IMM) : "memory");
}

constexpr int XS_STEP = 32 * LP * 2;
DEV int xs_voff(int w, int lane) {
  const int fsw = ((lane >> 4) & 1) | ((w & 1) << 1);
  return (w * 8 + (lane >> 3)) * (LP * 2) + ((((lane & 7) >> 1) ^ fsw) * 32) + (lane & 1) * 16;
}
template <int KS>
DEV void xs_dma(rsrc_t rs, int voff, int sbase, int ldsw) {
  int tmp;
  asm volatile("s_add_u32 m0, %4, %c5\n\ts_add_u32 %0, %3, %c6\n\tbuffer_load_dwordx4 %1, %2, %0 offen sc1 lds"
               : "=&s"(tmp) : "v"(voff), "s"(rs), "s"(sbase), "s"(ldsw), "i"(LD_XT + KS * 4096), "i"(KS * XS_STEP) : "memory", "scc");
}

__global__ __launch_bounds__(256, 1) void main_kernel(
    const u16* __restrict__ x0, const u16* __restrict__ Are, const u16* __restrict__ Aim,
    const u16* __restrict__ Bre, const u16* __restrict__ Bim, const u16* __restrict__ Cre,
    const u16* __restrict__ Cim, const u16* __restrict__ logdt, const u16* __restrict__ dskip,
    const u16* __restrict__ wg, const u16* __restrict__ wu, const u16* __restrict__ wd,
    char* __restrict__ ws, u16* __restrict__ out) {
  extern __shared__ __attribute__((aligned(16))) char smem[];
  const int w_ = __builtin_amdgcn_readfirstlane(threadIdx.x >> 6);
  const int b_ = blockIdx.x;

  const rsrc_t rs = __builtin_amdgcn_make_buffer_rsrc(ws, 0, WS_END, 0x00020000);
  asm volatile("" ::: ACLOB);
  u32 cpf;
  {
    unsigned long long pc; int len;
    asm volatile("s_getpc_b64 %0\n.Lmk_code_begin:\n\ts_mov_b32 %1, .Lmk_code_end-.Lmk_code_begin\n\t"
                 "s_max_i32 %1, %1, .Lmk_text_end-.Lmk_code_begin" : "=s"(pc), "=s"(len) :: "scc");
    const int off = min((int)((min(threadIdx.x & 63, 4u) * 4 + (threadIdx.x >> 6)) * 32 + (blockIdx.x >> 3)) * 128, len - 4) & ~3;
    cpf = *(const GAS u32*)(pc + (unsigned long long)(u32)off);
    SB();
    asm volatile("" :: "s"(x0), "s"(Are), "s"(Aim), "s"(Bre), "s"(Bim), "s"(Cre), "s"(Cim), "s"(logdt), "s"(dskip), "s"(wg), "s"(wu), "s"(wd), "s"(ws));
  }
  const int rt_ = ((b_ & 7) >> 2) * 8 + ((b_ >> 3) & 7), cb_ = (b_ & 3) * 4 + (b_ >> 6);
  const int rt2_ = (b_ & 7) * 4 + ((b_ >> 3) & 3), ct_ = b_ >> 5;
  const int start_ = cb_ * 11 - (cb_ >> 2), cnt_ = (cb_ & 3) == 3 ? 10 : 11;
  const int nbase_ = (w_ < 3) ? w_ * 3 : cnt_ - 2;
  const int nt0_ = start_ + nbase_;
  const int ok2_ = __builtin_amdgcn_readfirstlane((w_ < 3) && (nbase_ + 2 < cnt_ - 2));
#define ROLES(LANE) \
  const int w = w_, b = b_, rt = rt_, rt2 = rt2_, ct = ct_, nt0 = nt0_, ok2 = ok2_; \
  const int lane = LANE, tid = w * 64 + lane, c16 = lane & 15, kq = lane >> 4, wvo = lane * 16; \
  const int x1_off = OFF_X1 + layer * ACT2, h_off = OFF_H + layer * HID2; \
  const int hb_e = h_off + (rt2 * 2 * NT1 * 16 + w * 11 * 64) * 16, hb_l = hb_e + 64 * NT1 * 256; \
  const int s10 = (w == 3) ? 9 * 1024 : 10 * 1024; \
  (void)b; (void)rt; (void)ct; (void)nt0; (void)ok2; (void)tid; (void)c16; (void)kq; (void)wvo; (void)x1_off; (void)hb_e; (void)hb_l; (void)s10;

  u32 dsk;
  const int ldsb = (int)(u32)(size_t)(LAS char*)smem, ldsw = ldsb + w_ * 1024;
  {
    constexpr int layer = 0;
    ROLES(fresh_lane())
    const int d0 = 2 * b;
    u32x4 xf[4], xt[2], wr[4];
    const bool initw = (b == 0) && (w == 3);
    const int init_off = OFF_INIT + (b & 7) * 256;
    const u32 f0 = __builtin_amdgcn_raw_buffer_load_b32(rs, 0, init_off, SC1);
    if (initw) {
      __builtin_amdgcn_raw_buffer_store_b32(0u, rs, lane * 256, OFF_CA + 6 * 32768, SC1);
      __builtin_amdgcn_raw_buffer_store_b32(0u, rs, lane * 256, OFF_CA + 6 * 32768 + 16384, SC1);
      __builtin_amdgcn_raw_buffer_store_b32(0u, rs, c16 * 256, OFF_CW, SC1);
    }
    SB();
    {
      const rsrc_t rx = __builtin_amdgcn_make_buffer_rsrc((void*)x0, 0, L * D * 2, 0x00020000);
      const rsrc_t rg = __builtin_amdgcn_make_buffer_rsrc((void*)wg, 0, D * F * 2, 0x00020000);
      const rsrc_t ru = __builtin_amdgcn_make_buffer_rsrc((void*)wu, 0, D * F * 2, 0x00020000);
      const int fvo = (c16 & 7) * (D * 2) + kq * 16, fso = (b * 8 * D + w * 128) * 2;
#pragma unroll
      for (int i = 0; i < 4; ++i) xf[i] = __builtin_amdgcn_raw_buffer_load_b128(rx, fvo + i * 64, fso, 0);
      SB();
      const int tvo = (lane >> 1) * (D * 2) + (lane & 1) * 16, tso = (rt2 * 64 * D + ct * 64 + w * 16) * 2;
      xt[0] = __builtin_amdgcn_raw_buffer_load_b128(rx, tvo, tso, 0);
      xt[1] = __builtin_amdgcn_raw_buffer_load_b128(rx, tvo, tso + 32 * D * 2, 0);
      SB();
      const int gvo = kq * (F * 2) + (c16 < cnt_ ? c16 : 0) * 16, gso = ((rt * 32 + w * 8) * F + start_ * 8) * 2;
      wr[0] = __builtin_amdgcn_raw_buffer_load_b128(rg, gvo, gso, 0);
      wr[1] = __builtin_amdgcn_raw_buffer_load_b128(ru, gvo, gso, 0);
      wr[2] = __builtin_amdgcn_raw_buffer_load_b128(rg, gvo, gso + 4 * F * 2, 0);
      wr[3] = __builtin_amdgcn_raw_buffer_load_b128(ru, gvo, gso + 4 * F * 2, 0);
    }
    SB();
    {
      u32x4 z = splat4(0u);
      if (tid < 16) *(u32x4*)(smem + (tid >> 1) * KCOPY + (tid & 1) * 16) = z;
      if (tid >= 64 && tid < 128) *(u32x4*)(smem + LD_U + (lane >> 5) * UCH + (lane & 31) * 16) = z;
      if (tid == 128) *(u32x4*)(smem + LD_KT + 760 * 16) = z;
    }
    int ix[12];
#pragma unroll
    for (int i = 0; i < 12; i += 2) {
      const int l0 = tid * 8 - 4 + i;
      const u32 bits = pk_bf16((float)l0, (float)(l0 + 1));
      const int i0 = l0 < 256 ? (l0 < 0 ? 676 : l0) : (int)(bits & 0xffffu) - 17024;
      const int i1 = l0 < 256 ? (l0 < 0 ? 676 : l0 + 1) : (int)(bits >> 16) - 17024;
      ix[i] = ldsb + LD_KT + (i0 + (i0 >> 3)) * 16; ix[i + 1] = ldsb + LD_KT + (i1 + (i1 >> 3)) * 16;
      asm volatile("" : "+v"(ix[i]), "+v"(ix[i + 1]));
    }
    SB();
    if (initw) { asm volatile("s_waitcnt vmcnt(10)" ::: "memory"); init_bump(rs); }
    const u32 pn = b * (2 * NS) + tid;
    const u32 p_ar = Are[pn], p_ai = Aim[pn], p_br = Bre[pn], p_bi = Bim[pn];
    SB();
    const int ktw = ldsb + LD_KT + (w & 1) * 8 + (w >> 1) * 4;
    int kx[2][2][4];
#pragma unroll
    for (int c = 0; c < 2; ++c) {
      const int B = 16 * c + c16;
#pragma unroll
      for (int i = 0; i < 4; ++i) {
        const int i0 = kq < 2 ? 8 * B + 4 * kq + i : kq == 2 ? 256 + 4 * B + i : 384 + (i >> 1) * 64 + 2 * B + (i & 1);
        const int i1 = kq == 0 ? 512 + 32 * i + B : (kq == 1 && i == 0) ? 640 + B : 680;
        kx[0][c][i] = ktw + (i0 + (i0 >> 3)) * 16; kx[1][c][i] = ktw + (i1 + (i1 >> 3)) * 16;
        asm volatile("" : "+v"(kx[0][c][i]), "+v"(kx[1][c][i]));
      }
    }
    SB();
    const u32 p_cr = Cre[pn], p_ci = Cim[pn];
    const u32 p_ld = logdt[(u32)(d0 + (tid >> 7))];
    dsk = ((const u32*)dskip)[(u32)(b + (lane >> 6))];
    const int chunk = b + 256 * (w ^ 2);
    u32x4 w2r[4];
    SB();
    {
      const u32x4 ones = splat4(0x3f803f80u);
      f32x4 sa, qa;
      static_for<4>([&](auto ic) { mfma_v<IC(ic) == 0>(sa, ones, xf[IC(ic)]); mfma_v<IC(ic) == 0>(qa, xf[IC(ic)], xf[IC(ic)]); });
      asm volatile("s_nop 7" : "+v"(sa), "+v"(qa));
      if ((c16 >> 2) == kq && c16 < 8) {
        const int r = c16 & 3;
        float q = r == 0 ? qa[0] : r == 1 ? qa[1] : r == 2 ? qa[2] : qa[3];
        *(f32x2*)(smem + LD_SS + (w * 8 + c16) * 8) = (f32x2){sa[0], q};
      }
    }
    SB();
    {
      char* img = smem + LD_Y + w * YW;
      *(u32x4*)(img + wvo) = xt[0];
      *(u32x4*)(img + 1024 + wvo) = xt[1];
      const char* tp = img + (kq * 8 + (c16 >> 2)) * 32 + (c16 & 3) * 8;
      u32x4 t0 = tr_frag(tp, 0, 128), t1 = tr_frag(tp, 1024, 1024 + 128);
      const int so = OFF_XT + ((ct * 64 + w * 16) * LP + rt2 * 64) * 2;
      st16(rs, c16 * (LP * 2) + kq * 16, so, t0);
      st16(rs, c16 * (LP * 2) + kq * 16 + 64, so, t1);
    }
    bar();
    asm volatile("" :: "v"(cpf));
    if (initw) init_bump(rs);
    const int so01 = OFF_WGU + nt0 * KS1 * 1024, so2 = ok2 ? so01 + 2 * KS1 * 1024 : w == 3 ? OFF_WGU + (NT1 + cb_) * KS1 * 1024 : WS_END;
    if (tid < 8) {
      const f32x2* sp = (const f32x2*)(smem + LD_SS) + tid;
      f32x2 v = (sp[0] + sp[8]) + (sp[16] + sp[24]);
      __builtin_amdgcn_raw_buffer_store_b64(__builtin_bit_cast(u32x2, v), rs, tid * 8, OFF_ST0 + b * 64, SC1);
    }
    if (w == 2) {
      const int ln = b * 4 + lane - 8;
      st16(rs, lane < 8 ? OFF_AC + b * 128 + wvo : (lane < 12 && ln < 832) ? (ln < 768 ? ln : ln + 128) * 256 : OOB, 0, splat4(0u));
    }
    SB();
    constexpr int WROW = 368;
    if (c16 < 11) {
      char* wdst = smem + LD_XT + (w * 8 + kq) * WROW + c16 * 32;
#pragma unroll
      for (int i = 0; i < 2; ++i) {
        const u32x4 g = wr[2 * i], u = wr[2 * i + 1];
        *(u32x4*)(wdst + i * 4 * WROW) = (u32x4){g[0], u[0], g[1], u[1]};
        *(u32x4*)(wdst + i * 4 * WROW + 16) = (u32x4){g[2], u[2], g[3], u[3]};
      }
    }
    SB();
    SB();
    PrepRegs<1> pe, pl;
    u32 pv, pv2;
    const u32 f1 = __builtin_amdgcn_raw_buffer_load_b32(rs, 0, init_off, SC1);
    SB();
    {
      constexpr int TROW = 272;
      char* tab = smem + LD_XT + 32768 - 4096 + w * (32 * TROW);
      char* tar = smem + LD_Y + w * YW;
      char* tar2 = smem + LD_W2 + w * 4096;
      const float dt = rbf(expf(lo_f(p_ld)));
      const float ar = dt * lo_f(p_ar), ai = dt * lo_f(p_ai), br = dt * lo_f(p_br), bi = dt * lo_f(p_bi);
      const float cr = lo_f(p_cr), ci = lo_f(p_ci);
      const float I2PI_HI = 0.15915494309189535f;
      const float I2PI_LO = (float)(0.15915494309189535 - (double)0.15915494309189535f);
      float a2 = ar * 1.4426950408889634f, grow = 0.f;
      const bool grows = __any(a2 > 0.f);
      if (__builtin_expect(grows, 0)) {
        grow = fmaxf(a2, 0.f);
#pragma unroll
        for (int o = 1; o < 64; o <<= 1) grow = fmaxf(grow, __shfl_xor(grow, o));
        a2 -= grow;
      }
      const float rh = ai * I2PI_HI;
      const float rl = __builtin_fmaf(ai, I2PI_LO, __builtin_fmaf(ai, I2PI_HI, -rh));
      auto cexp = [&](float l) {
        const float e = __builtin_amdgcn_exp2f(a2 * l);
        const float fr = __builtin_fmaf(l, rl, __builtin_amdgcn_fractf(rh * l));
        return (f32x2){__builtin_amdgcn_cosf(fr), __builtin_amdgcn_sinf(fr)} * e;
      };
      const float kr = cr * br - ci * bi, ki = cr * bi + ci * br;
      u32 mx = max(__float_as_uint(kr) & 0x7f800000u, __float_as_uint(ki) & 0x7f800000u);
      mx = max(mx, (u32)__builtin_amdgcn_update_dpp(0, (int)mx, 0xB1, 0xf, 0xf, true));
      mx = max(mx, (u32)__builtin_amdgcn_update_dpp(0, (int)mx, 0x4E, 0xf, 0xf, true));
      mx = max(mx, (u32)__builtin_amdgcn_update_dpp(0, (int)mx, 0x124, 0xf, 0xf, true));
      mx = max(mx, (u32)__builtin_amdgcn_update_dpp(0, (int)mx, 0x128, 0xf, 0xf, true));
      mx = max(max((u32)__builtin_amdgcn_readlane((int)mx, 0), (u32)__builtin_amdgcn_readlane((int)mx, 16)),
               max((u32)__builtin_amdgcn_readlane((int)mx, 32), (u32)__builtin_amdgcn_readlane((int)mx, 48)));
      mx = min(max(mx, 60u << 23), 200u << 23);
      const float up = __uint_as_float((267u << 23) - mx);
      float kscale = __uint_as_float(mx - (27u << 23));
      const f32x2 e1 = cexp(1.f), e8 = cexp(8.f), e128 = cexp(128.f), e256 = cexp(256.f), e1024 = cexp(1024.f);
      const CM c8 = mkcm((f32x2){e8[0], -e8[1]}), c128 = mkcm((f32x2){e128[0], -e128[1]}), m1 = mkcm(e1), m256 = mkcm(e256);
      u32 hv;
      auto putr = [&](int r, u32 v) { *(u32*)((r < 16 ? tar + r * TROW : tar2 + (r - 16) * TROW) + lane * 4) = v; };
      auto putc = [&](int c, u32 v) { *(u32*)(tab + c * TROW + lane * 4) = v; };
      f32x2 p[8];
      p[0] = (f32x2){16384.f, 0.f}; putr(0, pk_f16(p[0]));
      p[1] = e1 * 16384.f; putr(1, pk_f16(p[1]));
#pragma unroll
      for (int i = 2; i < 8; ++i) { p[i] = cmul_h(p[i - 1], m1, hv); putr(i, hv); }
      f32x2 q0 = e256 * 16384.f; putr(8, pk_f16(q0));
      (void)cmul_h(p[2], m256, hv); putr(9, hv);
      f32x2 q4 = cmul_h(p[4], m256, hv); putr(10, hv);
      (void)cmul_h(p[6], m256, hv); putr(11, hv);
      q0 = cmul_h(q0, m256, hv); putr(12, hv);
      q4 = cmul_h(q4, m256, hv); putr(13, hv);
      (void)cmul_h(q0, m256, hv); putr(14, hv);
      (void)cmul_h(q4, m256, hv); putr(15, hv);
      q0 = e1024 * 16384.f; putr(16, pk_f16(q0));
#pragma unroll
      for (int i = 17; i < 21; ++i) { q0 = cmul_h(q0, m256, hv); putr(i, hv); }
      SB();
      if (initw) init_bump(rs);
      init_wait(rs, init_off, f0, f1);
      drain_bar<0, true>();
      signal_all(rs, 6, b, w);
      {
        const char* wsrc = smem + LD_XT + (8 * kq + (c16 >> 2)) * WROW + nbase_ * 32 + (c16 & 3) * 8;
        u32x4 f0 = tr_frag(wsrc, 0, 4 * WROW), f1 = tr_frag(wsrc, 32, 32 + 4 * WROW), f2 = tr_frag(wsrc, 64, 64 + 4 * WROW);
        st16(rs, wvo, so01 + rt * 1024, f0);
        st16(rs, wvo, so01 + (KS1 + rt) * 1024, f1);
        if (w == 3) f2 = splat4(0x3f803f80u);
        st16(rs, wvo, so2 + rt * 1024, f2);
      }
      SB();
    {
      const rsrc_t rwd = __builtin_amdgcn_make_buffer_rsrc((void*)wd, 0, F * D * 2, 0x00020000);
#pragma unroll
      for (int i = 0; i < 4; ++i)
        w2r[i] = __builtin_amdgcn_raw_buffer_load_b128(rwd, (lane >> 3) * (D * 2) + (lane & 7) * 16, ((chunk >> 3) * 32 * D + (chunk & 7) * 64 + i * 8 * D) * 2, 0);
    }
      SB();
      u32x4 fa[4][2];
      {
        const char* ra0 = tar + c16 * TROW + kq * 16;
        const char* ra1 = tar2 + (c16 < 5 ? c16 : 0) * TROW + kq * 16;
#pragma unroll
        for (int s = 0; s < 4; ++s) { fa[s][0] = *(const u32x4*)(ra0 + s * 64); fa[s][1] = *(const u32x4*)(ra1 + s * 64); }
      }
      SB();
      f32x2 v0 = (f32x2){kr, -ki} * up; putc(0, pk_f16(v0));
      f32x2 v1 = cmul_h(v0, c128, hv); putc(16, hv);
#pragma unroll
      for (int i = 1; i < 16; ++i) {
        v0 = cmul_h(v0, c8, hv); putc(i, hv);
        v1 = cmul_h(v1, c8, hv); putc(16 + i, hv);
      }
      f32x4 ka[2][2];
      const char* rb0 = tab + c16 * TROW + kq * 16;
      u32x4 fb[4][2];
#pragma unroll
      for (int s = 0; s < 4; ++s) { fb[s][0] = *(const u32x4*)(rb0 + s * 64); fb[s][1] = *(const u32x4*)(rb0 + 16 * TROW + s * 64); }
      SB();
      static_for<4>([&](auto sc) {
        constexpr int s = IC(sc);
        mfma_h<s == 0>(ka[0][0], fa[s][0], fb[s][0]); mfma_h<s == 0>(ka[0][1], fa[s][0], fb[s][1]);
        mfma_h<s == 0>(ka[1][0], fa[s][1], fb[s][0]); mfma_h<s == 0>(ka[1][1], fa[s][1], fb[s][1]);
      });
      pv = poll_ld(rs, all_off(6, b), true, w);
      mfma_done4(ka[0][0], ka[0][1], ka[1][0], ka[1][1]);
      if (__builtin_expect(grows, 0)) {
#pragma unroll
        for (int c = 0; c < 2; ++c)
#pragma unroll
          for (int i = 0; i < 4; ++i) {
            const int lo8 = 128 * c + 8 * c16;
            const int l0 = kq < 2 ? 4 * kq + i : kq == 2 ? 256 + 2 * i : 512 + (i >> 1) * 256 + (i & 1) * 4, l1 = 1024 + 256 * (4 * kq + i);
            ka[0][c][i] = ka[0][c][i] * kscale * __builtin_amdgcn_exp2f(grow * (float)(l0 + lo8));
            ka[1][c][i] = ka[1][c][i] * kscale * __builtin_amdgcn_exp2f(grow * (float)(l1 + lo8));
          }
        kscale = 1.f;
      }
#pragma unroll
      for (int r = 0; r < 2; ++r)
#pragma unroll
        for (int c = 0; c < 2; ++c) {
          const f32x4 v = ka[r][c] * kscale;
#pragma unroll
          for (int i = 0; i < 4; ++i) *(LAS float*)(size_t)(u32)kx[r][c][i] = v[i];
        }
    }
    drain_bar<5, true>();
    signal_grp(rs, (OFF_CW - OFF_CH) / 256 + cb_, w);
    {
      f32x4 tv[12];
#pragma unroll
      for (int i = 0; i < 12; ++i) tv[i] = *(const LAS f32x4*)(size_t)(u32)ix[i];
      SB();
      u32 e[2][6];
#pragma unroll
      for (int i = 0; i < 6; ++i) {
        const f32x2 a = tv[2 * i].xy + tv[2 * i].zw, c = tv[2 * i + 1].xy + tv[2 * i + 1].zw;
        e[0][i] = pk_bf16(a[0], c[0]);
        e[1][i] = pk_bf16(a[1], c[1]);
      }
#pragma unroll
      for (int c = 0; c < 2; ++c) {
        char* kb = smem + c * KCH + tid * 16 + 32;
        u32 o[5];
#pragma unroll
        for (int i = 0; i < 5; ++i) o[i] = __builtin_amdgcn_alignbit(e[c][i + 1], e[c][i], 16);
        *(u32x4*)(kb) = (u32x4){e[c][2], e[c][3], e[c][4], e[c][5]};
        *(u32x4*)(kb + KCOPY) = (u32x4){o[1], o[2], o[3], o[4]};
        *(u32x4*)(kb + 2 * KCOPY) = (u32x4){e[c][1], e[c][2], e[c][3], e[c][4]};
        *(u32x4*)(kb + 3 * KCOPY) = (u32x4){o[0], o[1], o[2], o[3]};
      }
    }
    SB();
    if (chunk < KS2P * 8) {
      char* img = smem + LD_W2 + 16384 + w * 4096;
#pragma unroll
      for (int i = 0; i < 4; ++i)
        *(u32x4*)(img + i * 1024 + (lane >> 3) * 128 + ((((lane & 7) >> 1) ^ (((lane >> 4) & 1) | ((i & 1) << 1))) * 32) + (lane & 1) * 16) = w2r[i];
      const int fr = ((c16 >> 3) & 1) | ((kq & 1) << 1);
#pragma unroll
      for (int i = 0; i < 4; ++i)
        st16(rs, wvo, OFF_WD + (((chunk & 7) * 4 + i) * KS2P + (chunk >> 3)) * 1024,
             tr_frag(img + (8 * kq + (c16 >> 2)) * 128 + ((i ^ fr) * 32) + (c16 & 3) * 8, 0, 512));
    }
    SB();
    if (initw) {
#pragma unroll 1
      for (int spins = 0; spins < SPIN_CAP && !__all(pv >= 64u); ++spins) { init_bump(rs); pv = poll_ld(rs, all_off(6, b), true, w); }
    }
    poll_wait(rs, all_off(6, b), true, pv, w);
    pv2 = poll_ld(rs, OFF_CW + cb_ * 256, false, w);
    prep_load<1>(rs, tid, OFF_XT + d0 * LP * 2, OFF_ST0, pe);
    prep_load<1>(rs, tid, OFF_XT + d0 * LP * 2 + 2048, OFF_ST0 + 8192, pl);
    f32x4 ze[2], zl[2];
    prep_ln(smem, 0, tid, dsk, pe, ze);
    asm volatile("" : "+v"(pl.pa[0]), "+v"(pl.pb[0]), "+v"(pl.xv[0]), "+v"(pl.xv[1]));
    SB();
    if (w == 3) poll_spin(rs, OFF_CW + cb_ * 256, false, pv2);
    bar();
    static_for<4>([&](auto ic) { ld_w1<IC(ic)>(rs, wvo, so01, so2); });
    f32x4 acc[4];
    conv_mfma<0, 0, 3, true>(smem, w, lane, acc, [&](auto nc) {
      constexpr int n = IC(nc);
      if constexpr (n % 3 == 0 && n < 36) ld_w1<4 + n / 3>(rs, wvo, so01, so2);
    });
    conv_out(rs, smem, OFF_X1 + d0 * LP * 2, w, lane, acc, ze);
    static_for<4>([&](auto ic) { ld_w1<16 + IC(ic)>(rs, wvo, so01, so2); });
    prep_ln(smem, 1, tid, dsk, pl, zl);
    SB();
    static_for<4>([&](auto ic) { ld_w1<20 + IC(ic)>(rs, wvo, so01, so2); });
    bar();
    conv_mfma<4, 0, 7, true>(smem, w, lane, acc, [&](auto nc) {
      constexpr int n = IC(nc);
      if constexpr (n % 4 == 0 && n < 96) ld_w1<24 + n / 4>(rs, wvo, so01, so2);
      if constexpr (n == SIG_C0) { drain_bar<9 + SIG_C0 / 4, false>(); signal_all(rs, 0, b, w); }
      if constexpr (n == 50 || n == 70)
        st16(rs, tid * 16, OFF_XT + ACT2 + b * 4 * LP + (n == 70) * LP * 2, splat4(~0u));
    });
    pv = poll_ld(rs, all_off(0, b), true, w);
    conv_out(rs, smem, OFF_X1 + d0 * LP * 2 + 2048, w, lane, acc, zl);
    pv2 = poll_ld(rs, all_off(0, b), true, w);
    SB();
    asm volatile("s_waitcnt vmcnt(3)" ::: "memory");
    poll_wait2(rs, all_off(0, b), true, pv, pv2, w);
    {
      const int gx = xs_voff(w, lane), gs = OFF_X1 + rt * 128;
      const u32x4 nones = splat4(0xbf80bf80u);
      f32x4 c[3];
      static_for<48>([&](auto ic) {
        mfma_aA<4 * IC(ic), (IC(ic) < 3)>(c[IC(ic) % 3], nones); SB();
        if constexpr (IC(ic) < 16 && !(IC(ic) & 1)) { xs_dma<IC(ic) / 2>(rs, gx, gs, ldsw); SB(); }
      });
      asm volatile("s_nop 7" : "+v"(c[0]), "+v"(c[1]), "+v"(c[2]));
#pragma unroll
      for (int i = 0; i < 3; ++i) *(f32x4*)(smem + LD_CS + w * 192 + i * 64 + kq * 16) = c[i];
    }
    drain_bar<5, true>();
    signal_all(rs, 1, b, w);
  }
  const int lane_g = fresh_lane();
  static_for<2>([&](auto lc) {
    constexpr int layer = IC(lc);
    AccRegs pr;
    bool rdy = false;
    u32x4 W2[5][4];
    u32x4 hf[11][2];
    {
      ROLES(lane_g)
      int sw2[4];
#pragma unroll
      for (int i = 0; i < 4; ++i) sw2[i] = OFF_WD + ((ct * 4 + ((w + i) & 3)) * KS2P + w * 11) * 1024;
      const int fr = ((c16 >> 3) & 1) | ((kq & 1) << 1);
      const char* xr[4];
#pragma unroll
      for (int j = 0; j < 4; ++j) xr[j] = smem + LD_XT + (8 * kq + (c16 >> 2)) * 128 + ((((w + j) & 3) ^ fr) * 32) + (c16 & 3) * 8;
      const int gx = xs_voff(w, lane), gs = x1_off + rt * 128;
      u32x4 xs[8];
      char* ssq = smem + ((c16 >> 2) == kq ? LD_SS + w * 128 : LD_Y) + c16 * 8 + 4;
      char* sss = smem + (w == 3 ? LD_SS : LD_Y) + c16 * 8;
      u32x2 xb[2][4][2];
#pragma unroll
      for (int j = 0; j < 4; ++j) { xb[0][j][0] = tr_half(xr[j]); xb[0][j][1] = tr_half(xr[j] + 512); }
      SB();
      static_for<2>([&](auto hc) {
        constexpr int h = IC(hc);
        f32x4 ga[3][4], qa;
        const int nxt_poll = h == 0 ? all_off(layer * 4 + 1, b) : grp_off(layer * 32 + (rt2 >> 1));
        u32 pv, pv2;
        {
          auto fill = [&](auto gc) {
            constexpr int g = IC(gc);
            if constexpr (g < 8) xs_dma<8 + g>(rs, gx, gs + h * 2048, ldsw);
            if constexpr (g == (h == 0 ? 7 : SIG_HE)) {
              drain_bar<8, false>();
              if constexpr (h == 1) signal_grp(rs, layer * 32 + rt, w); else if constexpr (layer == 1) signal_all(rs, layer * 4 + 1, b, w);
            }
            if constexpr (g == 27) drain_bar<(h == 0 && layer == 0) ? 16 : (h == 1 ? 7 : 0), true>();
            if constexpr (h == 0 && layer == 0) {
              if constexpr (g >= 10 && g < 26) ld_agpr_imm<192 + 16 * ((g - 10) & 3) + 4 * ((g - 10) >> 2), ((g - 10) & 3) * 1024>(rs, wvo, sw2[(g - 10) >> 2]);
              if constexpr (g >= 28 && g < 36) xs[g - 28] = ld16(rs, wvo + ((g - 28) >> 2) * 1024, sw2[(g - 28) & 3] + 4096);
              if constexpr (g >= 43 && g < 51) *(u32x4*)(smem + LD_W2 + w * 8192 + (g - 43) * 1024 + wvo) = xs[g - 43];
            }
            if constexpr (h == 1) {
              if constexpr (g >= 14 && g < 47 && (g > 27 || !(g & 1)) && (g < 34 || !(g & 1))) {
                constexpr int n = g < 28 ? (g - 14) / 2 : g < 34 ? g - 21 : 13 + (g - 34) / 2, i = n / 5, s = n % 5;
                W2[s][i] = ld16(rs, wvo + (s & 3) * 1024, sw2[i] + (6 + (s >> 2) * 4) * 1024);
              }
            }
            if constexpr (g == POLL_G - 12) pv = poll_ld(rs, nxt_poll, h == 0, w);
            if constexpr (g == POLL_G - 5) pv2 = poll_ld(rs, nxt_poll, h == 0, w);
            if constexpr (g == POLL_G) poll_wait2(rs, nxt_poll, h == 0, pv, pv2, w);
            if constexpr (g >= POLL_G && g < POLL_G + 8) {
              if constexpr (h == 0) xs_dma<g - POLL_G>(rs, gx, gs + 2048, ldsw);
              else hf[g - POLL_G][0] = ld16(rs, wvo + ((g - POLL_G) & 3) * 1024, hb_e + ((g - POLL_G) >> 2) * 4096);
            }
          };
          static_for<60>([&](auto gc) {
            constexpr int g = IC(gc), ks = g >> 2, j = g & 3;
            const u32x4 x = {xb[ks & 1][j][0][0], xb[ks & 1][j][0][1], xb[ks & 1][j][1][0], xb[ks & 1][j][1][1]};
            mfma_aA<12 * ks, ks == 0>(ga[0][j], x);
            xb[(ks + 1) & 1][j][0] = tr_half(xr[j] + (ks + 1) * 4096);
            SB();
            mfma_aA<12 * ks + 4, ks == 0>(ga[1][j], x);
            xb[(ks + 1) & 1][j][1] = tr_half(xr[j] + (ks + 1) * 4096 + 512);
            SB();
            mfma_aA<12 * ks + 8, ks == 0>(ga[2][j], x);
            SB();
            fill(gc);
            SB();
            if constexpr (j == 0) { mfma_v<ks == 0>(qa, x, x); SB(); }
          });
          u32x4 x[4];
#pragma unroll
          for (int j = 0; j < 4; ++j) x[j] = (u32x4){xb[1][j][0][0], xb[1][j][0][1], xb[1][j][1][0], xb[1][j][1][1]};
          mfma_vv(qa, x[0], x[0]); SB();
#pragma unroll
          for (int j = 0; j < 4; ++j) { mfma_aA<188, false>(ga[2][j], x[j]); SB(); }
#pragma unroll
          for (int j = 0; j < 4; ++j) { mfma_aA<180, false>(ga[0][j], x[j]); SB(); }
          asm volatile("" : "+v"(qa));
          {
            const int r = c16 & 3;
            const float q01 = (r & 1) ? qa[1] : qa[0], q23 = (r & 1) ? qa[3] : qa[2];
            SB();
            mfma_aA<184, false>(ga[1][0], x[0]); SB();
            const float q = (r & 2) ? q23 : q01;
            *(float*)ssq = q;
            SB();
            mfma_aA<184, false>(ga[1][1], x[1]); SB();
            tie4(ga[2][0], ga[2][1], ga[2][2], ga[2][3]);
            *(float*)(sss + 384) = ga[2][0][0];
            *(float*)(sss) = ga[2][1][0];
            SB();
            mfma_aA<184, false>(ga[1][2], x[2]); SB();
            *(float*)(sss + 128) = ga[2][2][0];
            *(float*)(sss + 256) = ga[2][3][0];
            SB();
            mfma_aA<184, false>(ga[1][3], x[3]); SB();
          }
        }
        f32x4 cs[3];
#pragma unroll
        for (int i = 0; i < 3; ++i) cs[i] = *(const f32x4*)(smem + LD_CS + w * 192 + i * 64 + kq * 16);
        bar();
        f32x2 st[4];
#pragma unroll
        for (int j = 0; j < 4; ++j) st[j] = *(const f32x2*)(smem + LD_SS + (((w + j) & 3) * 16 + c16) * 8);
        tie4(ga[0][0], ga[0][1], ga[0][2], ga[0][3]);
        tie4(ga[1][0], ga[1][1], ga[1][2], ga[1][3]);
        tie4(ga[2][0], ga[2][1], ga[2][2], ga[2][3]);
        SB();
        f32x2 mv[4];
        float nl[4];
#pragma unroll
        for (int j = 0; j < 4; ++j) { mv[j] = ln_mv(st[j]); nl[j] = __builtin_amdgcn_rsqf(mv[j][1]) * -1.4426950408889634f; }
        const int hb = (((w + kq) & 3) * NT1 * 16 + c16) * 16, hs = h_off + ((h * 64 + rt * 4) * NT1 + nt0) * 256;
#pragma unroll
        for (int i = 0; i < 3; ++i) {
          u32 hd[4];
#pragma unroll
          for (int j = 0; j < 4; ++j) {
            const f32x4 v = __builtin_elementwise_fma(cs[i], (f32x4){mv[j][0], mv[j][0], mv[j][0], mv[j][0]}, ga[i][j]);
            const f32x2 g = {v[0], v[1]}, u = {v[2], v[3]};
            const f32x2 a = g * nl[j];
            const f32x2 e = {__builtin_amdgcn_exp2f(a[0]), __builtin_amdgcn_exp2f(a[1])};
            const f32x2 d = __builtin_elementwise_fma(e, (f32x2){mv[j][1], mv[j][1]}, (f32x2){mv[j][1], mv[j][1]});
            const f32x2 r = {__builtin_amdgcn_rcpf(d[0]), __builtin_amdgcn_rcpf(d[1])};
            hd[j] = pk_bf16((g * u) * r);
            if constexpr (h == 1) {
              const int t = i * 4 + j;
              if (t < 3) hf[8 + t][0] = ld16(rs, wvo, hb_e + (t == 2 ? s10 : (8 + t) * 1024));
              else hf[t - 3][1] = ld16(rs, wvo + ((t - 3) & 3) * 1024, hb_e + NT1 * 256 + ((t - 3) >> 2) * 4096);
            }
          }
          auto a02 = __builtin_amdgcn_permlane32_swap(hd[0], hd[2], false, false);
          auto a13 = __builtin_amdgcn_permlane32_swap(hd[1], hd[3], false, false);
          auto o01 = __builtin_amdgcn_permlane16_swap(a02[0], a13[0], false, false);
          auto o23 = __builtin_amdgcn_permlane16_swap(a02[1], a13[1], false, false);
          u32x4 v = {o01[0], o01[1], o23[0], o23[1]};
          SB();
          st16(rs, (i < 2 || ok2) ? hb + i * 256 : OOB, hs, v);
          SB();
        }
        SB();
        if constexpr (h == 0) {
          drain_bar<3, false>();
#pragma unroll
          for (int j = 0; j < 4; ++j) { xb[0][j][0] = tr_half(xr[j]); xb[0][j][1] = tr_half(xr[j] + 512); }
        }
        SB();
      });
    }
    {
      ROLES(fresh_lane())
      char* rw[3];
#pragma unroll
      for (int i = 0; i < 3; ++i) rw[i] = smem + LD_XT + (((w + 1 + i) & 3) * 3 + i) * 2048 + wvo;
      const char* rb = smem + LD_XT + w * 6144 + wvo;
      const int rvo = c16 * (LP * 2) + kq * 8;
      const int ovo = c16 * (LP * 2) + (kq & 1) * 32 + (kq >> 1) * 16;
      char* ow = smem + LD_OT + (w * 16 + c16) * 64 + (kq & 1) * 32 + (kq >> 1) * 16;
      const char* ob = smem + LD_OT + (8 * kq + (c16 >> 2)) * 64 + (w & 1) * 32 + (c16 & 3) * 8;
      const char* wlp = smem + LD_W2 + w * 8192 + wvo;
      static_for<2>([&](auto hc) {
        constexpr int h = IC(hc);
        const int tok0 = h * 1024 + rt2 * 32;
        const int eso = ((ct * 64 + w * 16) * LP + tok0) * 2;
        const int hb = h ? hb_l : hb_e;
        f32x4 da[4][2];
        u32x4 wl[2][4];
        u32x2 res[2];
        u32 pv, pv2;
        static_for<88>([&](auto nc) {
          constexpr int n = IC(nc), j = n / 44, s = (n % 44) >> 2, i = (n + 1) & 3;
          if constexpr (s < 4) mfma_aB<192 + 16 * s + 4 * i, s == 0>(da[i][j], hf[s][j]);
          else if constexpr (s < 6) mfma_vv(da[i][j], hf[s][j], wl[s - 4][i]);
          else mfma_vv(da[i][j], hf[s][j], W2[s - 6][i]);
          SB();
          if constexpr (h == 0) {
            if constexpr (n == 3 || n == 7) hf[9 + (n >> 2)][1] = ld16(rs, wvo, hb + NT1 * 256 + (n == 7 ? s10 : 9 * 1024));
            if constexpr (n == 10) res[0] = ld8(rs, rvo, x1_off + eso);
            if constexpr (n == 13) res[1] = ld8(rs, rvo + 32, x1_off + eso);
            if constexpr (n == SIG_HL) { drain_bar<4, false>(); signal_grp(rs, layer * 32 + 16 + rt, w); }
            if constexpr (n == 86) pv = poll_ld(rs, grp_off(layer * 32 + 16 + (rt2 >> 1)), false, w);
          } else {
            if constexpr (n < 33 && n % 3 == 2) hf[n / 3][1] = ld16(rs, wvo + (n / 3 < 8 ? ((n / 3) & 3) * 1024 : 0), hb + NT1 * 256 + (n / 3 < 8 ? (n / 12) * 4096 : n / 3 == 10 ? s10 : (n / 3) * 1024));
            if constexpr (layer == 0 && n == SIG_XE) { drain_bar<(SIG_XE + 1) / 3, false>(); signal_all(rs, 2, b, w); }
            if constexpr (n == 35) res[0] = ld8(rs, rvo, x1_off + eso);
            if constexpr (n == 38) res[1] = ld8(rs, rvo + 32, x1_off + eso);
            if constexpr (layer == 0 && n >= ACC_E && n < ACC_E + 12 && !((n - ACC_E) & 1)) acc_ld1(rs, (n - ACC_E) >> 1, tid, OFF_XT + ACT2 + b * 2 * LP * 2, 0, pr);
          }
          if constexpr (n % 44 >= 6 && n % 44 < 14) wl[(n % 44 - 6) >> 2][(n % 44 - 5) & 3] = *(const u32x4*)(wlp + (((n % 44 - 6) & 4) + ((n % 44 - 5) & 3)) * 1024);
          if constexpr (n == 52 || n == 56 || n == 60) { asm volatile("" : "+v"(da[(n - 48) >> 2][0])); *(f32x4*)(rw[(n - 52) >> 2]) = da[(n - 48) >> 2][0]; }
          SB();
        });
        mfma_done4(da[0][1], da[1][1], da[2][1], da[3][1]);
        SB();
#pragma unroll
        for (int i = 0; i < 3; ++i) *(f32x4*)(rw[i] + 1024) = da[i + 1][1];
        if constexpr (h == 0) pv2 = poll_ld(rs, grp_off(layer * 32 + 16 + (rt2 >> 1)), false, w);
        bar();
        u32x2 xo[2];
#pragma unroll
        for (int j = 0; j < 2; ++j) {
          const f32x4 rf = {lo_f(res[j][0]), hi_f(res[j][0]), lo_f(res[j][1]), hi_f(res[j][1])};
          const f32x4 v = ((da[0][j] + rf) + *(const f32x4*)(rb + j * 1024)) + (*(const f32x4*)(rb + 2048 + j * 1024) + *(const f32x4*)(rb + 4096 + j * 1024));
          xo[j] = (u32x2){pk_bf16(v[0], v[1]), pk_bf16(v[2], v[3])};
        }
        auto s0 = __builtin_amdgcn_permlane16_swap(xo[0][0], xo[1][0], false, false);
        auto s1 = __builtin_amdgcn_permlane16_swap(xo[0][1], xo[1][1], false, false);
        const u32x4 xv = {s0[0], s1[0], s0[1], s1[1]};
        *(u32x4*)ow = xv;
        if constexpr (layer == 0 && h == 1) { SB(); rdy = acc_ready(pr); SB(); }
        if constexpr (layer == 0) st16(rs, ovo, OFF_XT + ACT2 + eso, xv);
        SB();
        if constexpr (h == 0) {
          poll_wait2(rs, grp_off(layer * 32 + 16 + (rt2 >> 1)), false, pv, pv2, w);
#pragma unroll
          for (int s = 0; s < 6; ++s) hf[s][0] = ld16(rs, wvo + (s & 3) * 1024, hb_l + (s >> 2) * 4096);
        }
        SB();
        bar();
        if constexpr (h == 0) {
#pragma unroll
          for (int s = 6; s < 11; ++s) hf[s][0] = ld16(rs, wvo + (s < 8 ? (s & 3) * 1024 : 0), hb_l + (s < 8 ? 4096 : s == 10 ? s10 : s * 1024));
          SB();
        }
        if constexpr (layer == 0) {
          if (w < 2) {
            u32x4 f0 = tr_frag(ob, 0, 256), f1 = tr_frag(ob, 2048, 2048 + 256);
            const u32x4 ones = splat4(0x3f803f80u);
            f32x4 sa, qa;
            mfma_v0(sa, ones, f0); mfma_v0(qa, f0, f0); mfma_vv(sa, ones, f1); mfma_vv(qa, f1, f1);
            asm volatile("s_nop 7" : "+v"(sa), "+v"(qa));
            const int r = c16 & 3;
            float q = r == 0 ? qa[0] : r == 1 ? qa[1] : r == 2 ? qa[2] : qa[3];
            u32x2 v = {__float_as_uint(sa[0]), __float_as_uint(q)};
            const bool diag = (c16 >> 2) == kq;
            const bool fits = q < 0x1p29f;
            const u32x2 fs = to_fix(fits ? sa[0] * 0x1p30f : 0.f), fq = to_fix(fits ? q * 0x1p26f : 0x1p59f);
            const int avo = diag ? (c16 & 3) * 4096 + kq * 16 : OOB, aso = OFF_AC + h * 16384 + (rt2 * 8 + w * 4) * 16;
            asm volatile("s_nop 4\n\tbuffer_atomic_add_x2 %0, %2, %3, %4 offen sc1\n\tbuffer_atomic_add_x2 %1, %2, %3, %4 offen offset:8 sc1"
                         :: "v"(fs), "v"(fq), "v"(avo), "s"(rs), "s"(aso) : "memory");
            __builtin_amdgcn_raw_buffer_store_b64(v, rs, diag ? c16 * 8 : OOB, OFF_ST1 + (ct * L + tok0 + w * 16) * 8, SC1);
          }
        } else {
          u32x4 f = tr_frag(ob, (w >> 1) * 2048, (w >> 1) * 2048 + 256);
          const rsrc_t ro = __builtin_amdgcn_make_buffer_rsrc(out, 0, L * D * 2, 0x00020000);
          __builtin_amdgcn_raw_buffer_store_b128(f, ro, (c16 * D + kq * 8) * 2, ((tok0 + (w & 1) * 16) * D + ct * 64 + (w >> 1) * 32) * 2, SC1);
        }
        SB();
      });
    }
    if constexpr (layer == 0) {
      ROLES(fresh_lane())
      const int xo = b * 2 * LP * 2;
      f32x4 z[2], acc[4];
      u32 pv, pv2;
      acc_wait(rs, tid, OFF_XT + ACT2 + xo, 0, all_off(2, b), pr, rdy);
      acc_ln(rs, smem, 0, tid, dsk, pr, z);
      bar();
      conv_mfma<0, 0, 3, true>(smem, w, lane, acc, [&](auto nc) { if constexpr (IC(nc) == 30) { drain_bar<0, false>(); signal_all(rs, 3, b, w); } });
      conv_out(rs, smem, OFF_X1 + ACT2 + xo, w, lane, acc, z);
      conv_mfma<4, 0, 3, true>(smem, w, lane, acc, [&](auto nc) {
        constexpr int n = IC(nc);
        if constexpr (n >= ACC_L && n < ACC_L + 12 && !((n - ACC_L) & 1)) acc_ld1(rs, (n - ACC_L) >> 1, tid, OFF_XT + ACT2 + xo + 2048, 1, pr);
        if constexpr (n == 36) { drain_bar<6, false>(); signal_all(rs, 4, b, w); }
      });
      acc_wait(rs, tid, OFF_XT + ACT2 + xo + 2048, 1, all_off(3, b), pr, acc_ready(pr));
      acc_ln(rs, smem, 1, tid, dsk, pr, z);
      bar();
      const int gx = xs_voff(w, lane), gs = OFF_X1 + ACT2 + rt * 128;
      conv_mfma<4, 4, 7, false>(smem, w, lane, acc, [&](auto nc) {
        constexpr int n = IC(nc);
        if constexpr (n == CE_P) pv = poll_ld(rs, all_off(4, b), true, w);
        if constexpr (n == CE_P + 14) pv2 = poll_ld(rs, all_off(4, b), true, w);
      });
      poll_wait2(rs, all_off(4, b), true, pv, pv2, w);
      static_for<8>([&](auto ic) { xs_dma<IC(ic)>(rs, gx, gs, ldsw); });
      conv_out(rs, smem, OFF_X1 + ACT2 + xo + 2048, w, lane, acc, z);
      drain_bar<6, false>();
    }
  });
  asm volatile(".Lmk_code_end:" ::: "memory");
}

__device__ __attribute__((used, noinline)) void text_end_marker() { asm volatile(".Lmk_text_end:"); }

extern "C" void kernel_launch(void* const* d_in, const int* in_sizes, int n_in, void* d_out, int out_size,
                              void* d_ws, size_t ws_size, hipStream_t stream) {
  char* ws = (char*)d_ws;
  (void)hipFuncSetAttribute((const void*)main_kernel, hipFuncAttributeMaxDynamicSharedMemorySize, LDS_BYTES);
  main_kernel<<<256, 256, LDS_BYTES, stream>>>(
      (const u16*)d_in[0], (const u16*)d_in[1], (const u16*)d_in[2], (const u16*)d_in[3], (const u16*)d_in[4],
      (const u16*)d_in[5], (const u16*)d_in[6], (const u16*)d_in[7], (const u16*)d_in[8], (const u16*)d_in[10],
      (const u16*)d_in[11], (const u16*)d_in[12], ws, (u16*)d_out);
}
```
